```python
import torch

def _softmax(x, axis=-1):
    return torch.softmax(x, dim=axis)
def _gelu(x):
    return torch.nn.functional.gelu(x, approximate="tanh")
def _silu(x):
    return torch.nn.functional.silu(x)
def _sigmoid(x):
    return torch.sigmoid(x)
def _layernorm(x, axis=-1, eps=1e-5):
    m = x.mean(dim=axis, keepdim=True)
    v = x.var(dim=axis, keepdim=True, unbiased=False)
    return (x - m) / torch.sqrt(v + eps)
def _scatter_add(tgt, idx, src, axis=0):
    return tgt.clone().index_add_(axis, idx, src)
def _segment_sum(src, seg, n):
    out = torch.zeros((n,) + src.shape[1:], dtype=src.dtype)
    return out.index_add_(0, seg, src)
def _pad(x, pw):
    flat = tuple(v for pair in reversed(pw) for v in pair)
    return torch.nn.functional.pad(x, flat)

kernel_name = 'standalone__mha_softmax__near_ridge'

def setup_inputs():

    _w = (lambda a: (lambda t: t.to(torch.bfloat16) if t.is_floating_point() else t)(torch.as_tensor(a)))
    B, H, Sq, Sk, D = 16, 16, 848, 512, 256
    return {
        "q": _w((lambda *s: torch.randn(*s, dtype=torch.bfloat16) * 0.8)(B, H, Sq, D)),
        "k": _w((lambda *s: torch.randn(*s, dtype=torch.bfloat16) * 0.8)(B, H, Sk, D)),
        "v": _w((lambda *s: torch.randn(*s, dtype=torch.bfloat16) * 0.05)(B, H, Sk, D)),
    }

def reference(q, k, v):
    scale = q.shape[-1] ** -0.5
    scores = torch.einsum("bhqd,bhkd->bhqk", q, k) * scale
    p = _softmax(scores, axis=-1)
    return torch.einsum("bhqk,bhkd->bhqd", p, v)
```

```cpp
#include <hip/hip_runtime.h>
#include <hip/hip_bf16.h>

typedef float  f32x4 __attribute__((ext_vector_type(4)));
typedef __bf16 bf16x8 __attribute__((ext_vector_type(8)));
typedef __bf16 bf16x2 __attribute__((ext_vector_type(2)));
typedef float  f32x2 __attribute__((ext_vector_type(2)));
typedef unsigned u32x4 __attribute__((ext_vector_type(4)));
typedef unsigned u32x2 __attribute__((ext_vector_type(2)));

constexpr int SQ = 848, SK = 512, D = 256, NH = 256;

__device__ __forceinline__ unsigned cvtpk(float lo, float hi) {
  f32x2 v = {lo, hi}; bf16x2 b = __builtin_convertvector(v, bf16x2); return __builtin_bit_cast(unsigned, b);
}

__device__ __forceinline__ void slow_rows16(const unsigned short* __restrict__ Q, const unsigned short* __restrict__ K,
                                            const unsigned short* __restrict__ V, unsigned short* __restrict__ O,
                                            int h, int row0) {
  const int lane = threadIdx.x & 63, c = lane & 15, g = lane >> 4;
  const float C = 0.0625f * 1.4426950408889634f;
  const unsigned short* q = Q + ((size_t)h * SQ + row0 + c) * D + 8 * g;
  const unsigned short* k = K + ((size_t)h * SK + c) * D + 8 * g;
  const unsigned short* v = V + ((size_t)h * SK + 4 * g) * D + c;
  bf16x8 qf[8];
#pragma unroll
  for (int s = 0; s < 8; ++s) qf[s] = *(const bf16x8*)(q + 32 * s);
  f32x4 o[16];
#pragma unroll
  for (int t = 0; t < 16; ++t) o[t] = (f32x4){0.f, 0.f, 0.f, 0.f};
  float m = -INFINITY, l = 0.f;
  for (int kt = 0; kt < SK / 32; ++kt) {
    f32x4 s0 = {0.f, 0.f, 0.f, 0.f}, s1 = {0.f, 0.f, 0.f, 0.f};
#pragma unroll
    for (int s = 0; s < 8; ++s) {
      bf16x8 k0 = *(const bf16x8*)(k + (size_t)(32 * kt) * D + 32 * s);
      bf16x8 k1 = *(const bf16x8*)(k + (size_t)(32 * kt + 16) * D + 32 * s);
      s0 = __builtin_amdgcn_mfma_f32_16x16x32_bf16(k0, qf[s], s0, 0, 0, 0);
      s1 = __builtin_amdgcn_mfma_f32_16x16x32_bf16(k1, qf[s], s1, 0, 0, 0);
    }
    float mx = fmaxf(fmaxf(fmaxf(s0[0], s0[1]), fmaxf(s0[2], s0[3])), fmaxf(fmaxf(s1[0], s1[1]), fmaxf(s1[2], s1[3])));
    mx = fmaxf(mx, __shfl_xor(mx, 16));
    mx = fmaxf(mx, __shfl_xor(mx, 32));
    const float mn = fmaxf(m, mx);
    const float alpha = __builtin_amdgcn_exp2f((m - mn) * C);
    m = mn;
    const float nm = -mn * C;
    float p[8];
#pragma unroll
    for (int r = 0; r < 4; ++r) { p[r] = __builtin_amdgcn_exp2f(fmaf(s0[r], C, nm)); p[4 + r] = __builtin_amdgcn_exp2f(fmaf(s1[r], C, nm)); }
    l = l * alpha + (((p[0] + p[1]) + (p[2] + p[3])) + ((p[4] + p[5]) + (p[6] + p[7])));
    u32x4 pw = {cvtpk(p[0], p[1]), cvtpk(p[2], p[3]), cvtpk(p[4], p[5]), cvtpk(p[6], p[7])};
    const bf16x8 pf = __builtin_bit_cast(bf16x8, pw);
#pragma unroll
    for (int t = 0; t < 16; ++t) {
      const unsigned short* vp = v + (size_t)(32 * kt) * D + 16 * t;
      u32x4 w;
      w[0] = vp[0] | ((unsigned)vp[D] << 16);
      w[1] = vp[2 * D] | ((unsigned)vp[3 * D] << 16);
      w[2] = vp[16 * D] | ((unsigned)vp[17 * D] << 16);
      w[3] = vp[18 * D] | ((unsigned)vp[19 * D] << 16);
      f32x4 oo = o[t] * alpha;
      o[t] = __builtin_amdgcn_mfma_f32_16x16x32_bf16(__builtin_bit_cast(bf16x8, w), pf, oo, 0, 0, 0);
    }
  }
  l += __shfl_xor(l, 16);
  l += __shfl_xor(l, 32);
  const float il = 1.f / l;
  unsigned short* op = O + ((size_t)h * SQ + row0 + c) * D + 4 * g;
#pragma unroll
  for (int t = 0; t < 16; ++t) {
    u32x2 w = {cvtpk(o[t][0] * il, o[t][1] * il), cvtpk(o[t][2] * il, o[t][3] * il)};
    *(u32x2*)(op + 16 * t) = w;
  }
}

__global__ __launch_bounds__(256) void attn_slow(const unsigned short* __restrict__ Q, const unsigned short* __restrict__ K,
                                                 const unsigned short* __restrict__ V, unsigned short* __restrict__ O) {
  const int w = blockIdx.x * 4 + (threadIdx.x >> 6);
  if (w >= NH * (SQ / 16)) return;
  slow_rows16(Q, K, V, O, w / (SQ / 16), (w % (SQ / 16)) * 16);
}

extern "C" void kernel_launch(void* const* d_in, const int* in_sizes, int n_in, void* d_out, int out_size, void* d_ws, size_t ws_size,
                              hipStream_t stream) {
  attn_slow<<<NH * (SQ / 16) / 4, 256, 0, stream>>>((const unsigned short*)d_in[0], (const unsigned short*)d_in[1],
                                                    (const unsigned short*)d_in[2], (unsigned short*)d_out);
}
```

```cpp
#include <hip/hip_runtime.h>
#include <hip/hip_bf16.h>

typedef float    f32x4  __attribute__((ext_vector_type(4)));
typedef float    f32x2  __attribute__((ext_vector_type(2)));
typedef __bf16   bf16x8 __attribute__((ext_vector_type(8)));
typedef __bf16   bf16x2 __attribute__((ext_vector_type(2)));
typedef unsigned u32x4  __attribute__((ext_vector_type(4)));
typedef unsigned u32x2  __attribute__((ext_vector_type(2)));

constexpr int SQ = 848, SK = 512, D = 256;

__device__ __forceinline__ unsigned cvtpk(float lo, float hi) {
  f32x2 v = {lo, hi}; bf16x2 b = __builtin_convertvector(v, bf16x2); return __builtin_bit_cast(unsigned, b);
}
__device__ __forceinline__ float xlane(float x, int lane, int mask) {
  return __builtin_bit_cast(float, __builtin_amdgcn_ds_bpermute((lane ^ mask) << 2, __builtin_bit_cast(int, x)));
}

__device__ __forceinline__ void slow_rows16(const unsigned short* __restrict__ Q, const unsigned short* __restrict__ K,
                                            const unsigned short* __restrict__ V, unsigned short* __restrict__ O,
                                            int h, int row0, int lane) {
  const int c = lane & 15, g = lane >> 4;
  const float C = 0.0625f * 1.4426950408889634f;
  const unsigned short* q = Q + ((size_t)h * SQ + row0 + c) * D + 8 * g;
  const unsigned short* k = K + ((size_t)h * SK + c) * D + 8 * g;
  const unsigned short* v = V + ((size_t)h * SK + 4 * g) * D + c;
  bf16x8 qf[8];
#pragma unroll
  for (int s = 0; s < 8; ++s) qf[s] = *(const bf16x8*)(q + 32 * s);
  f32x4 o[16];
#pragma unroll
  for (int t = 0; t < 16; ++t) o[t] = (f32x4){0.f, 0.f, 0.f, 0.f};
  float m = -INFINITY, l = 0.f;
  for (int kt = 0; kt < SK / 32; ++kt) {
    f32x4 s0 = {0.f, 0.f, 0.f, 0.f}, s1 = {0.f, 0.f, 0.f, 0.f};
#pragma unroll
    for (int s = 0; s < 8; ++s) {
      bf16x8 k0 = *(const bf16x8*)(k + (size_t)(32 * kt) * D + 32 * s);
      bf16x8 k1 = *(const bf16x8*)(k + (size_t)(32 * kt + 16) * D + 32 * s);
      s0 = __builtin_amdgcn_mfma_f32_16x16x32_bf16(k0, qf[s], s0, 0, 0, 0);
      s1 = __builtin_amdgcn_mfma_f32_16x16x32_bf16(k1, qf[s], s1, 0, 0, 0);
    }
    float mx = fmaxf(fmaxf(fmaxf(s0[0], s0[1]), fmaxf(s0[2], s0[3])), fmaxf(fmaxf(s1[0], s1[1]), fmaxf(s1[2], s1[3])));
    mx = fmaxf(mx, xlane(mx, lane, 16));
    mx = fmaxf(mx, xlane(mx, lane, 32));
    const float mn = fmaxf(m, mx);
    const float alpha = (m == mn) ? 1.f : __builtin_amdgcn_exp2f((m - mn) * C);
    m = mn;
    float p[8];
#pragma unroll
    for (int r = 0; r < 4; ++r) { p[r] = __builtin_amdgcn_exp2f((s0[r] - mn) * C); p[4 + r] = __builtin_amdgcn_exp2f((s1[r] - mn) * C); }
    l = l * alpha + (((p[0] + p[1]) + (p[2] + p[3])) + ((p[4] + p[5]) + (p[6] + p[7])));
    u32x4 pw = {cvtpk(p[0], p[1]), cvtpk(p[2], p[3]), cvtpk(p[4], p[5]), cvtpk(p[6], p[7])};
    const bf16x8 pf = __builtin_bit_cast(bf16x8, pw);
#pragma unroll
    for (int t = 0; t < 16; ++t) {
      const unsigned short* vp = v + (size_t)(32 * kt) * D + 16 * t;
      u32x4 w;
      w[0] = vp[0] | ((unsigned)vp[D] << 16);
      w[1] = vp[2 * D] | ((unsigned)vp[3 * D] << 16);
      w[2] = vp[16 * D] | ((unsigned)vp[17 * D] << 16);
      w[3] = vp[18 * D] | ((unsigned)vp[19 * D] << 16);
      f32x4 oo = o[t] * alpha;
      o[t] = __builtin_amdgcn_mfma_f32_16x16x32_bf16(__builtin_bit_cast(bf16x8, w), pf, oo, 0, 0, 0);
    }
  }
  l += xlane(l, lane, 16);
  l += xlane(l, lane, 32);
  const float il = 1.f / l;
  unsigned short* op = O + ((size_t)h * SQ + row0 + c) * D + 4 * g;
#pragma unroll
  for (int t = 0; t < 16; ++t) {
    u32x2 w = {cvtpk(o[t][0] * il, o[t][1] * il), cvtpk(o[t][2] * il, o[t][3] * il)};
    *(u32x2*)(op + 16 * t) = w;
  }
}

__global__ __launch_bounds__(256, 1) void attn_fwd(const unsigned short* __restrict__ Q, const unsigned short* __restrict__ K,
                                                   const unsigned short* __restrict__ V, unsigned short* __restrict__ O) {
  __shared__ __attribute__((aligned(16))) char lds[163840];
  const unsigned wid = __builtin_amdgcn_readfirstlane(threadIdx.x >> 6);
  const unsigned bid = blockIdx.x;
  unsigned flags;
  asm volatile(
    ".p2align 5\n"
    "s_mov_b32 s96, 0x8000\n"
    "s_mov_b32 s97, 896\n"
    "s_mov_b32 s82, m0\n"
    "s_mov_b32 s69, %6\n"
    "s_and_b32 s71, %5, 7\n"
    "s_lshl_b32 s71, s71, 5\n"
    "s_lshr_b32 s72, %5, 5\n"
    "s_lshl_b32 s72, s72, 2\n"
    "s_add_u32 s71, s71, s72\n"
    "s_lshr_b32 s72, %5, 3\n"
    "s_and_b32 s72, s72, 3\n"
    "v_mbcnt_lo_u32_b32 v0, -1, 0\n"
    "s_nop 0\n"
    "v_mbcnt_hi_u32_b32 v0, -1, v0\n"
    "v_lshrrev_b32 v1, 3, v0\n"
    "v_lshlrev_b32 v1, 9, v1\n"
    "v_lshrrev_b32 v2, 4, v0\n"
    "v_lshlrev_b32 v3, 1, v2\n"
    "v_and_b32 v4, 7, v0\n"
    "v_xor_b32 v3, v3, v4\n"
    "v_lshl_add_u32 v92, v3, 4, v1\n"
    "s_lshl_b32 s73, s71, 18\n"
    "s_mov_b64 s[36:37], %2\n"
    "s_add_u32 s36, s36, s73\n"
    "s_addc_u32 s37, s37, 0\n"
    "s_and_b32 s37, s37, 0xffff\n"
    "s_mov_b32 s38, 1048576\n"
    "s_mov_b32 s39, 0x20000\n"
    "s_lshl_b32 s60, s69, 13\n"
    "s_add_u32 s61, s60, 0x10000\n"
    "s_mov_b32 s53, s60\n"
    "s_add_u32 s54, s53, 0x1000\n"
    "s_mov_b32 m0, s60\n"
    "s_nop 0\n"
    "buffer_load_dwordx4 v92, s[36:39], s53 offen offset:0 lds\n"
    "s_add_u32 m0, m0, 896\n"
    "s_nop 0\n"
    "buffer_load_dwordx4 v92, s[36:39], s53 offen offset:128 lds\n"
    "s_add_u32 m0, m0, 896\n"
    "s_nop 0\n"
    "buffer_load_dwordx4 v92, s[36:39], s53 offen offset:256 lds\n"
    "s_add_u32 m0, m0, 896\n"
    "s_nop 0\n"
    "buffer_load_dwordx4 v92, s[36:39], s53 offen offset:384 lds\n"
    "s_add_u32 m0, m0, 1408\n"
    "s_nop 0\n"
    "buffer_load_dwordx4 v92, s[36:39], s54 offen offset:0 lds\n"
    "s_add_u32 m0, m0, s97\n"
    "s_nop 0\n"
    "buffer_load_dwordx4 v92, s[36:39], s54 offen offset:128 lds\n"
    "s_add_u32 m0, m0, 896\n"
    "s_nop 0\n"
    "buffer_load_dwordx4 v92, s[36:39], s54 offen offset:256 lds\n"
    "s_add_u32 m0, m0, 896\n"
    "s_nop 0\n"
    "buffer_load_dwordx4 v92, s[36:39], s54 offen offset:384 lds\n"
    "s_mov_b64 s[40:41], %3\n"
    "s_add_u32 s40, s40, s73\n"
    "s_addc_u32 s41, s41, 0\n"
    "s_sub_u32 s40, s40, 16384\n"
    "s_subb_u32 s41, s41, 0\n"
    "s_and_b32 s41, s41, 0xffff\n"
    "s_mov_b32 s42, 1064960\n"
    "s_mov_b32 s43, 0x20000\n"
    "s_mul_i32 s73, s71, 434176\n"
    "s_mov_b64 s[44:45], %1\n"
    "s_add_u32 s44, s44, s73\n"
    "s_addc_u32 s45, s45, 0\n"
    "s_and_b32 s45, s45, 0xffff\n"
    "s_mov_b32 s46, 434176\n"
    "s_mov_b32 s47, 0x20000\n"
    "s_mov_b64 s[48:49], %4\n"
    "s_add_u32 s48, s48, s73\n"
    "s_addc_u32 s49, s49, 0\n"
    "s_sub_u32 s48, s48, 434176\n"
    "s_subb_u32 s49, s49, 0\n"
    "s_and_b32 s49, s49, 0xffff\n"
    "s_mov_b32 s50, 0\n"
    "s_mov_b32 s51, 0x20000\n"
    "s_and_b32 s73, s72, 1\n"
    "s_lshl_b32 s78, s73, 17\n"
    "s_lshl_b32 s74, s69, 15\n"
    "s_add_u32 s78, s78, s74\n"
    "s_mul_i32 s79, s73, 98304\n"
    "s_mul_i32 s74, s69, 24576\n"
    "s_add_u32 s79, s79, s74\n"
    "s_add_u32 s79, s79, 262144\n"
    "s_cmp_eq_u32 s69, 3\n"
    "s_cselect_b32 s80, s73, 0\n"
    "s_lshl_b32 s70, s69, 14\n"
    "s_add_u32 s70, s70, 98304\n"
    "s_lshr_b32 s73, s72, 1\n"
    "s_cmp_eq_u32 s73, 0\n"
    "s_cselect_b32 s74, s78, s79\n"
    "s_add_u32 s57, s74, 0\n"
    "s_add_u32 s58, s74, 4096\n"
    "s_add_u32 m0, s70, 0\n"
    "s_nop 0\n"
    "buffer_load_dwordx4 v92, s[44:47], s57 offen offset:0 nt lds\n"
    "s_add_u32 m0, m0, s97\n"
    "s_nop 0\n"
    "buffer_load_dwordx4 v92, s[44:47], s57 offen offset:128 nt lds\n"
    "s_add_u32 m0, m0, s97\n"
    "s_nop 0\n"
    "buffer_load_dwordx4 v92, s[44:47], s57 offen offset:256 nt lds\n"
    "s_add_u32 m0, m0, s97\n"
    "s_nop 0\n"
    "buffer_load_dwordx4 v92, s[44:47], s57 offen offset:384 nt lds\n"
    "s_add_u32 m0, m0, 1408\n"
    "s_nop 0\n"
    "buffer_load_dwordx4 v92, s[44:47], s58 offen offset:0 nt lds\n"
    "s_add_u32 m0, m0, 896\n"
    "s_nop 0\n"
    "buffer_load_dwordx4 v92, s[44:47], s58 offen offset:128 nt lds\n"
    "s_add_u32 m0, m0, s97\n"
    "s_nop 0\n"
    "buffer_load_dwordx4 v92, s[44:47], s58 offen offset:256 nt lds\n"
    "s_add_u32 m0, m0, s97\n"
    "s_nop 0\n"
    "buffer_load_dwordx4 v92, s[44:47], s58 offen offset:384 nt lds\n"
    "s_add_u32 s57, s74, 8192\n"
    "s_add_u32 s58, s74, 12288\n"
    "s_add_u32 m0, s70, 8192\n"
    "s_nop 0\n"
    "buffer_load_dwordx4 v92, s[44:47], s57 offen offset:0 nt lds\n"
    "s_add_u32 m0, m0, s97\n"
    "s_nop 0\n"
    "buffer_load_dwordx4 v92, s[44:47], s57 offen offset:128 nt lds\n"
    "s_add_u32 m0, m0, s97\n"
    "s_nop 0\n"
    "buffer_load_dwordx4 v92, s[44:47], s57 offen offset:256 nt lds\n"
    "s_add_u32 m0, m0, 896\n"
    "s_nop 0\n"
    "buffer_load_dwordx4 v92, s[44:47], s57 offen offset:384 nt lds\n"
    "s_add_u32 m0, m0, 1408\n"
    "s_nop 0\n"
    "buffer_load_dwordx4 v92, s[44:47], s58 offen offset:0 nt lds\n"
    "s_add_u32 m0, m0, 896\n"
    "s_nop 0\n"
    "buffer_load_dwordx4 v92, s[44:47], s58 offen offset:128 nt lds\n"
    "s_add_u32 m0, m0, s97\n"
    "s_nop 0\n"
    "buffer_load_dwordx4 v92, s[44:47], s58 offen offset:256 nt lds\n"
    "s_add_u32 m0, m0, s97\n"
    "s_nop 0\n"
    "buffer_load_dwordx4 v92, s[44:47], s58 offen offset:384 nt lds\n"
    "s_add_u32 s57, s74, 16384\n"
    "s_add_u32 s58, s74, 20480\n"
    "s_add_u32 m0, s60, 32768\n"
    "s_nop 0\n"
    "buffer_load_dwordx4 v92, s[44:47], s57 offen offset:0 nt lds\n"
    "s_add_u32 m0, m0, s97\n"
    "s_nop 0\n"
    "buffer_load_dwordx4 v92, s[44:47], s57 offen offset:128 nt lds\n"
    "s_add_u32 m0, m0, s97\n"
    "s_nop 0\n"
    "buffer_load_dwordx4 v92, s[44:47], s57 offen offset:256 nt lds\n"
    "s_add_u32 m0, m0, 896\n"
    "s_nop 0\n"
    "buffer_load_dwordx4 v92, s[44:47], s57 offen offset:384 nt lds\n"
    "s_add_u32 m0, m0, 1408\n"
    "s_nop 0\n"
    "buffer_load_dwordx4 v92, s[44:47], s58 offen offset:0 nt lds\n"
    "s_add_u32 m0, m0, 896\n"
    "s_nop 0\n"
    "buffer_load_dwordx4 v92, s[44:47], s58 offen offset:128 nt lds\n"
    "s_add_u32 m0, m0, s97\n"
    "s_nop 0\n"
    "buffer_load_dwordx4 v92, s[44:47], s58 offen offset:256 nt lds\n"
    "s_add_u32 m0, m0, s97\n"
    "s_nop 0\n"
    "buffer_load_dwordx4 v92, s[44:47], s58 offen offset:384 nt lds\n"
    "s_cmp_eq_u32 s73, 0\n"
    "s_cbranch_scc0 LP3_%=\n"
    "s_add_u32 s57, s74, 24576\n"
    "s_add_u32 s58, s74, 28672\n"
    "s_add_u32 m0, s60, 65536\n"
    "s_nop 0\n"
    "buffer_load_dwordx4 v92, s[44:47], s57 offen offset:0 nt lds\n"
    "s_add_u32 m0, m0, 896\n"
    "s_nop 0\n"
    "buffer_load_dwordx4 v92, s[44:47], s57 offen offset:128 nt lds\n"
    "s_add_u32 m0, m0, s97\n"
    "s_nop 0\n"
    "buffer_load_dwordx4 v92, s[44:47], s57 offen offset:256 nt lds\n"
    "s_add_u32 m0, m0, s97\n"
    "s_nop 0\n"
    "buffer_load_dwordx4 v92, s[44:47], s57 offen offset:384 nt lds\n"
    "s_add_u32 m0, m0, 1408\n"
    "s_nop 0\n"
    "buffer_load_dwordx4 v92, s[44:47], s58 offen offset:0 nt lds\n"
    "s_add_u32 m0, m0, s97\n"
    "s_nop 0\n"
    "buffer_load_dwordx4 v92, s[44:47], s58 offen offset:128 nt lds\n"
    "s_add_u32 m0, m0, s97\n"
    "s_nop 0\n"
    "buffer_load_dwordx4 v92, s[44:47], s58 offen offset:256 nt lds\n"
    "s_add_u32 m0, m0, s97\n"
    "s_nop 0\n"
    "buffer_load_dwordx4 v92, s[44:47], s58 offen offset:384 nt lds\n"
    "LP3_%=:\n"
    "s_add_u32 s53, s53, s96\n"
    "s_add_u32 s54, s54, 0x8000\n"
    "s_or_b32 s55, s60, 0x4000\n"
    "s_add_u32 s56, s55, 0x1000\n"
    "s_mov_b32 s52, 0x3db8aa3b\n"
    "s_mov_b32 s81, 0xbdb8aa3b\n"
    "s_mov_b32 s75, 0x4f800000\n"
    "s_mov_b32 s66, 0\n"
    "s_mov_b32 s67, 0\n"
    "s_mov_b32 s68, 1\n"
    "s_mov_b32 s62, 0x8000\n"
    "s_add_u32 s63, s60, s96\n"
    "v_and_b32 v5, 7, v0\n"
    "v_lshlrev_b32 v6, 7, v5\n"
    "v_and_b32 v5, 6, v5\n"
    "v_and_b32 v7, 8, v0\n"
    "v_lshl_add_u32 v6, v7, 9, v6\n"
    "v_xor_b32 v7, v2, v5\n"
    "v_lshl_add_u32 v88, v7, 4, v6\n"
    "v_xor_b32 v7, 4, v7\n"
    "v_lshl_add_u32 v89, v7, 4, v6\n"
    "v_lshrrev_b32 v5, 2, v0\n"
    "v_and_b32 v5, 7, v5\n"
    "v_lshlrev_b32 v6, 7, v5\n"
    "v_and_b32 v5, 6, v5\n"
    "v_lshrrev_b32_e64 v7, 5, v0\n"
    "v_lshl_add_u32 v6, v7, 12, v6\n"
    "v_add_u32 v6, 0x10000, v6\n"
    "v_and_b32 v7, 3, v0\n"
    "v_xor_b32 v7, v7, v5\n"
    "v_lshl_add_u32 v90, v7, 4, v6\n"
    "v_xor_b32 v7, 4, v7\n"
    "v_lshl_add_u32 v91, v7, 4, v6\n"
    "v_add_u32 v94, s70, v88\n"
    "v_add_u32 v95, s70, v89\n"
    "v_lshlrev_b32 v93, 4, v0\n"
    "v_add_u32_e64 v93, s70, v93\n"
    "v_add_u32_e64 v10, s60, v88\n"
    "v_add_u32_e64 v11, s60, v89\n"
    "s_add_u32 s44, s44, 434176\n"
    "s_addc_u32 s45, s45, 0\n"
    "s_waitcnt vmcnt(0)\n"
    "ds_read_b128 a[0:3], v94 offset:0\n"
    "ds_read_b128 a[4:7], v95 offset:0\n"
    "ds_read_b128 a[8:11], v94 offset:1024\n"
    "ds_read_b128 a[12:15], v95 offset:1024\n"
    "ds_read_b128 a[16:19], v94 offset:2048\n"
    "ds_read_b128 a[20:23], v95 offset:2048\n"
    "ds_read_b128 a[24:27], v94 offset:3072\n"
    "ds_read_b128 a[28:31], v95 offset:3072\n"
    "ds_read_b128 a[32:35], v94 offset:8192\n"
    "ds_read_b128 a[36:39], v95 offset:8192\n"
    "ds_read_b128 a[40:43], v94 offset:9216\n"
    "ds_read_b128 a[44:47], v95 offset:9216\n"
    "ds_read_b128 a[48:51], v94 offset:10240\n"
    "ds_read_b128 a[52:55], v95 offset:10240\n"
    "ds_read_b128 a[56:59], v94 offset:11264\n"
    "ds_read_b128 a[60:63], v95 offset:11264\n"
    "ds_read_b128 a[64:67], v10 offset:32768\n"
    "ds_read_b128 a[68:71], v11 offset:32768\n"
    "ds_read_b128 a[72:75], v10 offset:33792\n"
    "ds_read_b128 a[76:79], v11 offset:33792\n"
    "ds_read_b128 a[80:83], v10 offset:34816\n"
    "ds_read_b128 a[84:87], v11 offset:34816\n"
    "ds_read_b128 a[88:91], v10 offset:35840\n"
    "ds_read_b128 a[92:95], v11 offset:35840\n"
    "v_add_u32 v10, 0x10000, v10\n"
    "v_add_u32 v11, 0x10000, v11\n"
    "ds_read_b128 a[96:99], v10 offset:0\n"
    "ds_read_b128 a[100:103], v11 offset:0\n"
    "ds_read_b128 a[104:107], v10 offset:1024\n"
    "ds_read_b128 a[108:111], v11 offset:1024\n"
    "ds_read_b128 a[112:115], v10 offset:2048\n"
    "ds_read_b128 a[116:119], v11 offset:2048\n"
    "ds_read_b128 a[120:123], v10 offset:3072\n"
    "ds_read_b128 a[124:127], v11 offset:3072\n"
    "s_waitcnt lgkmcnt(0)\n"
    "s_barrier\n"
    "ds_read_b128 a[128:131], v88 offset:0\n"
    "ds_read_b128 a[132:135], v89 offset:0\n"
    "ds_read_b128 a[136:139], v88 offset:1024\n"
    "ds_read_b128 a[140:143], v89 offset:1024\n"
    "ds_read_b128 a[144:147], v88 offset:2048\n"
    "ds_read_b128 a[148:151], v89 offset:2048\n"
    "ds_read_b128 a[152:155], v88 offset:3072\n"
    "ds_read_b128 a[156:159], v89 offset:3072\n"
    "s_cmp_eq_u32 s73, 0\n"
    "s_cbranch_scc0 LSP3f_%=\n"
    "LSP4f_%=:\n"
    "s_mov_b32 s57, s79\n"
    "s_mov_b32 s58, 0x100\n"
    "s_mov_b32 s59, s70\n"
    "s_mov_b32 s98, s79\n"
    "s_mov_b32 s99, 0x100\n"
    "s_mov_b32 s65, 5\n"
    "s_waitcnt lgkmcnt(6)\n"
    "v_mfma_f32_16x16x32_bf16 v[0:3], a[128:131], a[0:3], 0\n"
    "s_mov_b32 m0, s61\n"
    "v_add_u32 v93, 4096, v93\n"
    "v_mfma_f32_16x16x32_bf16 v[0:3], a[132:135], a[4:7], v[0:3]\n"
    "buffer_load_dwordx4 v92, s[40:43], s55 offen offset:0 lds\n"
    "v_mfma_f32_16x16x32_bf16 v[4:7], a[128:131], a[32:35], 0\n"
    "s_add_u32 m0, m0, 896\n"
    "v_mfma_f32_16x16x32_bf16 v[4:7], a[132:135], a[36:39], v[4:7]\n"
    "v_mfma_f32_16x16x32_bf16 v[8:11], a[128:131], a[64:67], 0\n"
    "v_mfma_f32_16x16x32_bf16 v[8:11], a[132:135], a[68:71], v[8:11]\n"
    "buffer_load_dwordx4 v92, s[40:43], s55 offen offset:128 lds\n"
    "v_mfma_f32_16x16x32_bf16 v[12:15], a[128:131], a[96:99], 0\n"
    "s_add_u32 m0, m0, s97\n"
    "ds_read_b128 a[128:131], v88 offset:8192\n"
    "v_mfma_f32_16x16x32_bf16 v[12:15], a[132:135], a[100:103], v[12:15]\n"
    "s_waitcnt lgkmcnt(5)\n"
    "ds_read_b128 a[132:135], v89 offset:8192\n"
    "v_mfma_f32_16x16x32_bf16 v[0:3], a[136:139], a[8:11], v[0:3]\n"
    "v_mfma_f32_16x16x32_bf16 v[0:3], a[140:143], a[12:15], v[0:3]\n"
    "buffer_load_dwordx4 v92, s[40:43], s55 offen offset:256 lds\n"
    "v_mfma_f32_16x16x32_bf16 v[4:7], a[136:139], a[40:43], v[4:7]\n"
    "s_add_u32 m0, m0, 896\n"
    "v_mfma_f32_16x16x32_bf16 v[4:7], a[140:143], a[44:47], v[4:7]\n"
    "v_mfma_f32_16x16x32_bf16 v[8:11], a[136:139], a[72:75], v[8:11]\n"
    "v_mfma_f32_16x16x32_bf16 v[8:11], a[140:143], a[76:79], v[8:11]\n"
    "buffer_load_dwordx4 v92, s[40:43], s55 offen offset:384 lds\n"
    "v_mfma_f32_16x16x32_bf16 v[12:15], a[136:139], a[104:107], v[12:15]\n"
    "s_add_u32 m0, m0, 1408\n"
    "ds_read_b128 a[136:139], v88 offset:9216\n"
    "v_mfma_f32_16x16x32_bf16 v[12:15], a[140:143], a[108:111], v[12:15]\n"
    "s_waitcnt lgkmcnt(5)\n"
    "ds_read_b128 a[140:143], v89 offset:9216\n"
    "v_mfma_f32_16x16x32_bf16 v[0:3], a[144:147], a[16:19], v[0:3]\n"
    "s_nop 0\n"
    "v_mfma_f32_16x16x32_bf16 v[0:3], a[148:151], a[20:23], v[0:3]\n"
    "buffer_load_dwordx4 v92, s[40:43], s56 offen offset:0 lds\n"
    "v_mfma_f32_16x16x32_bf16 v[4:7], a[144:147], a[48:51], v[4:7]\n"
    "s_add_u32 m0, m0, 896\n"
    "v_mfma_f32_16x16x32_bf16 v[4:7], a[148:151], a[52:55], v[4:7]\n"
    "v_mfma_f32_16x16x32_bf16 v[8:11], a[144:147], a[80:83], v[8:11]\n"
    "v_mfma_f32_16x16x32_bf16 v[8:11], a[148:151], a[84:87], v[8:11]\n"
    "buffer_load_dwordx4 v92, s[40:43], s56 offen offset:128 lds\n"
    "v_mfma_f32_16x16x32_bf16 v[12:15], a[144:147], a[112:115], v[12:15]\n"
    "s_add_u32 m0, m0, 896\n"
    "ds_read_b128 a[144:147], v88 offset:10240\n"
    "v_mfma_f32_16x16x32_bf16 v[12:15], a[148:151], a[116:119], v[12:15]\n"
    "s_waitcnt lgkmcnt(5)\n"
    "ds_read_b128 a[148:151], v89 offset:10240\n"
    "v_mfma_f32_16x16x32_bf16 v[0:3], a[152:155], a[24:27], v[0:3]\n"
    "v_mfma_f32_16x16x32_bf16 v[0:3], a[156:159], a[28:31], v[0:3]\n"
    "s_nop 0\n"
    "buffer_load_dwordx4 v92, s[40:43], s56 offen offset:256 lds\n"
    "v_mfma_f32_16x16x32_bf16 v[4:7], a[152:155], a[56:59], v[4:7]\n"
    "s_add_u32 m0, m0, 896\n"
    "v_mfma_f32_16x16x32_bf16 v[4:7], a[156:159], a[60:63], v[4:7]\n"
    "v_mfma_f32_16x16x32_bf16 v[8:11], a[152:155], a[88:91], v[8:11]\n"
    "v_mfma_f32_16x16x32_bf16 v[8:11], a[156:159], a[92:95], v[8:11]\n"
    "buffer_load_dwordx4 v92, s[40:43], s56 offen offset:384 lds\n"
    "v_mfma_f32_16x16x32_bf16 v[12:15], a[152:155], a[120:123], v[12:15]\n"
    "s_add_u32 s55, s60, s96\n"
    "ds_read_b128 a[152:155], v88 offset:11264\n"
    "v_max3_f32 v64, v0, v1, v2\n"
    "v_mfma_f32_16x16x32_bf16 v[12:15], a[156:159], a[124:127], v[12:15]\n"
    "s_waitcnt lgkmcnt(5)\n"
    "ds_read_b128 a[156:159], v89 offset:11264\n"
    "v_max_f32 v64, v64, v3\n"
    "v_mfma_f32_16x16x32_bf16 v[16:19], a[128:131], a[0:3], 0\n"
    "s_add_u32 s56, s60, 0x9000\n"
    "v_mov_b32 v65, v64\n"
    "v_max3_f32 v66, v4, v5, v6\n"
    "v_mfma_f32_16x16x32_bf16 v[16:19], a[132:135], a[4:7], v[16:19]\n"
    "s_add_u32 m0, s60, s96\n"
    "v_max_f32 v66, v66, v7\n"
    "v_max3_f32 v68, v8, v9, v10\n"
    "v_mfma_f32_16x16x32_bf16 v[20:23], a[128:131], a[32:35], 0\n"
    "buffer_load_dwordx4 v92, s[36:39], s53 offen offset:0 lds\n"
    "v_mfma_f32_16x16x32_bf16 v[20:23], a[132:135], a[36:39], v[20:23]\n"
    "s_add_u32 m0, m0, s97\n"
    "v_permlane32_swap_b32 v64, v65\n"
    "v_mfma_f32_16x16x32_bf16 v[24:27], a[128:131], a[64:67], 0\n"
    "v_mov_b32 v67, v66\n"
    "v_max_f32 v64, v64, v65\n"
    "v_mfma_f32_16x16x32_bf16 v[24:27], a[132:135], a[68:71], v[24:27]\n"
    "v_mov_b32 v65, v64\n"
    "v_max_f32 v68, v68, v11\n"
    "v_mfma_f32_16x16x32_bf16 v[28:31], a[128:131], a[96:99], 0\n"
    "ds_read_b128 a[128:131], v88 offset:16384\n"
    "v_mov_b32 v69, v68\n"
    "v_mfma_f32_16x16x32_bf16 v[28:31], a[132:135], a[100:103], v[28:31]\n"
    "s_waitcnt lgkmcnt(5)\n"
    "ds_read_b128 a[132:135], v89 offset:16384\n"
    "v_max3_f32 v70, v12, v13, v14\n"
    "v_mfma_f32_16x16x32_bf16 v[16:19], a[136:139], a[8:11], v[16:19]\n"
    "buffer_load_dwordx4 v92, s[36:39], s53 offen offset:128 lds\n"
    "v_mfma_f32_16x16x32_bf16 v[16:19], a[140:143], a[12:15], v[16:19]\n"
    "s_add_u32 m0, m0, 896\n"
    "v_permlane32_swap_b32 v66, v67\n"
    "v_mfma_f32_16x16x32_bf16 v[20:23], a[136:139], a[40:43], v[20:23]\n"
    "v_permlane16_swap_b32 v64, v65\n"
    "v_mfma_f32_16x16x32_bf16 v[20:23], a[140:143], a[44:47], v[20:23]\n"
    "v_permlane32_swap_b32 v68, v69\n"
    "v_mfma_f32_16x16x32_bf16 v[24:27], a[136:139], a[72:75], v[24:27]\n"
    "buffer_load_dwordx4 v92, s[36:39], s53 offen offset:256 lds\n"
    "v_max_f32 v66, v66, v67\n"
    "v_mov_b32 v67, v66\n"
    "v_max_f32 v70, v70, v15\n"
    "v_mfma_f32_16x16x32_bf16 v[24:27], a[140:143], a[76:79], v[24:27]\n"
    "s_add_u32 m0, m0, s97\n"
    "v_max_f32 v64, v64, v65\n"
    "v_mov_b32_e64 v71, v70\n"
    "v_mfma_f32_16x16x32_bf16 v[28:31], a[136:139], a[104:107], v[28:31]\n"
    "ds_read_b128 a[136:139], v88 offset:17408\n"
    "v_mul_f32 v84, s81, v64\n"
    "v_permlane16_swap_b32 v66, v67\n"
    "v_max_f32 v68, v68, v69\n"
    "v_mov_b32 v69, v68\n"
    "v_mfma_f32_16x16x32_bf16 v[28:31], a[140:143], a[108:111], v[28:31]\n"
    "s_waitcnt lgkmcnt(5)\n"
    "ds_read_b128 a[140:143], v89 offset:17408\n"
    "v_max_f32_e64 v66, v66, v67\n"
    "v_permlane32_swap_b32 v70, v71\n"
    "v_mfma_f32_16x16x32_bf16 v[16:19], a[144:147], a[16:19], v[16:19]\n"
    "buffer_load_dwordx4 v92, s[36:39], s53 offen offset:384 lds\n"
    "v_mul_f32 v85, s81, v66\n"
    "v_permlane16_swap_b32 v68, v69\n"
    "v_max_f32_e64 v70, v70, v71\n"
    "v_mov_b32_e64 v71, v70\n"
    "v_mfma_f32_16x16x32_bf16 v[16:19], a[148:151], a[20:23], v[16:19]\n"
    "s_add_u32 m0, m0, 1408\n"
    "v_max_f32_e64 v68, v68, v69\n"
    "v_mul_f32 v86, s81, v68\n"
    "v_mfma_f32_16x16x32_bf16 v[20:23], a[144:147], a[48:51], v[20:23]\n"
    "v_permlane16_swap_b32 v70, v71\n"
    "v_mfma_f32_16x16x32_bf16 v[20:23], a[148:151], a[52:55], v[20:23]\n"
    "v_max_f32_e64 v70, v70, v71\n"
    "v_mul_f32_e64 v87, s81, v70\n"
    "v_mfma_f32_16x16x32_bf16 v[24:27], a[144:147], a[80:83], v[24:27]\n"
    "buffer_load_dwordx4 v92, s[36:39], s54 offen offset:0 lds\n"
    "v_mfma_f32_16x16x32_bf16 v[24:27], a[148:151], a[84:87], v[24:27]\n"
    "s_add_u32 m0, m0, 896\n"
    "v_fma_f32 v0, v0, s52, v84\n"
    "v_fma_f32 v2, v2, s52, v84\n"
    "v_mfma_f32_16x16x32_bf16 v[28:31], a[144:147], a[112:115], v[28:31]\n"
    "s_nop 0\n"
    "ds_read_b128 a[144:147], v88 offset:18432\n"
    "v_fma_f32 v4, v4, s52, v85\n"
    "v_mfma_f32_16x16x32_bf16 v[28:31], a[148:151], a[116:119], v[28:31]\n"
    "s_waitcnt lgkmcnt(5)\n"
    "ds_read_b128 a[148:151], v89 offset:18432\n"
    "v_fma_f32 v6, v6, s52, v85\n"
    "v_mfma_f32_16x16x32_bf16 v[16:19], a[152:155], a[24:27], v[16:19]\n"
    "buffer_load_dwordx4 v92, s[36:39], s54 offen offset:128 lds\n"
    "v_mfma_f32_16x16x32_bf16 v[16:19], a[156:159], a[28:31], v[16:19]\n"
    "s_add_u32 m0, m0, s97\n"
    "v_exp_f32 v0, v0\n"
    "v_mfma_f32_16x16x32_bf16 v[20:23], a[152:155], a[56:59], v[20:23]\n"
    "v_exp_f32_e64 v2, v2\n"
    "v_mfma_f32_16x16x32_bf16 v[20:23], a[156:159], a[60:63], v[20:23]\n"
    "v_exp_f32_e64 v4, v4\n"
    "v_mfma_f32_16x16x32_bf16 v[24:27], a[152:155], a[88:91], v[24:27]\n"
    "buffer_load_dwordx4 v92, s[36:39], s54 offen offset:256 lds\n"
    "v_mfma_f32_16x16x32_bf16 v[24:27], a[156:159], a[92:95], v[24:27]\n"
    "s_add_u32 m0, m0, 896\n"
    "v_exp_f32_e64 v6, v6\n"
    "v_mfma_f32_16x16x32_bf16 v[28:31], a[152:155], a[120:123], v[28:31]\n"
    "s_nop 0\n"
    "ds_read_b128 a[152:155], v88 offset:19456\n"
    "v_fma_f32 v8, v8, s52, v86\n"
    "v_mfma_f32_16x16x32_bf16 v[28:31], a[156:159], a[124:127], v[28:31]\n"
    "s_waitcnt lgkmcnt(5)\n"
    "ds_read_b128 a[156:159], v89 offset:19456\n"
    "v_fma_f32 v10, v10, s52, v86\n"
    "v_mfma_f32_16x16x32_bf16 v[32:35], a[128:131], a[0:3], 0\n"
    "buffer_load_dwordx4 v92, s[36:39], s54 offen offset:384 lds\n"
    "v_mfma_f32_16x16x32_bf16 v[32:35], a[132:135], a[4:7], v[32:35]\n"
    "s_add_u32 s53, s53, s96\n"
    "v_exp_f32 v8, v8\n"
    "v_mfma_f32_16x16x32_bf16 v[36:39], a[128:131], a[32:35], 0\n"
    "s_add_u32 s54, s54, 0x8000\n"
    "v_exp_f32_e64 v10, v10\n"
    "v_mfma_f32_16x16x32_bf16 v[36:39], a[132:135], a[36:39], v[36:39]\n"
    "v_fma_f32 v12, v12, s52, v87\n"
    "v_fma_f32 v14, v14, s52, v87\n"
    "v_mfma_f32_16x16x32_bf16 v[40:43], a[128:131], a[64:67], 0\n"
    "v_exp_f32_e64 v12, v12\n"
    "v_mfma_f32_16x16x32_bf16 v[40:43], a[132:135], a[68:71], v[40:43]\n"
    "v_exp_f32_e64 v14, v14\n"
    "v_mfma_f32_16x16x32_bf16 v[44:47], a[128:131], a[96:99], 0\n"
    "ds_read_b128 a[128:131], v88 offset:24576\n"
    "v_fma_f32 v1, v1, s52, v84\n"
    "v_mfma_f32_16x16x32_bf16 v[44:47], a[132:135], a[100:103], v[44:47]\n"
    "s_waitcnt lgkmcnt(5)\n"
    "ds_read_b128 a[132:135], v89 offset:24576\n"
    "v_fma_f32 v3, v3, s52, v84\n"
    "v_mfma_f32_16x16x32_bf16 v[32:35], a[136:139], a[8:11], v[32:35]\n"
    "v_exp_f32 v1, v1\n"
    "v_mfma_f32_16x16x32_bf16 v[32:35], a[140:143], a[12:15], v[32:35]\n"
    "v_exp_f32_e64 v3, v3\n"
    "v_mfma_f32_16x16x32_bf16 v[36:39], a[136:139], a[40:43], v[36:39]\n"
    "v_fma_f32 v5, v5, s52, v85\n"
    "v_fma_f32 v7, v7, s52, v85\n"
    "v_mfma_f32_16x16x32_bf16 v[36:39], a[140:143], a[44:47], v[36:39]\n"
    "v_exp_f32_e64 v5, v5\n"
    "v_mfma_f32_16x16x32_bf16 v[40:43], a[136:139], a[72:75], v[40:43]\n"
    "v_exp_f32_e64 v7, v7\n"
    "v_mfma_f32_16x16x32_bf16 v[40:43], a[140:143], a[76:79], v[40:43]\n"
    "v_fma_f32 v9, v9, s52, v86\n"
    "v_fma_f32 v11, v11, s52, v86\n"
    "v_mfma_f32_16x16x32_bf16 v[44:47], a[136:139], a[104:107], v[44:47]\n"
    "ds_read_b128 a[136:139], v88 offset:25600\n"
    "v_fma_f32 v13, v13, s52, v87\n"
    "v_mfma_f32_16x16x32_bf16 v[44:47], a[140:143], a[108:111], v[44:47]\n"
    "s_waitcnt lgkmcnt(5)\n"
    "ds_read_b128 a[140:143], v89 offset:25600\n"
    "v_fma_f32 v15, v15, s52, v87\n"
    "v_mfma_f32_16x16x32_bf16 v[32:35], a[144:147], a[16:19], v[32:35]\n"
    "v_exp_f32 v9, v9\n"
    "v_mfma_f32_16x16x32_bf16 v[32:35], a[148:151], a[20:23], v[32:35]\n"
    "v_exp_f32 v13, v13\n"
    "v_mfma_f32_16x16x32_bf16 v[36:39], a[144:147], a[48:51], v[36:39]\n"
    "v_exp_f32 v11, v11\n"
    "v_mfma_f32_16x16x32_bf16 v[36:39], a[148:151], a[52:55], v[36:39]\n"
    "v_exp_f32 v15, v15\n"
    "v_mfma_f32_16x16x32_bf16 v[40:43], a[144:147], a[80:83], v[40:43]\n"
    "v_add_f32 v80, v0, v1\n"
    "v_add_f32 v81, v4, v5\n"
    "v_mfma_f32_16x16x32_bf16 v[40:43], a[148:151], a[84:87], v[40:43]\n"
    "v_add_f32 v82, v8, v9\n"
    "v_add_f32 v83, v12, v13\n"
    "v_mfma_f32_16x16x32_bf16 v[44:47], a[144:147], a[112:115], v[44:47]\n"
    "ds_read_b128 a[144:147], v88 offset:26624\n"
    "v_add_f32 v80, v80, v2\n"
    "v_mfma_f32_16x16x32_bf16 v[44:47], a[148:151], a[116:119], v[44:47]\n"
    "s_waitcnt lgkmcnt(5)\n"
    "ds_read_b128 a[148:151], v89 offset:26624\n"
    "v_add_f32 v81, v81, v6\n"
    "v_mfma_f32_16x16x32_bf16 v[32:35], a[152:155], a[24:27], v[32:35]\n"
    "v_add_f32_e64 v82, v82, v10\n"
    "v_add_f32_e64 v83, v83, v14\n"
    "v_mfma_f32_16x16x32_bf16 v[32:35], a[156:159], a[28:31], v[32:35]\n"
    "v_add_f32_e64 v80, v80, v3\n"
    "v_add_f32_e64 v81, v81, v7\n"
    "v_mfma_f32_16x16x32_bf16 v[36:39], a[152:155], a[56:59], v[36:39]\n"
    "v_add_f32_e64 v82, v82, v11\n"
    "v_add_f32_e64 v83, v83, v15\n"
    "v_mfma_f32_16x16x32_bf16 v[36:39], a[156:159], a[60:63], v[36:39]\n"
    "v_cvt_pk_bf16_f32 v0, v0, v1\n"
    "v_cvt_pk_bf16_f32 v1, v2, v3\n"
    "v_mfma_f32_16x16x32_bf16 v[40:43], a[152:155], a[88:91], v[40:43]\n"
    "v_cvt_pk_bf16_f32 v4, v4, v5\n"
    "v_cvt_pk_bf16_f32 v5, v6, v7\n"
    "v_mfma_f32_16x16x32_bf16 v[40:43], a[156:159], a[92:95], v[40:43]\n"
    "v_cvt_pk_bf16_f32 v8, v8, v9\n"
    "v_cvt_pk_bf16_f32 v9, v10, v11\n"
    "v_mfma_f32_16x16x32_bf16 v[44:47], a[152:155], a[120:123], v[44:47]\n"
    "ds_read_b128 a[152:155], v88 offset:27648\n"
    "v_cvt_pk_bf16_f32 v12, v12, v13\n"
    "v_mfma_f32_16x16x32_bf16 v[44:47], a[156:159], a[124:127], v[44:47]\n"
    "s_waitcnt lgkmcnt(5)\n"
    "ds_read_b128 a[156:159], v89 offset:27648\n"
    "v_cvt_pk_bf16_f32 v13, v14, v15\n"
    "v_mfma_f32_16x16x32_bf16 v[48:51], a[128:131], a[0:3], 0\n"
    "s_nop 0\n"
    "v_fma_f32 v16, v16, s52, v84\n"
    "v_fma_f32 v18, v18, s52, v84\n"
    "v_mfma_f32_16x16x32_bf16 v[48:51], a[132:135], a[4:7], v[48:51]\n"
    "v_exp_f32_e64 v16, v16\n"
    "v_mfma_f32_16x16x32_bf16 v[52:55], a[128:131], a[32:35], 0\n"
    "v_exp_f32_e64 v18, v18\n"
    "v_mfma_f32_16x16x32_bf16 v[52:55], a[132:135], a[36:39], v[52:55]\n"
    "v_fma_f32 v20, v20, s52, v85\n"
    "v_fma_f32 v22, v22, s52, v85\n"
    "v_mfma_f32_16x16x32_bf16 v[56:59], a[128:131], a[64:67], 0\n"
    "v_exp_f32_e64 v20, v20\n"
    "v_mfma_f32_16x16x32_bf16 v[56:59], a[132:135], a[68:71], v[56:59]\n"
    "v_exp_f32 v22, v22\n"
    "v_mfma_f32_16x16x32_bf16 v[60:63], a[128:131], a[96:99], 0\n"
    "s_waitcnt vmcnt(8) lgkmcnt(0)\n"
    "v_fma_f32 v24, v24, s52, v86\n"
    "v_fma_f32 v26, v26, s52, v86\n"
    "v_mfma_f32_16x16x32_bf16 v[60:63], a[132:135], a[100:103], v[60:63]\n"
    "s_barrier\n"
    "v_mfma_f32_16x16x32_bf16 v[48:51], a[136:139], a[8:11], v[48:51]\n"
    "s_mov_b32 m0, s60\n"
    "ds_read_b64_tr_b16 a[128:129], v90 offset:16384\n"
    "ds_read_b64_tr_b16 a[130:131], v90 offset:24576\n"
    "v_mfma_f32_16x16x32_bf16 v[48:51], a[140:143], a[12:15], v[48:51]\n"
    "ds_read_b64_tr_b16 a[132:133], v90 offset:16392\n"
    "ds_read_b64_tr_b16 a[134:135], v90 offset:24584\n"
    "v_mfma_f32_16x16x32_bf16 v[52:55], a[136:139], a[40:43], v[52:55]\n"
    "buffer_load_dwordx4 v92, s[40:43], s55 offen offset:0 lds\n"
    "v_mfma_f32_16x16x32_bf16 v[52:55], a[140:143], a[44:47], v[52:55]\n"
    "s_add_u32 m0, m0, s97\n"
    "v_exp_f32 v24, v24\n"
    "v_mfma_f32_16x16x32_bf16 v[56:59], a[136:139], a[72:75], v[56:59]\n"
    "v_exp_f32_e64 v26, v26\n"
    "v_mfma_f32_16x16x32_bf16 v[56:59], a[140:143], a[76:79], v[56:59]\n"
    "v_fma_f32 v28, v28, s52, v87\n"
    "v_fma_f32 v30, v30, s52, v87\n"
    "v_mfma_f32_16x16x32_bf16 v[60:63], a[136:139], a[104:107], v[60:63]\n"
    "ds_read_b64_tr_b16 a[136:137], v91 offset:16384\n"
    "ds_read_b64_tr_b16 a[138:139], v91 offset:24576\n"
    "v_mfma_f32_16x16x32_bf16 v[60:63], a[140:143], a[108:111], v[60:63]\n"
    "ds_read_b64_tr_b16 a[140:141], v91 offset:16392\n"
    "ds_read_b64_tr_b16 a[142:143], v91 offset:24584\n"
    "v_mfma_f32_16x16x32_bf16 v[48:51], a[144:147], a[16:19], v[48:51]\n"
    "buffer_load_dwordx4 v92, s[40:43], s55 offen offset:128 lds\n"
    "v_mfma_f32_16x16x32_bf16 v[48:51], a[148:151], a[20:23], v[48:51]\n"
    "s_add_u32 m0, m0, s97\n"
    "v_exp_f32 v28, v28\n"
    "v_mfma_f32_16x16x32_bf16 v[52:55], a[144:147], a[48:51], v[52:55]\n"
    "v_exp_f32_e64 v30, v30\n"
    "v_mfma_f32_16x16x32_bf16 v[52:55], a[148:151], a[52:55], v[52:55]\n"
    "v_fma_f32 v17, v17, s52, v84\n"
    "v_fma_f32 v19, v19, s52, v84\n"
    "v_mfma_f32_16x16x32_bf16 v[56:59], a[144:147], a[80:83], v[56:59]\n"
    "buffer_load_dwordx4 v92, s[40:43], s55 offen offset:256 lds\n"
    "v_mfma_f32_16x16x32_bf16 v[56:59], a[148:151], a[84:87], v[56:59]\n"
    "s_add_u32 m0, m0, s97\n"
    "v_exp_f32 v17, v17\n"
    "v_mfma_f32_16x16x32_bf16 v[60:63], a[144:147], a[112:115], v[60:63]\n"
    "ds_read_b64_tr_b16 a[144:145], v90 offset:17408\n"
    "ds_read_b64_tr_b16 a[146:147], v90 offset:25600\n"
    "v_mfma_f32_16x16x32_bf16 v[60:63], a[148:151], a[116:119], v[60:63]\n"
    "ds_read_b64_tr_b16 a[148:149], v90 offset:17416\n"
    "ds_read_b64_tr_b16 a[150:151], v90 offset:25608\n"
    "v_mfma_f32_16x16x32_bf16 v[48:51], a[152:155], a[24:27], v[48:51]\n"
    "buffer_load_dwordx4 v92, s[40:43], s55 offen offset:384 lds\n"
    "v_mfma_f32_16x16x32_bf16 v[48:51], a[156:159], a[28:31], v[48:51]\n"
    "s_add_u32 m0, m0, 1408\n"
    "v_fma_f32 v21, v21, s52, v85\n"
    "v_fma_f32 v23, v23, s52, v85\n"
    "v_fma_f32 v25, v25, s52, v86\n"
    "v_fma_f32 v27, v27, s52, v86\n"
    "v_fma_f32 v29, v29, s52, v87\n"
    "v_fma_f32 v31, v31, s52, v87\n"
    "v_mfma_f32_16x16x32_bf16 v[52:55], a[152:155], a[56:59], v[52:55]\n"
    "v_add_f32 v80, v80, v16\n"
    "v_exp_f32 v21, v21\n"
    "v_add_f32 v81, v81, v20\n"
    "v_exp_f32 v25, v25\n"
    "v_add_f32 v82, v82, v24\n"
    "v_exp_f32 v29, v29\n"
    "v_add_f32 v83, v83, v28\n"
    "v_mfma_f32_16x16x32_bf16 v[52:55], a[156:159], a[60:63], v[52:55]\n"
    "v_add_f32 v80, v80, v17\n"
    "v_exp_f32 v19, v19\n"
    "v_add_f32 v81, v81, v21\n"
    "v_exp_f32 v23, v23\n"
    "v_add_f32 v82, v82, v25\n"
    "v_exp_f32 v27, v27\n"
    "v_add_f32 v83, v83, v29\n"
    "v_exp_f32 v31, v31\n"
    "v_mfma_f32_16x16x32_bf16 v[56:59], a[152:155], a[88:91], v[56:59]\n"
    "buffer_load_dwordx4 v92, s[40:43], s56 offen offset:0 lds\n"
    "v_add_f32 v80, v80, v18\n"
    "v_add_f32 v81, v81, v22\n"
    "v_add_f32 v82, v82, v26\n"
    "v_add_f32 v83, v83, v30\n"
    "v_mfma_f32_16x16x32_bf16 v[56:59], a[156:159], a[92:95], v[56:59]\n"
    "s_add_u32 m0, m0, s97\n"
    "v_cvt_pk_bf16_f32 v2, v16, v17\n"
    "v_add_f32 v80, v80, v19\n"
    "v_cvt_pk_bf16_f32 v3, v18, v19\n"
    "v_add_f32 v81, v81, v23\n"
    "v_add_f32 v82, v82, v27\n"
    "v_add_f32 v83, v83, v31\n"
    "v_mfma_f32_16x16x32_bf16 v[60:63], a[152:155], a[120:123], v[60:63]\n"
    "s_nop 0\n"
    "ds_read_b64_tr_b16 a[152:153], v91 offset:17408\n"
    "ds_read_b64_tr_b16 a[154:155], v91 offset:25600\n"
    "v_mfma_f32_16x16x32_bf16 v[60:63], a[156:159], a[124:127], v[60:63]\n"
    "s_waitcnt lgkmcnt(10)\n"
    "ds_read_b64_tr_b16 a[156:157], v91 offset:17416\n"
    "ds_read_b64_tr_b16 a[158:159], v91 offset:25608\n"
    "v_cvt_pk_bf16_f32 v6, v20, v21\n"
    "v_cvt_pk_bf16_f32 v7, v22, v23\n"
    "v_mfma_f32_16x16x32_bf16 v[96:99], a[128:131], v[0:3], 0\n"
    "buffer_load_dwordx4 v92, s[40:43], s56 offen offset:128 lds\n"
    "v_mfma_f32_16x16x32_bf16 v[100:103], a[132:135], v[0:3], 0\n"
    "s_add_u32 m0, m0, 896\n"
    "v_cvt_pk_bf16_f32 v10, v24, v25\n"
    "v_cvt_pk_bf16_f32 v11, v26, v27\n"
    "v_mfma_f32_16x16x32_bf16 v[160:163], a[128:131], v[4:7], 0\n"
    "v_cvt_pk_bf16_f32 v14, v28, v29\n"
    "v_cvt_pk_bf16_f32 v15, v30, v31\n"
    "v_mfma_f32_16x16x32_bf16 v[164:167], a[132:135], v[4:7], 0\n"
    "v_fma_f32 v32, v32, s52, v84\n"
    "v_fma_f32 v34, v34, s52, v84\n"
    "v_mfma_f32_16x16x32_bf16 v[224:227], a[128:131], v[8:11], 0\n"
    "buffer_load_dwordx4 v92, s[40:43], s56 offen offset:256 lds\n"
    "v_mfma_f32_16x16x32_bf16 v[228:231], a[132:135], v[8:11], 0\n"
    "s_add_u32 m0, m0, s97\n"
    "v_exp_f32 v32, v32\n"
    "v_mfma_f32_16x16x32_bf16 a[160:163], a[128:131], v[12:15], 0\n"
    "ds_read_b64_tr_b16 a[128:129], v90 offset:18432\n"
    "ds_read_b64_tr_b16 a[130:131], v90 offset:26624\n"
    "v_mfma_f32_16x16x32_bf16 a[164:167], a[132:135], v[12:15], 0\n"
    "s_waitcnt lgkmcnt(10)\n"
    "ds_read_b64_tr_b16 a[132:133], v90 offset:18440\n"
    "ds_read_b64_tr_b16 a[134:135], v90 offset:26632\n"
    "v_mfma_f32_16x16x32_bf16 v[104:107], a[136:139], v[0:3], 0\n"
    "s_nop 0\n"
    "buffer_load_dwordx4 v92, s[40:43], s56 offen offset:384 lds\n"
    "v_mfma_f32_16x16x32_bf16 v[108:111], a[140:143], v[0:3], 0\n"
    "s_add_u32 s55, s55, s96\n"
    "v_exp_f32 v34, v34\n"
    "v_mfma_f32_16x16x32_bf16 v[168:171], a[136:139], v[4:7], 0\n"
    "s_add_u32 s56, s56, s96\n"
    "v_fma_f32 v36, v36, s52, v85\n"
    "v_fma_f32 v38, v38, s52, v85\n"
    "v_mfma_f32_16x16x32_bf16 v[172:175], a[140:143], v[4:7], 0\n"
    "s_mov_b32 m0, s59\n"
    "v_exp_f32 v36, v36\n"
    "v_mfma_f32_16x16x32_bf16 v[232:235], a[136:139], v[8:11], 0\n"
    "buffer_load_dwordx4 v92, s[44:47], s57 offen offset:0 nt lds\n"
    "v_mfma_f32_16x16x32_bf16 v[236:239], a[140:143], v[8:11], 0\n"
    "s_add_u32 m0, m0, s97\n"
    "v_exp_f32_e64 v38, v38\n"
    "v_mfma_f32_16x16x32_bf16 a[168:171], a[136:139], v[12:15], 0\n"
    "ds_read_b64_tr_b16 a[136:137], v91 offset:18432\n"
    "ds_read_b64_tr_b16 a[138:139], v91 offset:26624\n"
    "v_mfma_f32_16x16x32_bf16 a[172:175], a[140:143], v[12:15], 0\n"
    "s_waitcnt lgkmcnt(10)\n"
    "ds_read_b64_tr_b16 a[140:141], v91 offset:18440\n"
    "ds_read_b64_tr_b16 a[142:143], v91 offset:26632\n"
    "v_mfma_f32_16x16x32_bf16 v[112:115], a[144:147], v[0:3], 0\n"
    "buffer_load_dwordx4 v92, s[44:47], s57 offen offset:128 nt lds\n"
    "v_mfma_f32_16x16x32_bf16 v[116:119], a[148:151], v[0:3], 0\n"
    "s_add_u32 s57, s57, s58\n"
    "v_fma_f32 v40, v40, s52, v86\n"
    "v_fma_f32 v42, v42, s52, v86\n"
    "v_mfma_f32_16x16x32_bf16 v[176:179], a[144:147], v[4:7], 0\n"
    "s_xor_b32 s58, s58, 0xe00\n"
    "v_exp_f32_e64 v40, v40\n"
    "v_mfma_f32_16x16x32_bf16 v[180:183], a[148:151], v[4:7], 0\n"
    "s_add_u32 s59, s59, 0x800\n"
    "v_exp_f32_e64 v42, v42\n"
    "v_mfma_f32_16x16x32_bf16 v[240:243], a[144:147], v[8:11], 0\n"
    "v_fma_f32 v44, v44, s52, v87\n"
    "v_fma_f32 v46, v46, s52, v87\n"
    "v_mfma_f32_16x16x32_bf16 v[244:247], a[148:151], v[8:11], 0\n"
    "v_exp_f32_e64 v44, v44\n"
    "v_mfma_f32_16x16x32_bf16 a[176:179], a[144:147], v[12:15], 0\n"
    "ds_read_b64_tr_b16 a[144:145], v90 offset:19456\n"
    "ds_read_b64_tr_b16 a[146:147], v90 offset:27648\n"
    "v_mfma_f32_16x16x32_bf16 a[180:183], a[148:151], v[12:15], 0\n"
    "s_waitcnt lgkmcnt(10)\n"
    "ds_read_b64_tr_b16 a[148:149], v90 offset:19464\n"
    "ds_read_b64_tr_b16 a[150:151], v90 offset:27656\n"
    "v_mfma_f32_16x16x32_bf16 v[120:123], a[152:155], v[0:3], 0\n"
    "v_exp_f32 v46, v46\n"
    "v_mfma_f32_16x16x32_bf16 v[124:127], a[156:159], v[0:3], 0\n"
    "v_fma_f32 v33, v33, s52, v84\n"
    "v_fma_f32 v35, v35, s52, v84\n"
    "v_mfma_f32_16x16x32_bf16 v[184:187], a[152:155], v[4:7], 0\n"
    "v_exp_f32_e64 v33, v33\n"
    "v_mfma_f32_16x16x32_bf16 v[188:191], a[156:159], v[4:7], 0\n"
    "v_fma_f32 v37, v37, s52, v85\n"
    "v_fma_f32 v39, v39, s52, v85\n"
    "v_fma_f32 v41, v41, s52, v86\n"
    "v_fma_f32 v43, v43, s52, v86\n"
    "v_fma_f32 v45, v45, s52, v87\n"
    "v_fma_f32 v47, v47, s52, v87\n"
    "v_mfma_f32_16x16x32_bf16 v[248:251], a[152:155], v[8:11], 0\n"
    "v_add_f32 v80, v80, v32\n"
    "v_exp_f32 v37, v37\n"
    "v_add_f32 v81, v81, v36\n"
    "v_exp_f32 v41, v41\n"
    "v_add_f32 v82, v82, v40\n"
    "v_exp_f32 v45, v45\n"
    "v_add_f32 v83, v83, v44\n"
    "v_mfma_f32_16x16x32_bf16 v[252:255], a[156:159], v[8:11], 0\n"
    "v_add_f32 v80, v80, v33\n"
    "v_exp_f32 v35, v35\n"
    "v_add_f32 v81, v81, v37\n"
    "v_exp_f32 v39, v39\n"
    "v_add_f32 v82, v82, v41\n"
    "v_exp_f32 v43, v43\n"
    "v_add_f32 v83, v83, v45\n"
    "v_exp_f32_e64 v47, v47\n"
    "v_mfma_f32_16x16x32_bf16 a[184:187], a[152:155], v[12:15], 0\n"
    "ds_read_b64_tr_b16 a[152:153], v91 offset:19456\n"
    "ds_read_b64_tr_b16 a[154:155], v91 offset:27648\n"
    "v_add_f32 v80, v80, v34\n"
    "v_add_f32 v81, v81, v38\n"
    "v_add_f32 v82, v82, v42\n"
    "v_add_f32 v83, v83, v46\n"
    "v_mfma_f32_16x16x32_bf16 a[188:191], a[156:159], v[12:15], 0\n"
    "s_waitcnt lgkmcnt(10)\n"
    "ds_read_b64_tr_b16 a[156:157], v91 offset:19464\n"
    "ds_read_b64_tr_b16 a[158:159], v91 offset:27656\n"
    "v_add_f32 v80, v80, v35\n"
    "v_add_f32 v81, v81, v39\n"
    "v_add_f32 v82, v82, v43\n"
    "v_add_f32 v83, v83, v47\n"
    "v_mfma_f32_16x16x32_bf16 v[128:131], a[128:131], v[0:3], 0\n"
    "v_add_u32 v88, s62, v88\n"
    "v_add_u32_e64 v89, s62, v89\n"
    "v_mfma_f32_16x16x32_bf16 v[132:135], a[132:135], v[0:3], 0\n"
    "v_fma_f32 v48, v48, s52, v84\n"
    "v_fma_f32 v50, v50, s52, v84\n"
    "v_mfma_f32_16x16x32_bf16 v[192:195], a[128:131], v[4:7], 0\n"
    "v_exp_f32_e64 v48, v48\n"
    "v_mfma_f32_16x16x32_bf16 v[196:199], a[132:135], v[4:7], 0\n"
    "v_exp_f32_e64 v50, v50\n"
    "v_mfma_f32_16x16x32_bf16 a[224:227], a[128:131], v[8:11], 0\n"
    "v_fma_f32 v52, v52, s52, v85\n"
    "v_fma_f32 v54, v54, s52, v85\n"
    "v_mfma_f32_16x16x32_bf16 a[228:231], a[132:135], v[8:11], 0\n"
    "v_exp_f32 v52, v52\n"
    "v_mfma_f32_16x16x32_bf16 a[192:195], a[128:131], v[12:15], 0\n"
    "s_waitcnt vmcnt(10) lgkmcnt(0)\n"
    "v_exp_f32 v54, v54\n"
    "v_mfma_f32_16x16x32_bf16 a[196:199], a[132:135], v[12:15], 0\n"
    "s_barrier\n"
    "v_mfma_f32_16x16x32_bf16 v[136:139], a[136:139], v[0:3], 0\n"
    "ds_read_b128 a[128:131], v88 offset:0\n"
    "ds_read_b128 a[132:135], v89 offset:0\n"
    "v_mfma_f32_16x16x32_bf16 v[140:143], a[140:143], v[0:3], 0\n"
    "v_fma_f32 v56, v56, s52, v86\n"
    "v_fma_f32 v58, v58, s52, v86\n"
    "v_mfma_f32_16x16x32_bf16 v[200:203], a[136:139], v[4:7], 0\n"
    "v_exp_f32 v56, v56\n"
    "v_mfma_f32_16x16x32_bf16 v[204:207], a[140:143], v[4:7], 0\n"
    "v_exp_f32 v58, v58\n"
    "v_mfma_f32_16x16x32_bf16 a[232:235], a[136:139], v[8:11], 0\n"
    "v_fma_f32 v60, v60, s52, v87\n"
    "v_fma_f32 v62, v62, s52, v87\n"
    "v_mfma_f32_16x16x32_bf16 a[236:239], a[140:143], v[8:11], 0\n"
    "v_exp_f32_e64 v60, v60\n"
    "v_mfma_f32_16x16x32_bf16 a[200:203], a[136:139], v[12:15], 0\n"
    "ds_read_b128 a[136:139], v88 offset:1024\n"
    "v_fma_f32 v49, v49, s52, v84\n"
    "v_mfma_f32_16x16x32_bf16 a[204:207], a[140:143], v[12:15], 0\n"
    "ds_read_b128 a[140:143], v89 offset:1024\n"
    "v_fma_f32 v51, v51, s52, v84\n"
    "v_mfma_f32_16x16x32_bf16 v[144:147], a[144:147], v[0:3], 0\n"
    "v_exp_f32_e64 v49, v49\n"
    "v_mfma_f32_16x16x32_bf16 v[148:151], a[148:151], v[0:3], 0\n"
    "v_exp_f32 v62, v62\n"
    "v_mfma_f32_16x16x32_bf16 v[208:211], a[144:147], v[4:7], 0\n"
    "v_exp_f32 v51, v51\n"
    "v_mfma_f32_16x16x32_bf16 v[212:215], a[148:151], v[4:7], 0\n"
    "v_fma_f32 v53, v53, s52, v85\n"
    "v_fma_f32 v55, v55, s52, v85\n"
    "v_mfma_f32_16x16x32_bf16 a[240:243], a[144:147], v[8:11], 0\n"
    "v_exp_f32_e64 v53, v53\n"
    "v_mfma_f32_16x16x32_bf16 a[244:247], a[148:151], v[8:11], 0\n"
    "v_exp_f32_e64 v55, v55\n"
    "v_mfma_f32_16x16x32_bf16 a[208:211], a[144:147], v[12:15], 0\n"
    "ds_read_b128 a[144:147], v88 offset:2048\n"
    "v_fma_f32 v57, v57, s52, v86\n"
    "v_mfma_f32_16x16x32_bf16 a[212:215], a[148:151], v[12:15], 0\n"
    "ds_read_b128 a[148:151], v89 offset:2048\n"
    "v_fma_f32 v59, v59, s52, v86\n"
    "v_mfma_f32_16x16x32_bf16 v[152:155], a[152:155], v[0:3], 0\n"
    "v_exp_f32 v57, v57\n"
    "v_mfma_f32_16x16x32_bf16 v[156:159], a[156:159], v[0:3], 0\n"
    "v_exp_f32 v59, v59\n"
    "v_mfma_f32_16x16x32_bf16 v[216:219], a[152:155], v[4:7], 0\n"
    "v_fma_f32 v61, v61, s52, v87\n"
    "v_fma_f32 v63, v63, s52, v87\n"
    "v_mfma_f32_16x16x32_bf16 v[220:223], a[156:159], v[4:7], 0\n"
    "v_add_f32 v80, v80, v48\n"
    "v_add_f32 v81, v81, v52\n"
    "v_add_f32 v82, v82, v56\n"
    "v_exp_f32 v61, v61\n"
    "v_add_f32 v83, v83, v60\n"
    "v_mfma_f32_16x16x32_bf16 a[248:251], a[152:155], v[8:11], 0\n"
    "v_add_f32 v80, v80, v49\n"
    "v_add_f32 v81, v81, v53\n"
    "v_add_f32 v82, v82, v57\n"
    "v_add_f32 v83, v83, v61\n"
    "v_exp_f32 v63, v63\n"
    "v_mfma_f32_16x16x32_bf16 a[252:255], a[156:159], v[8:11], 0\n"
    "v_add_f32 v80, v80, v50\n"
    "v_add_f32 v81, v81, v54\n"
    "v_add_f32 v82, v82, v58\n"
    "v_add_f32 v83, v83, v62\n"
    "v_mfma_f32_16x16x32_bf16 a[216:219], a[152:155], v[12:15], 0\n"
    "ds_read_b128 a[152:155], v88 offset:3072\n"
    "v_add_f32 v80, v80, v51\n"
    "v_add_f32 v81, v81, v55\n"
    "v_add_f32 v82, v82, v59\n"
    "v_add_f32 v83, v83, v63\n"
    "v_mfma_f32_16x16x32_bf16 a[220:223], a[156:159], v[12:15], 0\n"
    "s_waitcnt lgkmcnt(5)\n"
    "ds_read_b128 a[156:159], v89 offset:3072\n"
    "s_branch LQ4_%=\n"
    ".p2align 5\n"
    "LSP3f_%=:\n"
    "s_cmp_eq_u32 s80, 1\n"
    "s_cbranch_scc1 GLSP3f_%=\n"
    "s_mov_b32 s57, s78\n"
    "s_mov_b32 s58, 0x100\n"
    "s_mov_b32 s59, s70\n"
    "s_mov_b32 s98, s78\n"
    "s_mov_b32 s99, 0x100\n"
    "s_mov_b32 s65, 5\n"
    "s_waitcnt lgkmcnt(6)\n"
    "v_mfma_f32_16x16x32_bf16 v[0:3], a[128:131], a[0:3], 0\n"
    "s_mov_b32 m0, s61\n"
    "v_add_u32 v93, 4096, v93\n"
    "v_mfma_f32_16x16x32_bf16 v[0:3], a[132:135], a[4:7], v[0:3]\n"
    "buffer_load_dwordx4 v92, s[40:43], s55 offen offset:0 lds\n"
    "v_mfma_f32_16x16x32_bf16 v[4:7], a[128:131], a[32:35], 0\n"
    "s_add_u32 m0, m0, 896\n"
    "v_mfma_f32_16x16x32_bf16 v[4:7], a[132:135], a[36:39], v[4:7]\n"
    "v_mfma_f32_16x16x32_bf16 v[8:11], a[128:131], a[64:67], 0\n"
    "ds_read_b128 a[128:131], v88 offset:8192\n"
    "v_mfma_f32_16x16x32_bf16 v[8:11], a[132:135], a[68:71], v[8:11]\n"
    "s_waitcnt lgkmcnt(5)\n"
    "ds_read_b128 a[132:135], v89 offset:8192\n"
    "v_mfma_f32_16x16x32_bf16 v[0:3], a[136:139], a[8:11], v[0:3]\n"
    "s_nop 0\n"
    "buffer_load_dwordx4 v92, s[40:43], s55 offen offset:128 lds\n"
    "v_mfma_f32_16x16x32_bf16 v[0:3], a[140:143], a[12:15], v[0:3]\n"
    "s_add_u32 m0, m0, 896\n"
    "v_mfma_f32_16x16x32_bf16 v[4:7], a[136:139], a[40:43], v[4:7]\n"
    "v_mfma_f32_16x16x32_bf16 v[4:7], a[140:143], a[44:47], v[4:7]\n"
    "buffer_load_dwordx4 v92, s[40:43], s55 offen offset:256 lds\n"
    "v_mfma_f32_16x16x32_bf16 v[8:11], a[136:139], a[72:75], v[8:11]\n"
    "s_add_u32 m0, m0, 896\n"
    "ds_read_b128 a[136:139], v88 offset:9216\n"
    "v_mfma_f32_16x16x32_bf16 v[8:11], a[140:143], a[76:79], v[8:11]\n"
    "s_waitcnt lgkmcnt(5)\n"
    "ds_read_b128 a[140:143], v89 offset:9216\n"
    "v_mfma_f32_16x16x32_bf16 v[0:3], a[144:147], a[16:19], v[0:3]\n"
    "s_nop 0\n"
    "buffer_load_dwordx4 v92, s[40:43], s55 offen offset:384 lds\n"
    "v_mfma_f32_16x16x32_bf16 v[0:3], a[148:151], a[20:23], v[0:3]\n"
    "s_add_u32 m0, m0, 1408\n"
    "v_mfma_f32_16x16x32_bf16 v[4:7], a[144:147], a[48:51], v[4:7]\n"
    "s_nop 0\n"
    "v_mfma_f32_16x16x32_bf16 v[4:7], a[148:151], a[52:55], v[4:7]\n"
    "s_nop 0\n"
    "buffer_load_dwordx4 v92, s[40:43], s56 offen offset:0 lds\n"
    "v_mfma_f32_16x16x32_bf16 v[8:11], a[144:147], a[80:83], v[8:11]\n"
    "s_add_u32 m0, m0, s97\n"
    "ds_read_b128 a[144:147], v88 offset:10240\n"
    "v_mfma_f32_16x16x32_bf16 v[8:11], a[148:151], a[84:87], v[8:11]\n"
    "s_waitcnt lgkmcnt(5)\n"
    "ds_read_b128 a[148:151], v89 offset:10240\n"
    "v_mfma_f32_16x16x32_bf16 v[0:3], a[152:155], a[24:27], v[0:3]\n"
    "buffer_load_dwordx4 v92, s[40:43], s56 offen offset:128 lds\n"
    "v_mfma_f32_16x16x32_bf16 v[0:3], a[156:159], a[28:31], v[0:3]\n"
    "s_add_u32 m0, m0, 896\n"
    "v_mfma_f32_16x16x32_bf16 v[4:7], a[152:155], a[56:59], v[4:7]\n"
    "v_mfma_f32_16x16x32_bf16 v[4:7], a[156:159], a[60:63], v[4:7]\n"
    "buffer_load_dwordx4 v92, s[40:43], s56 offen offset:256 lds\n"
    "v_mfma_f32_16x16x32_bf16 v[8:11], a[152:155], a[88:91], v[8:11]\n"
    "s_add_u32 m0, m0, s97\n"
    "ds_read_b128 a[152:155], v88 offset:11264\n"
    "v_mfma_f32_16x16x32_bf16 v[8:11], a[156:159], a[92:95], v[8:11]\n"
    "s_waitcnt lgkmcnt(5)\n"
    "ds_read_b128 a[156:159], v89 offset:11264\n"
    "v_max3_f32 v64, v0, v1, v2\n"
    "v_mfma_f32_16x16x32_bf16 v[16:19], a[128:131], a[0:3], 0\n"
    "buffer_load_dwordx4 v92, s[40:43], s56 offen offset:384 lds\n"
    "v_mfma_f32_16x16x32_bf16 v[16:19], a[132:135], a[4:7], v[16:19]\n"
    "s_add_u32 s55, s60, s96\n"
    "v_max_f32 v64, v64, v3\n"
    "v_max3_f32 v66, v4, v5, v6\n"
    "v_mfma_f32_16x16x32_bf16 v[20:23], a[128:131], a[32:35], 0\n"
    "s_add_u32 s56, s60, 0x9000\n"
    "v_mov_b32 v65, v64\n"
    "v_max_f32 v66, v66, v7\n"
    "v_mfma_f32_16x16x32_bf16 v[20:23], a[132:135], a[36:39], v[20:23]\n"
    "s_add_u32 m0, s60, s96\n"
    "v_mov_b32 v67, v66\n"
    "v_max3_f32 v68, v8, v9, v10\n"
    "v_mfma_f32_16x16x32_bf16 v[24:27], a[128:131], a[64:67], 0\n"
    "ds_read_b128 a[128:131], v88 offset:16384\n"
    "v_max_f32 v68, v68, v11\n"
    "v_mfma_f32_16x16x32_bf16 v[24:27], a[132:135], a[68:71], v[24:27]\n"
    "s_waitcnt lgkmcnt(5)\n"
    "ds_read_b128 a[132:135], v89 offset:16384\n"
    "v_mov_b32_e64 v69, v68\n"
    "v_mfma_f32_16x16x32_bf16 v[16:19], a[136:139], a[8:11], v[16:19]\n"
    "buffer_load_dwordx4 v92, s[36:39], s53 offen offset:0 lds\n"
    "v_mfma_f32_16x16x32_bf16 v[16:19], a[140:143], a[12:15], v[16:19]\n"
    "s_add_u32 m0, m0, 896\n"
    "v_permlane32_swap_b32 v64, v65\n"
    "v_mfma_f32_16x16x32_bf16 v[20:23], a[136:139], a[40:43], v[20:23]\n"
    "v_permlane32_swap_b32 v66, v67\n"
    "v_mfma_f32_16x16x32_bf16 v[20:23], a[140:143], a[44:47], v[20:23]\n"
    "buffer_load_dwordx4 v92, s[36:39], s53 offen offset:128 lds\n"
    "v_mfma_f32_16x16x32_bf16 v[24:27], a[136:139], a[72:75], v[24:27]\n"
    "s_add_u32 m0, m0, 896\n"
    "ds_read_b128 a[136:139], v88 offset:17408\n"
    "v_max_f32 v64, v64, v65\n"
    "v_mfma_f32_16x16x32_bf16 v[24:27], a[140:143], a[76:79], v[24:27]\n"
    "s_waitcnt lgkmcnt(5)\n"
    "ds_read_b128 a[140:143], v89 offset:17408\n"
    "v_mov_b32 v65, v64\n"
    "v_mfma_f32_16x16x32_bf16 v[16:19], a[144:147], a[16:19], v[16:19]\n"
    "buffer_load_dwordx4 v92, s[36:39], s53 offen offset:256 lds\n"
    "v_mfma_f32_16x16x32_bf16 v[16:19], a[148:151], a[20:23], v[16:19]\n"
    "s_add_u32 m0, m0, s97\n"
    "v_permlane16_swap_b32 v64, v65\n"
    "v_mfma_f32_16x16x32_bf16 v[20:23], a[144:147], a[48:51], v[20:23]\n"
    "v_max_f32 v66, v66, v67\n"
    "v_mov_b32_e64 v67, v66\n"
    "v_mfma_f32_16x16x32_bf16 v[20:23], a[148:151], a[52:55], v[20:23]\n"
    "buffer_load_dwordx4 v92, s[36:39], s53 offen offset:384 lds\n"
    "v_max_f32 v64, v64, v65\n"
    "v_permlane32_swap_b32 v68, v69\n"
    "v_mfma_f32_16x16x32_bf16 v[24:27], a[144:147], a[80:83], v[24:27]\n"
    "s_add_u32 m0, m0, 1408\n"
    "ds_read_b128 a[144:147], v88 offset:18432\n"
    "v_mul_f32 v84, s81, v64\n"
    "v_permlane16_swap_b32 v66, v67\n"
    "v_max_f32_e64 v68, v68, v69\n"
    "v_mov_b32_e64 v69, v68\n"
    "v_mfma_f32_16x16x32_bf16 v[24:27], a[148:151], a[84:87], v[24:27]\n"
    "s_waitcnt lgkmcnt(5)\n"
    "ds_read_b128 a[148:151], v89 offset:18432\n"
    "v_max_f32 v66, v66, v67\n"
    "v_mfma_f32_16x16x32_bf16 v[16:19], a[152:155], a[24:27], v[16:19]\n"
    "buffer_load_dwordx4 v92, s[36:39], s54 offen offset:0 lds\n"
    "v_mul_f32 v85, s81, v66\n"
    "v_permlane16_swap_b32 v68, v69\n"
    "v_mfma_f32_16x16x32_bf16 v[16:19], a[156:159], a[28:31], v[16:19]\n"
    "s_add_u32 m0, m0, 896\n"
    "v_max_f32_e64 v68, v68, v69\n"
    "v_mul_f32_e64 v86, s81, v68\n"
    "v_mfma_f32_16x16x32_bf16 v[20:23], a[152:155], a[56:59], v[20:23]\n"
    "v_fma_f32 v0, v0, s52, v84\n"
    "v_fma_f32 v2, v2, s52, v84\n"
    "v_mfma_f32_16x16x32_bf16 v[20:23], a[156:159], a[60:63], v[20:23]\n"
    "buffer_load_dwordx4 v92, s[36:39], s54 offen offset:128 lds\n"
    "v_mfma_f32_16x16x32_bf16 v[24:27], a[152:155], a[88:91], v[24:27]\n"
    "s_add_u32 m0, m0, s97\n"
    "ds_read_b128 a[152:155], v88 offset:19456\n"
    "v_fma_f32 v4, v4, s52, v85\n"
    "v_mfma_f32_16x16x32_bf16 v[24:27], a[156:159], a[92:95], v[24:27]\n"
    "s_waitcnt lgkmcnt(5)\n"
    "ds_read_b128 a[156:159], v89 offset:19456\n"
    "v_fma_f32 v6, v6, s52, v85\n"
    "v_mfma_f32_16x16x32_bf16 v[32:35], a[128:131], a[0:3], 0\n"
    "buffer_load_dwordx4 v92, s[36:39], s54 offen offset:256 lds\n"
    "v_mfma_f32_16x16x32_bf16 v[32:35], a[132:135], a[4:7], v[32:35]\n"
    "s_add_u32 m0, m0, 896\n"
    "v_exp_f32_e64 v0, v0\n"
    "v_mfma_f32_16x16x32_bf16 v[36:39], a[128:131], a[32:35], 0\n"
    "v_exp_f32_e64 v2, v2\n"
    "v_mfma_f32_16x16x32_bf16 v[36:39], a[132:135], a[36:39], v[36:39]\n"
    "buffer_load_dwordx4 v92, s[36:39], s54 offen offset:384 lds\n"
    "v_mfma_f32_16x16x32_bf16 v[40:43], a[128:131], a[64:67], 0\n"
    "s_add_u32 s53, s53, s96\n"
    "ds_read_b128 a[128:131], v88 offset:24576\n"
    "v_fma_f32 v8, v8, s52, v86\n"
    "v_mfma_f32_16x16x32_bf16 v[40:43], a[132:135], a[68:71], v[40:43]\n"
    "s_waitcnt lgkmcnt(5)\n"
    "ds_read_b128 a[132:135], v89 offset:24576\n"
    "v_fma_f32 v10, v10, s52, v86\n"
    "v_mfma_f32_16x16x32_bf16 v[32:35], a[136:139], a[8:11], v[32:35]\n"
    "s_add_u32 s54, s54, 0x8000\n"
    "v_exp_f32_e64 v4, v4\n"
    "v_mfma_f32_16x16x32_bf16 v[32:35], a[140:143], a[12:15], v[32:35]\n"
    "v_exp_f32_e64 v6, v6\n"
    "v_mfma_f32_16x16x32_bf16 v[36:39], a[136:139], a[40:43], v[36:39]\n"
    "v_exp_f32_e64 v8, v8\n"
    "v_mfma_f32_16x16x32_bf16 v[36:39], a[140:143], a[44:47], v[36:39]\n"
    "v_exp_f32_e64 v10, v10\n"
    "v_mfma_f32_16x16x32_bf16 v[40:43], a[136:139], a[72:75], v[40:43]\n"
    "s_nop 0\n"
    "ds_read_b128 a[136:139], v88 offset:25600\n"
    "v_fma_f32 v1, v1, s52, v84\n"
    "v_mfma_f32_16x16x32_bf16 v[40:43], a[140:143], a[76:79], v[40:43]\n"
    "s_waitcnt lgkmcnt(5)\n"
    "ds_read_b128 a[140:143], v89 offset:25600\n"
    "v_fma_f32 v3, v3, s52, v84\n"
    "v_mfma_f32_16x16x32_bf16 v[32:35], a[144:147], a[16:19], v[32:35]\n"
    "v_exp_f32_e64 v1, v1\n"
    "v_mfma_f32_16x16x32_bf16 v[32:35], a[148:151], a[20:23], v[32:35]\n"
    "v_exp_f32_e64 v3, v3\n"
    "v_mfma_f32_16x16x32_bf16 v[36:39], a[144:147], a[48:51], v[36:39]\n"
    "v_fma_f32 v5, v5, s52, v85\n"
    "v_fma_f32 v7, v7, s52, v85\n"
    "v_mfma_f32_16x16x32_bf16 v[36:39], a[148:151], a[52:55], v[36:39]\n"
    "v_exp_f32_e64 v5, v5\n"
    "v_mfma_f32_16x16x32_bf16 v[40:43], a[144:147], a[80:83], v[40:43]\n"
    "s_nop 0\n"
    "ds_read_b128 a[144:147], v88 offset:26624\n"
    "v_fma_f32 v9, v9, s52, v86\n"
    "v_mfma_f32_16x16x32_bf16 v[40:43], a[148:151], a[84:87], v[40:43]\n"
    "s_waitcnt lgkmcnt(5)\n"
    "ds_read_b128 a[148:151], v89 offset:26624\n"
    "v_fma_f32 v11, v11, s52, v86\n"
    "v_mfma_f32_16x16x32_bf16 v[32:35], a[152:155], a[24:27], v[32:35]\n"
    "v_exp_f32_e64 v9, v9\n"
    "v_mfma_f32_16x16x32_bf16 v[32:35], a[156:159], a[28:31], v[32:35]\n"
    "v_exp_f32 v7, v7\n"
    "v_mfma_f32_16x16x32_bf16 v[36:39], a[152:155], a[56:59], v[36:39]\n"
    "v_exp_f32 v11, v11\n"
    "v_mfma_f32_16x16x32_bf16 v[36:39], a[156:159], a[60:63], v[36:39]\n"
    "v_add_f32 v80, v0, v1\n"
    "v_add_f32 v81, v4, v5\n"
    "v_mfma_f32_16x16x32_bf16 v[40:43], a[152:155], a[88:91], v[40:43]\n"
    "ds_read_b128 a[152:155], v88 offset:27648\n"
    "v_add_f32_e64 v82, v8, v9\n"
    "v_mfma_f32_16x16x32_bf16 v[40:43], a[156:159], a[92:95], v[40:43]\n"
    "s_waitcnt lgkmcnt(5)\n"
    "ds_read_b128 a[156:159], v89 offset:27648\n"
    "v_add_f32 v80, v80, v2\n"
    "v_mfma_f32_16x16x32_bf16 v[48:51], a[128:131], a[0:3], 0\n"
    "v_add_f32_e64 v81, v81, v6\n"
    "v_add_f32_e64 v82, v82, v10\n"
    "v_mfma_f32_16x16x32_bf16 v[48:51], a[132:135], a[4:7], v[48:51]\n"
    "v_add_f32_e64 v80, v80, v3\n"
    "v_add_f32_e64 v81, v81, v7\n"
    "v_mfma_f32_16x16x32_bf16 v[52:55], a[128:131], a[32:35], 0\n"
    "v_add_f32_e64 v82, v82, v11\n"
    "v_cvt_pk_bf16_f32 v0, v0, v1\n"
    "v_mfma_f32_16x16x32_bf16 v[52:55], a[132:135], a[36:39], v[52:55]\n"
    "v_cvt_pk_bf16_f32 v1, v2, v3\n"
    "v_cvt_pk_bf16_f32 v4, v4, v5\n"
    "v_mfma_f32_16x16x32_bf16 v[56:59], a[128:131], a[64:67], 0\n"
    "s_waitcnt vmcnt(8) lgkmcnt(0)\n"
    "v_cvt_pk_bf16_f32 v5, v6, v7\n"
    "v_cvt_pk_bf16_f32 v8, v8, v9\n"
    "v_mfma_f32_16x16x32_bf16 v[56:59], a[132:135], a[68:71], v[56:59]\n"
    "s_barrier\n"
    "v_mfma_f32_16x16x32_bf16 v[48:51], a[136:139], a[8:11], v[48:51]\n"
    "s_mov_b32 m0, s60\n"
    "ds_read_b64_tr_b16 a[128:129], v90 offset:16384\n"
    "ds_read_b64_tr_b16 a[130:131], v90 offset:24576\n"
    "s_nop 0\n"
    "v_mfma_f32_16x16x32_bf16 v[48:51], a[140:143], a[12:15], v[48:51]\n"
    "ds_read_b64_tr_b16 a[132:133], v90 offset:16392\n"
    "ds_read_b64_tr_b16 a[134:135], v90 offset:24584\n"
    "v_mfma_f32_16x16x32_bf16 v[52:55], a[136:139], a[40:43], v[52:55]\n"
    "buffer_load_dwordx4 v92, s[40:43], s55 offen offset:0 lds\n"
    "v_mfma_f32_16x16x32_bf16 v[52:55], a[140:143], a[44:47], v[52:55]\n"
    "s_add_u32 m0, m0, 896\n"
    "v_cvt_pk_bf16_f32 v9, v10, v11\n"
    "v_fma_f32 v16, v16, s52, v84\n"
    "v_mfma_f32_16x16x32_bf16 v[56:59], a[136:139], a[72:75], v[56:59]\n"
    "ds_read_b64_tr_b16 a[136:137], v91 offset:16384\n"
    "ds_read_b64_tr_b16 a[138:139], v91 offset:24576\n"
    "v_mfma_f32_16x16x32_bf16 v[56:59], a[140:143], a[76:79], v[56:59]\n"
    "ds_read_b64_tr_b16 a[140:141], v91 offset:16392\n"
    "ds_read_b64_tr_b16 a[142:143], v91 offset:24584\n"
    "v_mfma_f32_16x16x32_bf16 v[48:51], a[144:147], a[16:19], v[48:51]\n"
    "buffer_load_dwordx4 v92, s[40:43], s55 offen offset:128 lds\n"
    "v_mfma_f32_16x16x32_bf16 v[48:51], a[148:151], a[20:23], v[48:51]\n"
    "s_add_u32 m0, m0, s97\n"
    "v_exp_f32 v16, v16\n"
    "v_mfma_f32_16x16x32_bf16 v[52:55], a[144:147], a[48:51], v[52:55]\n"
    "v_fma_f32 v18, v18, s52, v84\n"
    "v_fma_f32 v20, v20, s52, v85\n"
    "v_mfma_f32_16x16x32_bf16 v[52:55], a[148:151], a[52:55], v[52:55]\n"
    "v_exp_f32_e64 v20, v20\n"
    "v_mfma_f32_16x16x32_bf16 v[56:59], a[144:147], a[80:83], v[56:59]\n"
    "ds_read_b64_tr_b16 a[144:145], v90 offset:17408\n"
    "ds_read_b64_tr_b16 a[146:147], v90 offset:25600\n"
    "v_fma_f32 v22, v22, s52, v85\n"
    "v_fma_f32 v24, v24, s52, v86\n"
    "v_fma_f32 v26, v26, s52, v86\n"
    "v_mfma_f32_16x16x32_bf16 v[56:59], a[148:151], a[84:87], v[56:59]\n"
    "ds_read_b64_tr_b16 a[148:149], v90 offset:17416\n"
    "ds_read_b64_tr_b16 a[150:151], v90 offset:25608\n"
    "v_fma_f32 v17, v17, s52, v84\n"
    "v_fma_f32 v19, v19, s52, v84\n"
    "v_fma_f32 v21, v21, s52, v85\n"
    "v_fma_f32 v23, v23, s52, v85\n"
    "v_fma_f32 v25, v25, s52, v86\n"
    "v_exp_f32_e64 v24, v24\n"
    "v_fma_f32 v27, v27, s52, v86\n"
    "v_mfma_f32_16x16x32_bf16 v[48:51], a[152:155], a[24:27], v[48:51]\n"
    "buffer_load_dwordx4 v92, s[40:43], s55 offen offset:256 lds\n"
    "v_exp_f32 v17, v17\n"
    "v_add_f32 v80, v80, v16\n"
    "v_exp_f32 v18, v18\n"
    "v_exp_f32 v21, v21\n"
    "v_add_f32 v81, v81, v20\n"
    "v_exp_f32 v22, v22\n"
    "v_exp_f32 v25, v25\n"
    "v_add_f32 v82, v82, v24\n"
    "v_exp_f32 v26, v26\n"
    "v_mfma_f32_16x16x32_bf16 v[48:51], a[156:159], a[28:31], v[48:51]\n"
    "s_add_u32 m0, m0, s97\n"
    "v_add_f32 v80, v80, v17\n"
    "v_exp_f32 v19, v19\n"
    "v_add_f32 v81, v81, v21\n"
    "v_exp_f32 v23, v23\n"
    "v_add_f32 v82, v82, v25\n"
    "v_exp_f32 v27, v27\n"
    "v_mfma_f32_16x16x32_bf16 v[52:55], a[152:155], a[56:59], v[52:55]\n"
    "v_add_f32 v80, v80, v18\n"
    "v_add_f32 v81, v81, v22\n"
    "v_add_f32 v82, v82, v26\n"
    "v_mfma_f32_16x16x32_bf16 v[52:55], a[156:159], a[60:63], v[52:55]\n"
    "v_cvt_pk_bf16_f32 v2, v16, v17\n"
    "v_add_f32 v80, v80, v19\n"
    "v_cvt_pk_bf16_f32 v3, v18, v19\n"
    "v_add_f32_e64 v81, v81, v23\n"
    "v_add_f32_e64 v82, v82, v27\n"
    "v_mfma_f32_16x16x32_bf16 v[56:59], a[152:155], a[88:91], v[56:59]\n"
    "s_nop 0\n"
    "ds_read_b64_tr_b16 a[152:153], v91 offset:17408\n"
    "ds_read_b64_tr_b16 a[154:155], v91 offset:25600\n"
    "v_mfma_f32_16x16x32_bf16 v[56:59], a[156:159], a[92:95], v[56:59]\n"
    "s_waitcnt lgkmcnt(10)\n"
    "ds_read_b64_tr_b16 a[156:157], v91 offset:17416\n"
    "ds_read_b64_tr_b16 a[158:159], v91 offset:25608\n"
    "v_cvt_pk_bf16_f32 v6, v20, v21\n"
    "v_cvt_pk_bf16_f32 v7, v22, v23\n"
    "v_mfma_f32_16x16x32_bf16 v[96:99], a[128:131], v[0:3], 0\n"
    "buffer_load_dwordx4 v92, s[40:43], s55 offen offset:384 lds\n"
    "v_mfma_f32_16x16x32_bf16 v[100:103], a[132:135], v[0:3], 0\n"
    "s_add_u32 m0, m0, 1408\n"
    "v_cvt_pk_bf16_f32 v10, v24, v25\n"
    "v_cvt_pk_bf16_f32 v11, v26, v27\n"
    "v_mfma_f32_16x16x32_bf16 v[160:163], a[128:131], v[4:7], 0\n"
    "v_fma_f32 v32, v32, s52, v84\n"
    "v_fma_f32 v34, v34, s52, v84\n"
    "v_mfma_f32_16x16x32_bf16 v[164:167], a[132:135], v[4:7], 0\n"
    "v_exp_f32_e64 v32, v32\n"
    "v_mfma_f32_16x16x32_bf16 v[224:227], a[128:131], v[8:11], 0\n"
    "s_nop 0\n"
    "ds_read_b64_tr_b16 a[128:129], v90 offset:18432\n"
    "ds_read_b64_tr_b16 a[130:131], v90 offset:26624\n"
    "v_mfma_f32_16x16x32_bf16 v[228:231], a[132:135], v[8:11], 0\n"
    "s_waitcnt lgkmcnt(10)\n"
    "ds_read_b64_tr_b16 a[132:133], v90 offset:18440\n"
    "ds_read_b64_tr_b16 a[134:135], v90 offset:26632\n"
    "v_mfma_f32_16x16x32_bf16 v[104:107], a[136:139], v[0:3], 0\n"
    "buffer_load_dwordx4 v92, s[40:43], s56 offen offset:0 lds\n"
    "v_mfma_f32_16x16x32_bf16 v[108:111], a[140:143], v[0:3], 0\n"
    "s_add_u32 m0, m0, s97\n"
    "v_exp_f32 v34, v34\n"
    "v_mfma_f32_16x16x32_bf16 v[168:171], a[136:139], v[4:7], 0\n"
    "v_fma_f32 v36, v36, s52, v85\n"
    "v_fma_f32 v38, v38, s52, v85\n"
    "v_mfma_f32_16x16x32_bf16 v[172:175], a[140:143], v[4:7], 0\n"
    "v_exp_f32_e64 v36, v36\n"
    "v_mfma_f32_16x16x32_bf16 v[232:235], a[136:139], v[8:11], 0\n"
    "s_nop 0\n"
    "ds_read_b64_tr_b16 a[136:137], v91 offset:18432\n"
    "ds_read_b64_tr_b16 a[138:139], v91 offset:26624\n"
    "v_mfma_f32_16x16x32_bf16 v[236:239], a[140:143], v[8:11], 0\n"
    "s_waitcnt lgkmcnt(10)\n"
    "ds_read_b64_tr_b16 a[140:141], v91 offset:18440\n"
    "ds_read_b64_tr_b16 a[142:143], v91 offset:26632\n"
    "v_mfma_f32_16x16x32_bf16 v[112:115], a[144:147], v[0:3], 0\n"
    "buffer_load_dwordx4 v92, s[40:43], s56 offen offset:128 lds\n"
    "v_mfma_f32_16x16x32_bf16 v[116:119], a[148:151], v[0:3], 0\n"
    "s_add_u32 m0, m0, s97\n"
    "v_exp_f32 v38, v38\n"
    "v_mfma_f32_16x16x32_bf16 v[176:179], a[144:147], v[4:7], 0\n"
    "v_fma_f32 v40, v40, s52, v86\n"
    "v_fma_f32 v42, v42, s52, v86\n"
    "v_mfma_f32_16x16x32_bf16 v[180:183], a[148:151], v[4:7], 0\n"
    "v_exp_f32_e64 v40, v40\n"
    "v_mfma_f32_16x16x32_bf16 v[240:243], a[144:147], v[8:11], 0\n"
    "s_nop 0\n"
    "ds_read_b64_tr_b16 a[144:145], v90 offset:19456\n"
    "ds_read_b64_tr_b16 a[146:147], v90 offset:27648\n"
    "v_mfma_f32_16x16x32_bf16 v[244:247], a[148:151], v[8:11], 0\n"
    "s_waitcnt lgkmcnt(10)\n"
    "ds_read_b64_tr_b16 a[148:149], v90 offset:19464\n"
    "ds_read_b64_tr_b16 a[150:151], v90 offset:27656\n"
    "v_mfma_f32_16x16x32_bf16 v[120:123], a[152:155], v[0:3], 0\n"
    "buffer_load_dwordx4 v92, s[40:43], s56 offen offset:256 lds\n"
    "v_mfma_f32_16x16x32_bf16 v[124:127], a[156:159], v[0:3], 0\n"
    "s_add_u32 m0, m0, 896\n"
    "v_fma_f32 v33, v33, s52, v84\n"
    "v_fma_f32 v35, v35, s52, v84\n"
    "v_fma_f32 v37, v37, s52, v85\n"
    "v_fma_f32 v39, v39, s52, v85\n"
    "v_fma_f32 v41, v41, s52, v86\n"
    "v_fma_f32 v43, v43, s52, v86\n"
    "v_mfma_f32_16x16x32_bf16 v[184:187], a[152:155], v[4:7], 0\n"
    "v_exp_f32 v33, v33\n"
    "v_add_f32 v80, v80, v32\n"
    "v_exp_f32 v37, v37\n"
    "v_add_f32 v81, v81, v36\n"
    "v_exp_f32 v41, v41\n"
    "v_add_f32 v82, v82, v40\n"
    "v_exp_f32 v42, v42\n"
    "v_mfma_f32_16x16x32_bf16 v[188:191], a[156:159], v[4:7], 0\n"
    "v_add_f32 v80, v80, v33\n"
    "v_exp_f32 v35, v35\n"
    "v_add_f32 v81, v81, v37\n"
    "v_exp_f32 v39, v39\n"
    "v_add_f32 v82, v82, v41\n"
    "v_exp_f32 v43, v43\n"
    "v_mfma_f32_16x16x32_bf16 v[248:251], a[152:155], v[8:11], 0\n"
    "ds_read_b64_tr_b16 a[152:153], v91 offset:19456\n"
    "ds_read_b64_tr_b16 a[154:155], v91 offset:27648\n"
    "v_add_f32 v80, v80, v34\n"
    "v_add_f32 v81, v81, v38\n"
    "v_add_f32_e64 v82, v82, v42\n"
    "v_mfma_f32_16x16x32_bf16 v[252:255], a[156:159], v[8:11], 0\n"
    "s_waitcnt lgkmcnt(10)\n"
    "ds_read_b64_tr_b16 a[156:157], v91 offset:19464\n"
    "ds_read_b64_tr_b16 a[158:159], v91 offset:27656\n"
    "v_add_f32 v80, v80, v35\n"
    "v_add_f32 v81, v81, v39\n"
    "v_add_f32_e64 v82, v82, v43\n"
    "v_mfma_f32_16x16x32_bf16 v[128:131], a[128:131], v[0:3], 0\n"
    "buffer_load_dwordx4 v92, s[40:43], s56 offen offset:384 lds\n"
    "v_mfma_f32_16x16x32_bf16 v[132:135], a[132:135], v[0:3], 0\n"
    "s_add_u32 s55, s55, s96\n"
    "v_add_u32 v88, s62, v88\n"
    "v_add_u32_e64 v89, s62, v89\n"
    "v_mfma_f32_16x16x32_bf16 v[192:195], a[128:131], v[4:7], 0\n"
    "s_add_u32 s56, s56, 0x8000\n"
    "v_fma_f32 v48, v48, s52, v84\n"
    "v_fma_f32 v50, v50, s52, v84\n"
    "v_mfma_f32_16x16x32_bf16 v[196:199], a[132:135], v[4:7], 0\n"
    "s_mov_b32 m0, s59\n"
    "v_exp_f32_e64 v48, v48\n"
    "v_mfma_f32_16x16x32_bf16 a[224:227], a[128:131], v[8:11], 0\n"
    "s_waitcnt vmcnt(8) lgkmcnt(0)\n"
    "buffer_load_dwordx4 v92, s[44:47], s57 offen offset:0 nt lds\n"
    "v_mfma_f32_16x16x32_bf16 a[228:231], a[132:135], v[8:11], 0\n"
    "s_barrier\n"
    "v_mfma_f32_16x16x32_bf16 v[136:139], a[136:139], v[0:3], 0\n"
    "s_add_u32 m0, m0, s97\n"
    "ds_read_b128 a[128:131], v88 offset:0\n"
    "ds_read_b128 a[132:135], v89 offset:0\n"
    "v_mfma_f32_16x16x32_bf16 v[140:143], a[140:143], v[0:3], 0\n"
    "buffer_load_dwordx4 v92, s[44:47], s57 offen offset:128 nt lds\n"
    "v_mfma_f32_16x16x32_bf16 v[200:203], a[136:139], v[4:7], 0\n"
    "s_add_u32 s57, s57, s58\n"
    "v_exp_f32 v50, v50\n"
    "v_mfma_f32_16x16x32_bf16 v[204:207], a[140:143], v[4:7], 0\n"
    "s_xor_b32 s58, s58, 0xe00\n"
    "v_fma_f32 v52, v52, s52, v85\n"
    "v_fma_f32 v54, v54, s52, v85\n"
    "v_mfma_f32_16x16x32_bf16 a[232:235], a[136:139], v[8:11], 0\n"
    "s_add_u32 s59, s59, 0x800\n"
    "ds_read_b128 a[136:139], v88 offset:1024\n"
    "v_fma_f32 v56, v56, s52, v86\n"
    "v_mfma_f32_16x16x32_bf16 a[236:239], a[140:143], v[8:11], 0\n"
    "ds_read_b128 a[140:143], v89 offset:1024\n"
    "v_fma_f32 v58, v58, s52, v86\n"
    "v_mfma_f32_16x16x32_bf16 v[144:147], a[144:147], v[0:3], 0\n"
    "v_exp_f32 v52, v52\n"
    "v_mfma_f32_16x16x32_bf16 v[148:151], a[148:151], v[0:3], 0\n"
    "v_exp_f32 v56, v56\n"
    "v_mfma_f32_16x16x32_bf16 v[208:211], a[144:147], v[4:7], 0\n"
    "v_exp_f32 v54, v54\n"
    "v_mfma_f32_16x16x32_bf16 v[212:215], a[148:151], v[4:7], 0\n"
    "v_exp_f32 v58, v58\n"
    "v_mfma_f32_16x16x32_bf16 a[240:243], a[144:147], v[8:11], 0\n"
    "ds_read_b128 a[144:147], v88 offset:2048\n"
    "v_fma_f32 v49, v49, s52, v84\n"
    "v_mfma_f32_16x16x32_bf16 a[244:247], a[148:151], v[8:11], 0\n"
    "ds_read_b128 a[148:151], v89 offset:2048\n"
    "v_fma_f32 v51, v51, s52, v84\n"
    "v_mfma_f32_16x16x32_bf16 v[152:155], a[152:155], v[0:3], 0\n"
    "v_fma_f32 v53, v53, s52, v85\n"
    "v_fma_f32 v55, v55, s52, v85\n"
    "v_fma_f32 v57, v57, s52, v86\n"
    "v_fma_f32 v59, v59, s52, v86\n"
    "v_mfma_f32_16x16x32_bf16 v[156:159], a[156:159], v[0:3], 0\n"
    "v_exp_f32 v49, v49\n"
    "v_add_f32 v80, v80, v48\n"
    "v_exp_f32 v53, v53\n"
    "v_add_f32 v81, v81, v52\n"
    "v_exp_f32 v57, v57\n"
    "v_add_f32 v82, v82, v56\n"
    "v_mfma_f32_16x16x32_bf16 v[216:219], a[152:155], v[4:7], 0\n"
    "v_add_f32 v80, v80, v49\n"
    "v_exp_f32 v51, v51\n"
    "v_add_f32 v81, v81, v53\n"
    "v_exp_f32 v55, v55\n"
    "v_add_f32 v82, v82, v57\n"
    "v_exp_f32 v59, v59\n"
    "v_mfma_f32_16x16x32_bf16 v[220:223], a[156:159], v[4:7], 0\n"
    "v_add_f32 v80, v80, v50\n"
    "v_add_f32 v81, v81, v54\n"
    "v_add_f32 v82, v82, v58\n"
    "v_mfma_f32_16x16x32_bf16 a[248:251], a[152:155], v[8:11], 0\n"
    "ds_read_b128 a[152:155], v88 offset:3072\n"
    "v_add_f32 v80, v80, v51\n"
    "v_add_f32 v81, v81, v55\n"
    "v_add_f32 v82, v82, v59\n"
    "v_mfma_f32_16x16x32_bf16 a[252:255], a[156:159], v[8:11], 0\n"
    "s_waitcnt lgkmcnt(5)\n"
    "ds_read_b128 a[156:159], v89 offset:3072\n"
    "s_branch LQ3_%=\n"
    ".p2align 5\n"
    "LSP4_%=:\n"
    "s_waitcnt lgkmcnt(12)\n"
    "v_mfma_f32_16x16x32_bf16 v[0:3], a[128:131], a[0:3], 0\n"
    "ds_write_b128 v94, v[96:99] offset:0\n"
    "v_mfma_f32_16x16x32_bf16 v[0:3], a[132:135], a[4:7], v[0:3]\n"
    "s_nop 0\n"
    "v_mfma_f32_16x16x32_bf16 v[4:7], a[128:131], a[32:35], 0\n"
    "v_mfma_f32_16x16x32_bf16 v[4:7], a[132:135], a[36:39], v[4:7]\n"
    "ds_write_b128 v95, v[104:107] offset:0\n"
    "v_mfma_f32_16x16x32_bf16 v[8:11], a[128:131], a[64:67], 0\n"
    "v_mfma_f32_16x16x32_bf16 v[8:11], a[132:135], a[68:71], v[8:11]\n"
    "v_mfma_f32_16x16x32_bf16 v[12:15], a[128:131], a[96:99], 0\n"
    "ds_read_b128 a[128:131], v88 offset:8192\n"
    "v_mfma_f32_16x16x32_bf16 v[12:15], a[132:135], a[100:103], v[12:15]\n"
    "s_waitcnt lgkmcnt(11)\n"
    "ds_read_b128 a[132:135], v89 offset:8192\n"
    "v_mfma_f32_16x16x32_bf16 v[0:3], a[136:139], a[8:11], v[0:3]\n"
    "s_nop 0\n"
    "ds_write_b128 v94, v[112:115] offset:1024\n"
    "v_mfma_f32_16x16x32_bf16 v[0:3], a[140:143], a[12:15], v[0:3]\n"
    "v_mfma_f32_16x16x32_bf16 v[4:7], a[136:139], a[40:43], v[4:7]\n"
    "v_mfma_f32_16x16x32_bf16 v[4:7], a[140:143], a[44:47], v[4:7]\n"
    "ds_write_b128 v95, v[120:123] offset:1024\n"
    "v_mfma_f32_16x16x32_bf16 v[8:11], a[136:139], a[72:75], v[8:11]\n"
    "v_mfma_f32_16x16x32_bf16 v[8:11], a[140:143], a[76:79], v[8:11]\n"
    "v_mfma_f32_16x16x32_bf16 v[12:15], a[136:139], a[104:107], v[12:15]\n"
    "ds_read_b128 a[136:139], v88 offset:9216\n"
    "v_mfma_f32_16x16x32_bf16 v[12:15], a[140:143], a[108:111], v[12:15]\n"
    "s_waitcnt lgkmcnt(11)\n"
    "ds_read_b128 a[140:143], v89 offset:9216\n"
    "v_mfma_f32_16x16x32_bf16 v[0:3], a[144:147], a[16:19], v[0:3]\n"
    "s_nop 0\n"
    "ds_write_b128 v94, v[128:131] offset:2048\n"
    "v_mfma_f32_16x16x32_bf16 v[0:3], a[148:151], a[20:23], v[0:3]\n"
    "v_mfma_f32_16x16x32_bf16 v[4:7], a[144:147], a[48:51], v[4:7]\n"
    "v_mfma_f32_16x16x32_bf16 v[4:7], a[148:151], a[52:55], v[4:7]\n"
    "ds_write_b128 v95, v[136:139] offset:2048\n"
    "v_mfma_f32_16x16x32_bf16 v[8:11], a[144:147], a[80:83], v[8:11]\n"
    "v_mfma_f32_16x16x32_bf16 v[8:11], a[148:151], a[84:87], v[8:11]\n"
    "v_mfma_f32_16x16x32_bf16 v[12:15], a[144:147], a[112:115], v[12:15]\n"
    "s_nop 0\n"
    "ds_read_b128 a[144:147], v88 offset:10240\n"
    "v_mfma_f32_16x16x32_bf16 v[12:15], a[148:151], a[116:119], v[12:15]\n"
    "s_waitcnt lgkmcnt(11)\n"
    "ds_read_b128 a[148:151], v89 offset:10240\n"
    "v_mfma_f32_16x16x32_bf16 v[0:3], a[152:155], a[24:27], v[0:3]\n"
    "ds_write_b128 v94, v[144:147] offset:3072\n"
    "v_mfma_f32_16x16x32_bf16 v[0:3], a[156:159], a[28:31], v[0:3]\n"
    "v_mfma_f32_16x16x32_bf16 v[4:7], a[152:155], a[56:59], v[4:7]\n"
    "v_mfma_f32_16x16x32_bf16 v[4:7], a[156:159], a[60:63], v[4:7]\n"
    "s_nop 0\n"
    "ds_write_b128 v95, v[152:155] offset:3072\n"
    "v_mfma_f32_16x16x32_bf16 v[8:11], a[152:155], a[88:91], v[8:11]\n"
    "s_nop 0\n"
    "v_mfma_f32_16x16x32_bf16 v[8:11], a[156:159], a[92:95], v[8:11]\n"
    "ds_read_b128 v[48:51], v93 offset:0\n"
    "ds_read_b128 v[52:55], v93 offset:1024\n"
    "v_mfma_f32_16x16x32_bf16 v[12:15], a[152:155], a[120:123], v[12:15]\n"
    "s_nop 0\n"
    "ds_read_b128 a[152:155], v88 offset:11264\n"
    "ds_read_b128 v[56:59], v93 offset:2048\n"
    "v_mfma_f32_16x16x32_bf16 v[12:15], a[156:159], a[124:127], v[12:15]\n"
    "s_waitcnt lgkmcnt(14)\n"
    "ds_read_b128 a[156:159], v89 offset:11264\n"
    "ds_read_b128 v[60:63], v93 offset:3072\n"
    "v_mfma_f32_16x16x32_bf16 v[16:19], a[128:131], a[0:3], 0\n"
    "ds_write_b128 v94, v[160:163] offset:8192\n"
    "v_mfma_f32_16x16x32_bf16 v[16:19], a[132:135], a[4:7], v[16:19]\n"
    "v_max3_f32 v64, v0, v1, v2\n"
    "v_max_f32 v64, v64, v3\n"
    "v_mfma_f32_16x16x32_bf16 v[20:23], a[128:131], a[32:35], 0\n"
    "v_mov_b32 v65, v64\n"
    "v_max3_f32 v66, v4, v5, v6\n"
    "v_mfma_f32_16x16x32_bf16 v[20:23], a[132:135], a[36:39], v[20:23]\n"
    "ds_write_b128 v95, v[168:171] offset:8192\n"
    "v_mfma_f32_16x16x32_bf16 v[24:27], a[128:131], a[64:67], 0\n"
    "v_permlane32_swap_b32 v64, v65\n"
    "v_mfma_f32_16x16x32_bf16 v[24:27], a[132:135], a[68:71], v[24:27]\n"
    "s_waitcnt lgkmcnt(2)\n"
    "buffer_store_dwordx4 v[48:51], v92, s[48:51], s98 offen offset:0 nt\n"
    "v_mfma_f32_16x16x32_bf16 v[28:31], a[128:131], a[96:99], 0\n"
    "ds_read_b128 a[128:131], v88 offset:16384\n"
    "v_max_f32 v66, v66, v7\n"
    "v_mfma_f32_16x16x32_bf16 v[28:31], a[132:135], a[100:103], v[28:31]\n"
    "s_waitcnt lgkmcnt(15)\n"
    "ds_read_b128 a[132:135], v89 offset:16384\n"
    "v_mov_b32_e64 v67, v66\n"
    "v_mfma_f32_16x16x32_bf16 v[16:19], a[136:139], a[8:11], v[16:19]\n"
    "ds_write_b128 v94, v[176:179] offset:9216\n"
    "v_mfma_f32_16x16x32_bf16 v[16:19], a[140:143], a[12:15], v[16:19]\n"
    "buffer_store_dwordx4 v[52:55], v92, s[48:51], s98 offen offset:128 nt\n"
    "v_mfma_f32_16x16x32_bf16 v[20:23], a[136:139], a[40:43], v[20:23]\n"
    "v_max_f32 v64, v64, v65\n"
    "v_mov_b32 v65, v64\n"
    "v_max3_f32 v68, v8, v9, v10\n"
    "v_max_f32 v68, v68, v11\n"
    "v_mfma_f32_16x16x32_bf16 v[20:23], a[140:143], a[44:47], v[20:23]\n"
    "ds_write_b128 v95, v[184:187] offset:9216\n"
    "v_permlane32_swap_b32 v66, v67\n"
    "v_mov_b32_e64 v69, v68\n"
    "v_mfma_f32_16x16x32_bf16 v[24:27], a[136:139], a[72:75], v[24:27]\n"
    "buffer_store_dwordx4 v[56:59], v92, s[48:51], s98 offen offset:256 nt\n"
    "v_permlane16_swap_b32 v64, v65\n"
    "v_max_f32 v66, v66, v67\n"
    "v_mov_b32_e64 v67, v66\n"
    "v_max3_f32 v70, v12, v13, v14\n"
    "v_max_f32 v70, v70, v15\n"
    "v_mfma_f32_16x16x32_bf16 v[24:27], a[140:143], a[76:79], v[24:27]\n"
    "v_max_f32 v64, v64, v65\n"
    "v_permlane32_swap_b32 v68, v69\n"
    "v_mov_b32 v71, v70\n"
    "v_mfma_f32_16x16x32_bf16 v[28:31], a[136:139], a[104:107], v[28:31]\n"
    "ds_read_b128 a[136:139], v88 offset:17408\n"
    "v_mul_f32 v84, s81, v64\n"
    "v_permlane16_swap_b32 v66, v67\n"
    "v_max_f32 v68, v68, v69\n"
    "v_mov_b32 v69, v68\n"
    "v_mfma_f32_16x16x32_bf16 v[28:31], a[140:143], a[108:111], v[28:31]\n"
    "s_waitcnt lgkmcnt(15)\n"
    "ds_read_b128 a[140:143], v89 offset:17408\n"
    "v_max_f32_e64 v66, v66, v67\n"
    "v_permlane32_swap_b32 v70, v71\n"
    "v_mfma_f32_16x16x32_bf16 v[16:19], a[144:147], a[16:19], v[16:19]\n"
    "ds_write_b128 v94, v[192:195] offset:10240\n"
    "v_mul_f32 v85, s81, v66\n"
    "v_permlane16_swap_b32 v68, v69\n"
    "v_max_f32_e64 v70, v70, v71\n"
    "v_mov_b32_e64 v71, v70\n"
    "v_mfma_f32_16x16x32_bf16 v[16:19], a[148:151], a[20:23], v[16:19]\n"
    "buffer_store_dwordx4 v[60:63], v92, s[48:51], s98 offen offset:384 nt\n"
    "v_max_f32_e64 v68, v68, v69\n"
    "v_mfma_f32_16x16x32_bf16 v[20:23], a[144:147], a[48:51], v[20:23]\n"
    "s_add_u32 s98, s98, 0x1000\n"
    "v_mul_f32 v86, s81, v68\n"
    "v_permlane16_swap_b32 v70, v71\n"
    "v_mfma_f32_16x16x32_bf16 v[20:23], a[148:151], a[52:55], v[20:23]\n"
    "ds_write_b128 v95, v[200:203] offset:10240\n"
    "v_max_f32_e64 v70, v70, v71\n"
    "v_mfma_f32_16x16x32_bf16 v[24:27], a[144:147], a[80:83], v[24:27]\n"
    "v_mul_f32_e64 v87, s81, v70\n"
    "v_fma_f32 v0, v0, s52, v84\n"
    "v_mfma_f32_16x16x32_bf16 v[24:27], a[148:151], a[84:87], v[24:27]\n"
    "v_exp_f32_e64 v0, v0\n"
    "v_mfma_f32_16x16x32_bf16 v[28:31], a[144:147], a[112:115], v[28:31]\n"
    "s_nop 0\n"
    "ds_read_b128 a[144:147], v88 offset:18432\n"
    "v_fma_f32 v2, v2, s52, v84\n"
    "v_mfma_f32_16x16x32_bf16 v[28:31], a[148:151], a[116:119], v[28:31]\n"
    "s_waitcnt lgkmcnt(12)\n"
    "ds_read_b128 a[148:151], v89 offset:18432\n"
    "v_fma_f32 v4, v4, s52, v85\n"
    "v_mfma_f32_16x16x32_bf16 v[16:19], a[152:155], a[24:27], v[16:19]\n"
    "ds_write_b128 v94, v[208:211] offset:11264\n"
    "v_mfma_f32_16x16x32_bf16 v[16:19], a[156:159], a[28:31], v[16:19]\n"
    "v_exp_f32 v2, v2\n"
    "v_mfma_f32_16x16x32_bf16 v[20:23], a[152:155], a[56:59], v[20:23]\n"
    "v_exp_f32 v4, v4\n"
    "v_mfma_f32_16x16x32_bf16 v[20:23], a[156:159], a[60:63], v[20:23]\n"
    "s_nop 0\n"
    "ds_write_b128 v95, v[216:219] offset:11264\n"
    "v_mfma_f32_16x16x32_bf16 v[24:27], a[152:155], a[88:91], v[24:27]\n"
    "s_mov_b32 m0, s63\n"
    "v_fma_f32 v6, v6, s52, v85\n"
    "v_fma_f32 v8, v8, s52, v86\n"
    "v_mfma_f32_16x16x32_bf16 v[24:27], a[156:159], a[92:95], v[24:27]\n"
    "buffer_load_dwordx4 v92, s[36:39], s53 offen offset:0 lds\n"
    "v_mfma_f32_16x16x32_bf16 v[28:31], a[152:155], a[120:123], v[28:31]\n"
    "s_add_u32 m0, m0, s97\n"
    "ds_read_b128 a[152:155], v88 offset:19456\n"
    "v_fma_f32 v10, v10, s52, v86\n"
    "v_mfma_f32_16x16x32_bf16 v[28:31], a[156:159], a[124:127], v[28:31]\n"
    "s_waitcnt lgkmcnt(11)\n"
    "ds_read_b128 a[156:159], v89 offset:19456\n"
    "v_fma_f32 v12, v12, s52, v87\n"
    "v_mfma_f32_16x16x32_bf16 v[32:35], a[128:131], a[0:3], 0\n"
    "v_exp_f32_e64 v6, v6\n"
    "v_mfma_f32_16x16x32_bf16 v[32:35], a[132:135], a[4:7], v[32:35]\n"
    "buffer_load_dwordx4 v92, s[36:39], s53 offen offset:128 lds\n"
    "v_mfma_f32_16x16x32_bf16 v[36:39], a[128:131], a[32:35], 0\n"
    "s_add_u32 m0, m0, s97\n"
    "v_exp_f32 v8, v8\n"
    "v_mfma_f32_16x16x32_bf16 v[36:39], a[132:135], a[36:39], v[36:39]\n"
    "v_exp_f32 v10, v10\n"
    "v_mfma_f32_16x16x32_bf16 v[40:43], a[128:131], a[64:67], 0\n"
    "v_exp_f32 v12, v12\n"
    "v_mfma_f32_16x16x32_bf16 v[40:43], a[132:135], a[68:71], v[40:43]\n"
    "buffer_load_dwordx4 v92, s[36:39], s53 offen offset:256 lds\n"
    "v_mfma_f32_16x16x32_bf16 v[44:47], a[128:131], a[96:99], 0\n"
    "s_add_u32 m0, m0, 896\n"
    "ds_read_b128 a[128:131], v88 offset:24576\n"
    "v_fma_f32 v14, v14, s52, v87\n"
    "v_mfma_f32_16x16x32_bf16 v[44:47], a[132:135], a[100:103], v[44:47]\n"
    "s_waitcnt lgkmcnt(9)\n"
    "ds_read_b128 a[132:135], v89 offset:24576\n"
    "s_nop 0\n"
    "v_fma_f32 v1, v1, s52, v84\n"
    "v_mfma_f32_16x16x32_bf16 v[32:35], a[136:139], a[8:11], v[32:35]\n"
    "v_exp_f32_e64 v1, v1\n"
    "v_mfma_f32_16x16x32_bf16 v[32:35], a[140:143], a[12:15], v[32:35]\n"
    "buffer_load_dwordx4 v92, s[36:39], s53 offen offset:384 lds\n"
    "v_mfma_f32_16x16x32_bf16 v[36:39], a[136:139], a[40:43], v[36:39]\n"
    "s_add_u32 m0, m0, 1408\n"
    "v_exp_f32_e64 v14, v14\n"
    "v_mfma_f32_16x16x32_bf16 v[36:39], a[140:143], a[44:47], v[36:39]\n"
    "v_fma_f32 v3, v3, s52, v84\n"
    "v_fma_f32 v5, v5, s52, v85\n"
    "v_mfma_f32_16x16x32_bf16 v[40:43], a[136:139], a[72:75], v[40:43]\n"
    "v_exp_f32_e64 v5, v5\n"
    "v_mfma_f32_16x16x32_bf16 v[40:43], a[140:143], a[76:79], v[40:43]\n"
    "buffer_load_dwordx4 v92, s[36:39], s54 offen offset:0 lds\n"
    "v_mfma_f32_16x16x32_bf16 v[44:47], a[136:139], a[104:107], v[44:47]\n"
    "s_add_u32 m0, m0, s97\n"
    "ds_read_b128 a[136:139], v88 offset:25600\n"
    "v_fma_f32 v7, v7, s52, v85\n"
    "v_mfma_f32_16x16x32_bf16 v[44:47], a[140:143], a[108:111], v[44:47]\n"
    "s_waitcnt lgkmcnt(7)\n"
    "ds_read_b128 a[140:143], v89 offset:25600\n"
    "v_fma_f32 v9, v9, s52, v86\n"
    "v_mfma_f32_16x16x32_bf16 v[32:35], a[144:147], a[16:19], v[32:35]\n"
    "v_exp_f32_e64 v9, v9\n"
    "v_mfma_f32_16x16x32_bf16 v[32:35], a[148:151], a[20:23], v[32:35]\n"
    "buffer_load_dwordx4 v92, s[36:39], s54 offen offset:128 lds\n"
    "v_mfma_f32_16x16x32_bf16 v[36:39], a[144:147], a[48:51], v[36:39]\n"
    "s_add_u32 m0, m0, s97\n"
    "v_exp_f32 v3, v3\n"
    "v_mfma_f32_16x16x32_bf16 v[36:39], a[148:151], a[52:55], v[36:39]\n"
    "v_exp_f32_e64 v7, v7\n"
    "v_mfma_f32_16x16x32_bf16 v[40:43], a[144:147], a[80:83], v[40:43]\n"
    "v_fma_f32 v11, v11, s52, v86\n"
    "v_fma_f32 v13, v13, s52, v87\n"
    "v_mfma_f32_16x16x32_bf16 v[40:43], a[148:151], a[84:87], v[40:43]\n"
    "buffer_load_dwordx4 v92, s[36:39], s54 offen offset:256 lds\n"
    "v_mfma_f32_16x16x32_bf16 v[44:47], a[144:147], a[112:115], v[44:47]\n"
    "s_add_u32 m0, m0, s97\n"
    "ds_read_b128 a[144:147], v88 offset:26624\n"
    "v_fma_f32 v15, v15, s52, v87\n"
    "v_mfma_f32_16x16x32_bf16 v[44:47], a[148:151], a[116:119], v[44:47]\n"
    "s_waitcnt lgkmcnt(5)\n"
    "ds_read_b128 a[148:151], v89 offset:26624\n"
    "v_add_f32 v80, v0, v1\n"
    "v_mfma_f32_16x16x32_bf16 v[32:35], a[152:155], a[24:27], v[32:35]\n"
    "v_exp_f32 v13, v13\n"
    "v_mfma_f32_16x16x32_bf16 v[32:35], a[156:159], a[28:31], v[32:35]\n"
    "buffer_load_dwordx4 v92, s[36:39], s54 offen offset:384 lds\n"
    "v_mfma_f32_16x16x32_bf16 v[36:39], a[152:155], a[56:59], v[36:39]\n"
    "s_add_u32 s53, s53, s96\n"
    "v_exp_f32 v11, v11\n"
    "v_mfma_f32_16x16x32_bf16 v[36:39], a[156:159], a[60:63], v[36:39]\n"
    "s_add_u32 s54, s54, s96\n"
    "v_exp_f32 v15, v15\n"
    "v_mfma_f32_16x16x32_bf16 v[40:43], a[152:155], a[88:91], v[40:43]\n"
    "v_add_f32 v81, v4, v5\n"
    "v_add_f32 v82, v8, v9\n"
    "v_mfma_f32_16x16x32_bf16 v[40:43], a[156:159], a[92:95], v[40:43]\n"
    "v_add_f32 v83, v12, v13\n"
    "v_add_f32 v80, v80, v2\n"
    "v_mfma_f32_16x16x32_bf16 v[44:47], a[152:155], a[120:123], v[44:47]\n"
    "ds_read_b128 a[152:155], v88 offset:27648\n"
    "v_add_f32_e64 v81, v81, v6\n"
    "v_mfma_f32_16x16x32_bf16 v[44:47], a[156:159], a[124:127], v[44:47]\n"
    "s_waitcnt lgkmcnt(5)\n"
    "ds_read_b128 a[156:159], v89 offset:27648\n"
    "v_add_f32 v82, v82, v10\n"
    "v_mfma_f32_16x16x32_bf16 v[48:51], a[128:131], a[0:3], 0\n"
    "v_add_f32 v83, v83, v14\n"
    "v_add_f32 v80, v80, v3\n"
    "v_mfma_f32_16x16x32_bf16 v[48:51], a[132:135], a[4:7], v[48:51]\n"
    "v_add_f32 v81, v81, v7\n"
    "v_add_f32 v82, v82, v11\n"
    "v_mfma_f32_16x16x32_bf16 v[52:55], a[128:131], a[32:35], 0\n"
    "v_add_f32_e64 v83, v83, v15\n"
    "v_cvt_pk_bf16_f32 v0, v0, v1\n"
    "v_mfma_f32_16x16x32_bf16 v[52:55], a[132:135], a[36:39], v[52:55]\n"
    "v_cvt_pk_bf16_f32 v1, v2, v3\n"
    "v_cvt_pk_bf16_f32 v4, v4, v5\n"
    "v_mfma_f32_16x16x32_bf16 v[56:59], a[128:131], a[64:67], 0\n"
    "v_cvt_pk_bf16_f32 v5, v6, v7\n"
    "v_cvt_pk_bf16_f32 v8, v8, v9\n"
    "v_mfma_f32_16x16x32_bf16 v[56:59], a[132:135], a[68:71], v[56:59]\n"
    "v_cvt_pk_bf16_f32 v9, v10, v11\n"
    "v_cvt_pk_bf16_f32 v12, v12, v13\n"
    "v_mfma_f32_16x16x32_bf16 v[60:63], a[128:131], a[96:99], 0\n"
    "s_waitcnt lgkmcnt(0)\n"
    "v_cvt_pk_bf16_f32 v13, v14, v15\n"
    "v_fma_f32 v16, v16, s52, v84\n"
    "v_mfma_f32_16x16x32_bf16 v[60:63], a[132:135], a[100:103], v[60:63]\n"
    "s_barrier\n"
    "v_mfma_f32_16x16x32_bf16 v[48:51], a[136:139], a[8:11], v[48:51]\n"
    "s_mov_b32 m0, s60\n"
    "ds_read_b64_tr_b16 a[128:129], v90 offset:16384\n"
    "ds_read_b64_tr_b16 a[130:131], v90 offset:24576\n"
    "s_nop 0\n"
    "v_mfma_f32_16x16x32_bf16 v[48:51], a[140:143], a[12:15], v[48:51]\n"
    "ds_read_b64_tr_b16 a[132:133], v90 offset:16392\n"
    "ds_read_b64_tr_b16 a[134:135], v90 offset:24584\n"
    "v_mfma_f32_16x16x32_bf16 v[52:55], a[136:139], a[40:43], v[52:55]\n"
    "buffer_load_dwordx4 v92, s[40:43], s55 offen offset:0 lds\n"
    "v_mfma_f32_16x16x32_bf16 v[52:55], a[140:143], a[44:47], v[52:55]\n"
    "s_add_u32 m0, m0, s97\n"
    "v_exp_f32 v16, v16\n"
    "v_mfma_f32_16x16x32_bf16 v[56:59], a[136:139], a[72:75], v[56:59]\n"
    "v_fma_f32 v18, v18, s52, v84\n"
    "v_fma_f32 v20, v20, s52, v85\n"
    "v_mfma_f32_16x16x32_bf16 v[56:59], a[140:143], a[76:79], v[56:59]\n"
    "v_exp_f32_e64 v20, v20\n"
    "v_mfma_f32_16x16x32_bf16 v[60:63], a[136:139], a[104:107], v[60:63]\n"
    "ds_read_b64_tr_b16 a[136:137], v91 offset:16384\n"
    "ds_read_b64_tr_b16 a[138:139], v91 offset:24576\n"
    "v_mfma_f32_16x16x32_bf16 v[60:63], a[140:143], a[108:111], v[60:63]\n"
    "ds_read_b64_tr_b16 a[140:141], v91 offset:16392\n"
    "ds_read_b64_tr_b16 a[142:143], v91 offset:24584\n"
    "v_mfma_f32_16x16x32_bf16 v[48:51], a[144:147], a[16:19], v[48:51]\n"
    "buffer_load_dwordx4 v92, s[40:43], s55 offen offset:128 lds\n"
    "v_mfma_f32_16x16x32_bf16 v[48:51], a[148:151], a[20:23], v[48:51]\n"
    "s_add_u32 m0, m0, s97\n"
    "v_exp_f32 v18, v18\n"
    "v_mfma_f32_16x16x32_bf16 v[52:55], a[144:147], a[48:51], v[52:55]\n"
    "v_fma_f32 v22, v22, s52, v85\n"
    "v_fma_f32 v24, v24, s52, v86\n"
    "v_mfma_f32_16x16x32_bf16 v[52:55], a[148:151], a[52:55], v[52:55]\n"
    "v_exp_f32_e64 v24, v24\n"
    "v_mfma_f32_16x16x32_bf16 v[56:59], a[144:147], a[80:83], v[56:59]\n"
    "buffer_load_dwordx4 v92, s[40:43], s55 offen offset:256 lds\n"
    "v_mfma_f32_16x16x32_bf16 v[56:59], a[148:151], a[84:87], v[56:59]\n"
    "s_add_u32 m0, m0, s97\n"
    "v_exp_f32 v22, v22\n"
    "v_mfma_f32_16x16x32_bf16 v[60:63], a[144:147], a[112:115], v[60:63]\n"
    "ds_read_b64_tr_b16 a[144:145], v90 offset:17408\n"
    "ds_read_b64_tr_b16 a[146:147], v90 offset:25600\n"
    "v_mfma_f32_16x16x32_bf16 v[60:63], a[148:151], a[116:119], v[60:63]\n"
    "ds_read_b64_tr_b16 a[148:149], v90 offset:17416\n"
    "ds_read_b64_tr_b16 a[150:151], v90 offset:25608\n"
    "v_mfma_f32_16x16x32_bf16 v[48:51], a[152:155], a[24:27], v[48:51]\n"
    "buffer_load_dwordx4 v92, s[40:43], s55 offen offset:384 lds\n"
    "v_fma_f32 v26, v26, s52, v86\n"
    "v_fma_f32 v28, v28, s52, v87\n"
    "v_fma_f32 v30, v30, s52, v87\n"
    "v_mfma_f32_16x16x32_bf16 v[48:51], a[156:159], a[28:31], v[48:51]\n"
    "s_add_u32 m0, m0, 1408\n"
    "v_fma_f32 v17, v17, s52, v84\n"
    "v_fma_f32 v19, v19, s52, v84\n"
    "v_fma_f32 v21, v21, s52, v85\n"
    "v_fma_f32 v23, v23, s52, v85\n"
    "v_fma_f32 v25, v25, s52, v86\n"
    "v_fma_f32 v27, v27, s52, v86\n"
    "v_fma_f32 v29, v29, s52, v87\n"
    "v_exp_f32_e64 v28, v28\n"
    "v_fma_f32 v31, v31, s52, v87\n"
    "v_mfma_f32_16x16x32_bf16 v[52:55], a[152:155], a[56:59], v[52:55]\n"
    "v_exp_f32 v17, v17\n"
    "v_add_f32 v80, v80, v16\n"
    "v_exp_f32 v21, v21\n"
    "v_add_f32 v81, v81, v20\n"
    "v_exp_f32 v25, v25\n"
    "v_add_f32 v82, v82, v24\n"
    "v_exp_f32 v26, v26\n"
    "v_exp_f32 v29, v29\n"
    "v_add_f32 v83, v83, v28\n"
    "v_exp_f32 v30, v30\n"
    "v_mfma_f32_16x16x32_bf16 v[52:55], a[156:159], a[60:63], v[52:55]\n"
    "v_add_f32 v80, v80, v17\n"
    "v_exp_f32 v19, v19\n"
    "v_add_f32 v81, v81, v21\n"
    "v_exp_f32 v23, v23\n"
    "v_add_f32 v82, v82, v25\n"
    "v_exp_f32 v27, v27\n"
    "v_add_f32 v83, v83, v29\n"
    "v_exp_f32 v31, v31\n"
    "v_mfma_f32_16x16x32_bf16 v[56:59], a[152:155], a[88:91], v[56:59]\n"
    "buffer_load_dwordx4 v92, s[40:43], s56 offen offset:0 lds\n"
    "v_add_f32 v80, v80, v18\n"
    "v_add_f32 v81, v81, v22\n"
    "v_add_f32 v82, v82, v26\n"
    "v_add_f32 v83, v83, v30\n"
    "v_mfma_f32_16x16x32_bf16 v[56:59], a[156:159], a[92:95], v[56:59]\n"
    "s_add_u32 m0, m0, s97\n"
    "v_cvt_pk_bf16_f32 v2, v16, v17\n"
    "v_add_f32 v80, v80, v19\n"
    "v_cvt_pk_bf16_f32 v3, v18, v19\n"
    "v_add_f32_e64 v81, v81, v23\n"
    "v_add_f32_e64 v82, v82, v27\n"
    "v_add_f32_e64 v83, v83, v31\n"
    "v_mfma_f32_16x16x32_bf16 v[60:63], a[152:155], a[120:123], v[60:63]\n"
    "s_nop 0\n"
    "ds_read_b64_tr_b16 a[152:153], v91 offset:17408\n"
    "ds_read_b64_tr_b16 a[154:155], v91 offset:25600\n"
    "v_mfma_f32_16x16x32_bf16 v[60:63], a[156:159], a[124:127], v[60:63]\n"
    "s_waitcnt lgkmcnt(10)\n"
    "ds_read_b64_tr_b16 a[156:157], v91 offset:17416\n"
    "ds_read_b64_tr_b16 a[158:159], v91 offset:25608\n"
    "v_cvt_pk_bf16_f32 v6, v20, v21\n"
    "v_cvt_pk_bf16_f32 v7, v22, v23\n"
    "v_mfma_f32_16x16x32_bf16 v[96:99], a[128:131], v[0:3], 0\n"
    "buffer_load_dwordx4 v92, s[40:43], s56 offen offset:128 lds\n"
    "v_mfma_f32_16x16x32_bf16 v[100:103], a[132:135], v[0:3], 0\n"
    "s_add_u32 m0, m0, 896\n"
    "v_cvt_pk_bf16_f32 v10, v24, v25\n"
    "v_cvt_pk_bf16_f32 v11, v26, v27\n"
    "v_mfma_f32_16x16x32_bf16 v[160:163], a[128:131], v[4:7], 0\n"
    "v_cvt_pk_bf16_f32 v14, v28, v29\n"
    "v_cvt_pk_bf16_f32 v15, v30, v31\n"
    "v_mfma_f32_16x16x32_bf16 v[164:167], a[132:135], v[4:7], 0\n"
    "v_fma_f32 v32, v32, s52, v84\n"
    "v_fma_f32 v34, v34, s52, v84\n"
    "v_mfma_f32_16x16x32_bf16 v[224:227], a[128:131], v[8:11], 0\n"
    "buffer_load_dwordx4 v92, s[40:43], s56 offen offset:256 lds\n"
    "v_mfma_f32_16x16x32_bf16 v[228:231], a[132:135], v[8:11], 0\n"
    "s_add_u32 m0, m0, s97\n"
    "v_exp_f32 v32, v32\n"
    "v_mfma_f32_16x16x32_bf16 a[160:163], a[128:131], v[12:15], 0\n"
    "ds_read_b64_tr_b16 a[128:129], v90 offset:18432\n"
    "ds_read_b64_tr_b16 a[130:131], v90 offset:26624\n"
    "v_mfma_f32_16x16x32_bf16 a[164:167], a[132:135], v[12:15], 0\n"
    "s_waitcnt lgkmcnt(10)\n"
    "ds_read_b64_tr_b16 a[132:133], v90 offset:18440\n"
    "ds_read_b64_tr_b16 a[134:135], v90 offset:26632\n"
    "v_mfma_f32_16x16x32_bf16 v[104:107], a[136:139], v[0:3], 0\n"
    "s_nop 0\n"
    "buffer_load_dwordx4 v92, s[40:43], s56 offen offset:384 lds\n"
    "v_mfma_f32_16x16x32_bf16 v[108:111], a[140:143], v[0:3], 0\n"
    "s_add_u32 s55, s55, s96\n"
    "v_exp_f32 v34, v34\n"
    "v_mfma_f32_16x16x32_bf16 v[168:171], a[136:139], v[4:7], 0\n"
    "s_add_u32 s56, s56, s96\n"
    "v_fma_f32 v36, v36, s52, v85\n"
    "v_fma_f32 v38, v38, s52, v85\n"
    "v_mfma_f32_16x16x32_bf16 v[172:175], a[140:143], v[4:7], 0\n"
    "s_mov_b32 m0, s59\n"
    "v_exp_f32 v36, v36\n"
    "v_mfma_f32_16x16x32_bf16 v[232:235], a[136:139], v[8:11], 0\n"
    "buffer_load_dwordx4 v92, s[44:47], s57 offen offset:0 nt lds\n"
    "v_mfma_f32_16x16x32_bf16 v[236:239], a[140:143], v[8:11], 0\n"
    "s_add_u32 m0, m0, s97\n"
    "v_exp_f32_e64 v38, v38\n"
    "v_mfma_f32_16x16x32_bf16 a[168:171], a[136:139], v[12:15], 0\n"
    "ds_read_b64_tr_b16 a[136:137], v91 offset:18432\n"
    "ds_read_b64_tr_b16 a[138:139], v91 offset:26624\n"
    "v_mfma_f32_16x16x32_bf16 a[172:175], a[140:143], v[12:15], 0\n"
    "s_waitcnt lgkmcnt(10)\n"
    "ds_read_b64_tr_b16 a[140:141], v91 offset:18440\n"
    "ds_read_b64_tr_b16 a[142:143], v91 offset:26632\n"
    "v_mfma_f32_16x16x32_bf16 v[112:115], a[144:147], v[0:3], 0\n"
    "buffer_load_dwordx4 v92, s[44:47], s57 offen offset:128 nt lds\n"
    "v_mfma_f32_16x16x32_bf16 v[116:119], a[148:151], v[0:3], 0\n"
    "s_add_u32 s57, s57, s58\n"
    "v_fma_f32 v40, v40, s52, v86\n"
    "v_fma_f32 v42, v42, s52, v86\n"
    "v_mfma_f32_16x16x32_bf16 v[176:179], a[144:147], v[4:7], 0\n"
    "s_xor_b32 s58, s58, 0xe00\n"
    "v_exp_f32_e64 v40, v40\n"
    "v_mfma_f32_16x16x32_bf16 v[180:183], a[148:151], v[4:7], 0\n"
    "s_add_u32 s59, s59, 0x800\n"
    "v_exp_f32_e64 v42, v42\n"
    "v_mfma_f32_16x16x32_bf16 v[240:243], a[144:147], v[8:11], 0\n"
    "v_fma_f32 v44, v44, s52, v87\n"
    "v_fma_f32 v46, v46, s52, v87\n"
    "v_mfma_f32_16x16x32_bf16 v[244:247], a[148:151], v[8:11], 0\n"
    "v_exp_f32_e64 v44, v44\n"
    "v_mfma_f32_16x16x32_bf16 a[176:179], a[144:147], v[12:15], 0\n"
    "ds_read_b64_tr_b16 a[144:145], v90 offset:19456\n"
    "ds_read_b64_tr_b16 a[146:147], v90 offset:27648\n"
    "v_mfma_f32_16x16x32_bf16 a[180:183], a[148:151], v[12:15], 0\n"
    "s_waitcnt lgkmcnt(10)\n"
    "ds_read_b64_tr_b16 a[148:149], v90 offset:19464\n"
    "ds_read_b64_tr_b16 a[150:151], v90 offset:27656\n"
    "v_mfma_f32_16x16x32_bf16 v[120:123], a[152:155], v[0:3], 0\n"
    "v_exp_f32 v46, v46\n"
    "v_mfma_f32_16x16x32_bf16 v[124:127], a[156:159], v[0:3], 0\n"
    "v_fma_f32 v33, v33, s52, v84\n"
    "v_fma_f32 v35, v35, s52, v84\n"
    "v_mfma_f32_16x16x32_bf16 v[184:187], a[152:155], v[4:7], 0\n"
    "v_exp_f32_e64 v33, v33\n"
    "v_mfma_f32_16x16x32_bf16 v[188:191], a[156:159], v[4:7], 0\n"
    "v_fma_f32 v37, v37, s52, v85\n"
    "v_fma_f32 v39, v39, s52, v85\n"
    "v_fma_f32 v41, v41, s52, v86\n"
    "v_fma_f32 v43, v43, s52, v86\n"
    "v_fma_f32 v45, v45, s52, v87\n"
    "v_fma_f32 v47, v47, s52, v87\n"
    "v_mfma_f32_16x16x32_bf16 v[248:251], a[152:155], v[8:11], 0\n"
    "v_add_f32 v80, v80, v32\n"
    "v_exp_f32 v37, v37\n"
    "v_add_f32 v81, v81, v36\n"
    "v_exp_f32 v41, v41\n"
    "v_add_f32 v82, v82, v40\n"
    "v_exp_f32 v45, v45\n"
    "v_add_f32 v83, v83, v44\n"
    "v_mfma_f32_16x16x32_bf16 v[252:255], a[156:159], v[8:11], 0\n"
    "v_add_f32 v80, v80, v33\n"
    "v_exp_f32 v35, v35\n"
    "v_add_f32 v81, v81, v37\n"
    "v_exp_f32 v39, v39\n"
    "v_add_f32 v82, v82, v41\n"
    "v_exp_f32 v43, v43\n"
    "v_add_f32 v83, v83, v45\n"
    "v_exp_f32_e64 v47, v47\n"
    "v_mfma_f32_16x16x32_bf16 a[184:187], a[152:155], v[12:15], 0\n"
    "ds_read_b64_tr_b16 a[152:153], v91 offset:19456\n"
    "ds_read_b64_tr_b16 a[154:155], v91 offset:27648\n"
    "v_add_f32 v80, v80, v34\n"
    "v_add_f32 v81, v81, v38\n"
    "v_add_f32 v82, v82, v42\n"
    "v_add_f32 v83, v83, v46\n"
    "v_mfma_f32_16x16x32_bf16 a[188:191], a[156:159], v[12:15], 0\n"
    "s_waitcnt lgkmcnt(10)\n"
    "ds_read_b64_tr_b16 a[156:157], v91 offset:19464\n"
    "ds_read_b64_tr_b16 a[158:159], v91 offset:27656\n"
    "v_add_f32 v80, v80, v35\n"
    "v_add_f32 v81, v81, v39\n"
    "v_add_f32 v82, v82, v43\n"
    "v_add_f32 v83, v83, v47\n"
    "v_mfma_f32_16x16x32_bf16 v[128:131], a[128:131], v[0:3], 0\n"
    "v_add_u32 v88, s62, v88\n"
    "v_add_u32_e64 v89, s62, v89\n"
    "v_mfma_f32_16x16x32_bf16 v[132:135], a[132:135], v[0:3], 0\n"
    "v_fma_f32 v48, v48, s52, v84\n"
    "v_fma_f32 v50, v50, s52, v84\n"
    "v_mfma_f32_16x16x32_bf16 v[192:195], a[128:131], v[4:7], 0\n"
    "v_exp_f32_e64 v48, v48\n"
    "v_mfma_f32_16x16x32_bf16 v[196:199], a[132:135], v[4:7], 0\n"
    "v_exp_f32_e64 v50, v50\n"
    "v_mfma_f32_16x16x32_bf16 a[224:227], a[128:131], v[8:11], 0\n"
    "v_fma_f32 v52, v52, s52, v85\n"
    "v_fma_f32 v54, v54, s52, v85\n"
    "v_mfma_f32_16x16x32_bf16 a[228:231], a[132:135], v[8:11], 0\n"
    "v_exp_f32 v52, v52\n"
    "v_mfma_f32_16x16x32_bf16 a[192:195], a[128:131], v[12:15], 0\n"
    "s_waitcnt vmcnt(10) lgkmcnt(0)\n"
    "v_exp_f32 v54, v54\n"
    "v_mfma_f32_16x16x32_bf16 a[196:199], a[132:135], v[12:15], 0\n"
    "s_barrier\n"
    "v_mfma_f32_16x16x32_bf16 v[136:139], a[136:139], v[0:3], 0\n"
    "ds_read_b128 a[128:131], v88 offset:0\n"
    "ds_read_b128 a[132:135], v89 offset:0\n"
    "v_mfma_f32_16x16x32_bf16 v[140:143], a[140:143], v[0:3], 0\n"
    "v_fma_f32 v56, v56, s52, v86\n"
    "v_fma_f32 v58, v58, s52, v86\n"
    "v_mfma_f32_16x16x32_bf16 v[200:203], a[136:139], v[4:7], 0\n"
    "v_exp_f32 v56, v56\n"
    "v_mfma_f32_16x16x32_bf16 v[204:207], a[140:143], v[4:7], 0\n"
    "v_exp_f32 v58, v58\n"
    "v_mfma_f32_16x16x32_bf16 a[232:235], a[136:139], v[8:11], 0\n"
    "v_fma_f32 v60, v60, s52, v87\n"
    "v_fma_f32 v62, v62, s52, v87\n"
    "v_mfma_f32_16x16x32_bf16 a[236:239], a[140:143], v[8:11], 0\n"
    "v_exp_f32_e64 v60, v60\n"
    "v_mfma_f32_16x16x32_bf16 a[200:203], a[136:139], v[12:15], 0\n"
    "ds_read_b128 a[136:139], v88 offset:1024\n"
    "v_fma_f32 v49, v49, s52, v84\n"
    "v_mfma_f32_16x16x32_bf16 a[204:207], a[140:143], v[12:15], 0\n"
    "ds_read_b128 a[140:143], v89 offset:1024\n"
    "v_fma_f32 v51, v51, s52, v84\n"
    "v_mfma_f32_16x16x32_bf16 v[144:147], a[144:147], v[0:3], 0\n"
    "v_exp_f32_e64 v49, v49\n"
    "v_mfma_f32_16x16x32_bf16 v[148:151], a[148:151], v[0:3], 0\n"
    "v_exp_f32 v62, v62\n"
    "v_mfma_f32_16x16x32_bf16 v[208:211], a[144:147], v[4:7], 0\n"
    "v_exp_f32 v51, v51\n"
    "v_mfma_f32_16x16x32_bf16 v[212:215], a[148:151], v[4:7], 0\n"
    "v_fma_f32 v53, v53, s52, v85\n"
    "v_fma_f32 v55, v55, s52, v85\n"
    "v_mfma_f32_16x16x32_bf16 a[240:243], a[144:147], v[8:11], 0\n"
    "v_exp_f32_e64 v53, v53\n"
    "v_mfma_f32_16x16x32_bf16 a[244:247], a[148:151], v[8:11], 0\n"
    "v_exp_f32_e64 v55, v55\n"
    "v_mfma_f32_16x16x32_bf16 a[208:211], a[144:147], v[12:15], 0\n"
    "ds_read_b128 a[144:147], v88 offset:2048\n"
    "v_fma_f32 v57, v57, s52, v86\n"
    "v_mfma_f32_16x16x32_bf16 a[212:215], a[148:151], v[12:15], 0\n"
    "ds_read_b128 a[148:151], v89 offset:2048\n"
    "v_fma_f32 v59, v59, s52, v86\n"
    "v_mfma_f32_16x16x32_bf16 v[152:155], a[152:155], v[0:3], 0\n"
    "v_exp_f32 v57, v57\n"
    "v_mfma_f32_16x16x32_bf16 v[156:159], a[156:159], v[0:3], 0\n"
    "v_exp_f32 v59, v59\n"
    "v_mfma_f32_16x16x32_bf16 v[216:219], a[152:155], v[4:7], 0\n"
    "v_fma_f32 v61, v61, s52, v87\n"
    "v_fma_f32 v63, v63, s52, v87\n"
    "v_mfma_f32_16x16x32_bf16 v[220:223], a[156:159], v[4:7], 0\n"
    "v_add_f32 v80, v80, v48\n"
    "v_add_f32 v81, v81, v52\n"
    "v_add_f32 v82, v82, v56\n"
    "v_exp_f32 v61, v61\n"
    "v_add_f32 v83, v83, v60\n"
    "v_mfma_f32_16x16x32_bf16 a[248:251], a[152:155], v[8:11], 0\n"
    "v_add_f32 v80, v80, v49\n"
    "v_add_f32 v81, v81, v53\n"
    "v_add_f32 v82, v82, v57\n"
    "v_add_f32 v83, v83, v61\n"
    "v_exp_f32 v63, v63\n"
    "v_mfma_f32_16x16x32_bf16 a[252:255], a[156:159], v[8:11], 0\n"
    "v_add_f32 v80, v80, v50\n"
    "v_add_f32 v81, v81, v54\n"
    "v_add_f32 v82, v82, v58\n"
    "v_add_f32 v83, v83, v62\n"
    "v_mfma_f32_16x16x32_bf16 a[216:219], a[152:155], v[12:15], 0\n"
    "ds_read_b128 a[152:155], v88 offset:3072\n"
    "v_add_f32_e64 v80, v80, v51\n"
    "v_add_f32_e64 v81, v81, v55\n"
    "v_add_f32 v82, v82, v59\n"
    "v_add_f32_e64 v83, v83, v63\n"
    "v_mfma_f32_16x16x32_bf16 a[220:223], a[156:159], v[12:15], 0\n"
    "s_waitcnt lgkmcnt(5)\n"
    "ds_read_b128 a[156:159], v89 offset:3072\n"
    "v_add_u32 v93, 4096, v93\n"
    "LQ4_%=:\n"
    "v_mfma_f32_16x16x32_bf16 v[0:3], a[128:131], a[0:3], 0\n"
    "s_mov_b32 m0, s61\n"
    "v_cvt_pk_bf16_f32 v64, v32, v33\n"
    "s_nop 0\n"
    "v_cvt_pk_bf16_f32 v65, v34, v35\n"
    "v_mfma_f32_16x16x32_bf16 v[0:3], a[132:135], a[4:7], v[0:3]\n"
    "buffer_load_dwordx4 v92, s[36:39], s53 offen offset:0 lds\n"
    "v_mfma_f32_16x16x32_bf16 v[4:7], a[128:131], a[32:35], 0\n"
    "s_add_u32 m0, m0, s97\n"
    "v_cvt_pk_bf16_f32 v66, v48, v49\n"
    "v_cvt_pk_bf16_f32 v67, v50, v51\n"
    "v_mfma_f32_16x16x32_bf16 v[4:7], a[132:135], a[36:39], v[4:7]\n"
    "s_sub_u32 s64, s60, s61\n"
    "v_cvt_pk_bf16_f32 v68, v36, v37\n"
    "v_cvt_pk_bf16_f32 v69, v38, v39\n"
    "v_mfma_f32_16x16x32_bf16 v[8:11], a[128:131], a[64:67], 0\n"
    "s_mov_b32 s71, s61\n"
    "v_add_u32 v90, s64, v90\n"
    "v_add_u32 v91, s64, v91\n"
    "v_mfma_f32_16x16x32_bf16 v[8:11], a[132:135], a[68:71], v[8:11]\n"
    "buffer_load_dwordx4 v92, s[36:39], s53 offen offset:128 lds\n"
    "v_mfma_f32_16x16x32_bf16 v[12:15], a[128:131], a[96:99], 0\n"
    "s_add_u32 m0, m0, s97\n"
    "ds_read_b128 a[128:131], v88 offset:8192\n"
    "v_cvt_pk_bf16_f32 v70, v52, v53\n"
    "v_mfma_f32_16x16x32_bf16 v[12:15], a[132:135], a[100:103], v[12:15]\n"
    "s_waitcnt lgkmcnt(5)\n"
    "ds_read_b128 a[132:135], v89 offset:8192\n"
    "v_cvt_pk_bf16_f32 v71, v54, v55\n"
    "v_mfma_f32_16x16x32_bf16 v[0:3], a[136:139], a[8:11], v[0:3]\n"
    "s_mov_b32 s61, s60\n"
    "v_cvt_pk_bf16_f32 v72, v40, v41\n"
    "v_cvt_pk_bf16_f32 v73, v42, v43\n"
    "v_mfma_f32_16x16x32_bf16 v[0:3], a[140:143], a[12:15], v[0:3]\n"
    "buffer_load_dwordx4 v92, s[36:39], s53 offen offset:256 lds\n"
    "v_mfma_f32_16x16x32_bf16 v[4:7], a[136:139], a[40:43], v[4:7]\n"
    "s_add_u32 m0, m0, 896\n"
    "v_cvt_pk_bf16_f32 v74, v56, v57\n"
    "v_cvt_pk_bf16_f32 v75, v58, v59\n"
    "v_mfma_f32_16x16x32_bf16 v[4:7], a[140:143], a[44:47], v[4:7]\n"
    "s_mov_b32 s60, s63\n"
    "v_cvt_pk_bf16_f32 v76, v44, v45\n"
    "v_cvt_pk_bf16_f32 v77, v46, v47\n"
    "v_mfma_f32_16x16x32_bf16 v[8:11], a[136:139], a[72:75], v[8:11]\n"
    "s_mov_b32 s63, s71\n"
    "v_cvt_pk_bf16_f32 v78, v60, v61\n"
    "v_cvt_pk_bf16_f32 v79, v62, v63\n"
    "v_mfma_f32_16x16x32_bf16 v[8:11], a[140:143], a[76:79], v[8:11]\n"
    "buffer_load_dwordx4 v92, s[36:39], s53 offen offset:384 lds\n"
    "v_mfma_f32_16x16x32_bf16 v[12:15], a[136:139], a[104:107], v[12:15]\n"
    "s_add_u32 m0, m0, 1408\n"
    "ds_read_b128 a[136:139], v88 offset:9216\n"
    "v_mfma_f32_16x16x32_bf16 v[12:15], a[140:143], a[108:111], v[12:15]\n"
    "s_waitcnt lgkmcnt(5)\n"
    "ds_read_b128 a[140:143], v89 offset:9216\n"
    "v_mfma_f32_16x16x32_bf16 v[0:3], a[144:147], a[16:19], v[0:3]\n"
    "s_sub_u32 s62, s63, s60\n"
    "v_mfma_f32_16x16x32_bf16 v[0:3], a[148:151], a[20:23], v[0:3]\n"
    "buffer_load_dwordx4 v92, s[36:39], s54 offen offset:0 lds\n"
    "v_mfma_f32_16x16x32_bf16 v[4:7], a[144:147], a[48:51], v[4:7]\n"
    "s_add_u32 m0, m0, 896\n"
    "v_mfma_f32_16x16x32_bf16 v[4:7], a[148:151], a[52:55], v[4:7]\n"
    "v_mfma_f32_16x16x32_bf16 v[8:11], a[144:147], a[80:83], v[8:11]\n"
    "v_mfma_f32_16x16x32_bf16 v[8:11], a[148:151], a[84:87], v[8:11]\n"
    "buffer_load_dwordx4 v92, s[36:39], s54 offen offset:128 lds\n"
    "v_mfma_f32_16x16x32_bf16 v[12:15], a[144:147], a[112:115], v[12:15]\n"
    "s_add_u32 m0, m0, s97\n"
    "ds_read_b128 a[144:147], v88 offset:10240\n"
    "v_mfma_f32_16x16x32_bf16 v[12:15], a[148:151], a[116:119], v[12:15]\n"
    "s_waitcnt lgkmcnt(5)\n"
    "ds_read_b128 a[148:151], v89 offset:10240\n"
    "v_mfma_f32_16x16x32_bf16 v[0:3], a[152:155], a[24:27], v[0:3]\n"
    "v_mfma_f32_16x16x32_bf16 v[0:3], a[156:159], a[28:31], v[0:3]\n"
    "buffer_load_dwordx4 v92, s[36:39], s54 offen offset:256 lds\n"
    "v_mfma_f32_16x16x32_bf16 v[4:7], a[152:155], a[56:59], v[4:7]\n"
    "s_add_u32 m0, m0, 896\n"
    "v_mfma_f32_16x16x32_bf16 v[4:7], a[156:159], a[60:63], v[4:7]\n"
    "v_mfma_f32_16x16x32_bf16 v[8:11], a[152:155], a[88:91], v[8:11]\n"
    "v_mfma_f32_16x16x32_bf16 v[8:11], a[156:159], a[92:95], v[8:11]\n"
    "buffer_load_dwordx4 v92, s[36:39], s54 offen offset:384 lds\n"
    "v_mfma_f32_16x16x32_bf16 v[12:15], a[152:155], a[120:123], v[12:15]\n"
    "s_add_u32 s53, s53, 0x8000\n"
    "ds_read_b128 a[152:155], v88 offset:11264\n"
    "v_fma_f32 v0, v0, s52, v84\n"
    "v_mfma_f32_16x16x32_bf16 v[12:15], a[156:159], a[124:127], v[12:15]\n"
    "s_waitcnt lgkmcnt(5)\n"
    "ds_read_b128 a[156:159], v89 offset:11264\n"
    "s_nop 0\n"
    "v_fma_f32 v2, v2, s52, v84\n"
    "v_mfma_f32_16x16x32_bf16 v[16:19], a[128:131], a[0:3], 0\n"
    "s_add_u32 s54, s54, s96\n"
    "v_exp_f32 v0, v0\n"
    "v_mfma_f32_16x16x32_bf16 v[16:19], a[132:135], a[4:7], v[16:19]\n"
    "s_mov_b32 m0, s59\n"
    "v_exp_f32 v2, v2\n"
    "v_mfma_f32_16x16x32_bf16 v[20:23], a[128:131], a[32:35], 0\n"
    "buffer_load_dwordx4 v92, s[44:47], s57 offen offset:0 nt lds\n"
    "v_mfma_f32_16x16x32_bf16 v[20:23], a[132:135], a[36:39], v[20:23]\n"
    "s_add_u32 m0, m0, 896\n"
    "v_fma_f32 v4, v4, s52, v85\n"
    "v_fma_f32 v6, v6, s52, v85\n"
    "v_mfma_f32_16x16x32_bf16 v[24:27], a[128:131], a[64:67], 0\n"
    "v_exp_f32_e64 v4, v4\n"
    "v_mfma_f32_16x16x32_bf16 v[24:27], a[132:135], a[68:71], v[24:27]\n"
    "buffer_load_dwordx4 v92, s[44:47], s57 offen offset:128 nt lds\n"
    "v_mfma_f32_16x16x32_bf16 v[28:31], a[128:131], a[96:99], 0\n"
    "s_add_u32 s57, s57, s58\n"
    "ds_read_b128 a[128:131], v88 offset:16384\n"
    "v_fma_f32 v8, v8, s52, v86\n"
    "v_mfma_f32_16x16x32_bf16 v[28:31], a[132:135], a[100:103], v[28:31]\n"
    "s_waitcnt lgkmcnt(5)\n"
    "ds_read_b128 a[132:135], v89 offset:16384\n"
    "v_fma_f32 v10, v10, s52, v86\n"
    "v_mfma_f32_16x16x32_bf16 v[16:19], a[136:139], a[8:11], v[16:19]\n"
    "s_xor_b32 s58, s58, 0xe00\n"
    "v_exp_f32 v8, v8\n"
    "v_mfma_f32_16x16x32_bf16 v[16:19], a[140:143], a[12:15], v[16:19]\n"
    "s_add_u32 s59, s59, 0x800\n"
    "v_exp_f32 v6, v6\n"
    "v_mfma_f32_16x16x32_bf16 v[20:23], a[136:139], a[40:43], v[20:23]\n"
    "v_exp_f32_e64 v10, v10\n"
    "v_mfma_f32_16x16x32_bf16 v[20:23], a[140:143], a[44:47], v[20:23]\n"
    "v_fma_f32 v12, v12, s52, v87\n"
    "v_fma_f32 v14, v14, s52, v87\n"
    "v_mfma_f32_16x16x32_bf16 v[24:27], a[136:139], a[72:75], v[24:27]\n"
    "v_exp_f32_e64 v12, v12\n"
    "v_mfma_f32_16x16x32_bf16 v[24:27], a[140:143], a[76:79], v[24:27]\n"
    "v_exp_f32_e64 v14, v14\n"
    "v_mfma_f32_16x16x32_bf16 v[28:31], a[136:139], a[104:107], v[28:31]\n"
    "ds_read_b128 a[136:139], v88 offset:17408\n"
    "v_fma_f32 v1, v1, s52, v84\n"
    "v_mfma_f32_16x16x32_bf16 v[28:31], a[140:143], a[108:111], v[28:31]\n"
    "s_waitcnt lgkmcnt(5)\n"
    "ds_read_b128 a[140:143], v89 offset:17408\n"
    "v_fma_f32 v3, v3, s52, v84\n"
    "v_mfma_f32_16x16x32_bf16 v[16:19], a[144:147], a[16:19], v[16:19]\n"
    "v_exp_f32 v1, v1\n"
    "v_mfma_f32_16x16x32_bf16 v[16:19], a[148:151], a[20:23], v[16:19]\n"
    "v_exp_f32_e64 v3, v3\n"
    "v_mfma_f32_16x16x32_bf16 v[20:23], a[144:147], a[48:51], v[20:23]\n"
    "v_fma_f32 v5, v5, s52, v85\n"
    "v_fma_f32 v7, v7, s52, v85\n"
    "v_mfma_f32_16x16x32_bf16 v[20:23], a[148:151], a[52:55], v[20:23]\n"
    "v_exp_f32_e64 v5, v5\n"
    "v_mfma_f32_16x16x32_bf16 v[24:27], a[144:147], a[80:83], v[24:27]\n"
    "v_exp_f32_e64 v7, v7\n"
    "v_mfma_f32_16x16x32_bf16 v[24:27], a[148:151], a[84:87], v[24:27]\n"
    "v_fma_f32 v9, v9, s52, v86\n"
    "v_fma_f32 v11, v11, s52, v86\n"
    "v_mfma_f32_16x16x32_bf16 v[28:31], a[144:147], a[112:115], v[28:31]\n"
    "ds_read_b128 a[144:147], v88 offset:18432\n"
    "v_fma_f32 v13, v13, s52, v87\n"
    "v_mfma_f32_16x16x32_bf16 v[28:31], a[148:151], a[116:119], v[28:31]\n"
    "s_waitcnt lgkmcnt(5)\n"
    "ds_read_b128 a[148:151], v89 offset:18432\n"
    "v_fma_f32 v15, v15, s52, v87\n"
    "v_mfma_f32_16x16x32_bf16 v[16:19], a[152:155], a[24:27], v[16:19]\n"
    "v_exp_f32 v9, v9\n"
    "v_mfma_f32_16x16x32_bf16 v[16:19], a[156:159], a[28:31], v[16:19]\n"
    "v_exp_f32 v13, v13\n"
    "v_mfma_f32_16x16x32_bf16 v[20:23], a[152:155], a[56:59], v[20:23]\n"
    "v_exp_f32 v11, v11\n"
    "v_mfma_f32_16x16x32_bf16 v[20:23], a[156:159], a[60:63], v[20:23]\n"
    "v_exp_f32 v15, v15\n"
    "v_mfma_f32_16x16x32_bf16 v[24:27], a[152:155], a[88:91], v[24:27]\n"
    "v_add_f32 v80, v80, v0\n"
    "v_add_f32 v81, v81, v4\n"
    "v_mfma_f32_16x16x32_bf16 v[24:27], a[156:159], a[92:95], v[24:27]\n"
    "v_add_f32 v82, v82, v8\n"
    "v_add_f32 v83, v83, v12\n"
    "v_mfma_f32_16x16x32_bf16 v[28:31], a[152:155], a[120:123], v[28:31]\n"
    "ds_read_b128 a[152:155], v88 offset:19456\n"
    "v_add_f32 v80, v80, v1\n"
    "v_mfma_f32_16x16x32_bf16 v[28:31], a[156:159], a[124:127], v[28:31]\n"
    "s_waitcnt lgkmcnt(5)\n"
    "ds_read_b128 a[156:159], v89 offset:19456\n"
    "v_add_f32 v81, v81, v5\n"
    "v_mfma_f32_16x16x32_bf16 v[32:35], a[128:131], a[0:3], 0\n"
    "v_add_f32 v82, v82, v9\n"
    "v_add_f32 v83, v83, v13\n"
    "v_mfma_f32_16x16x32_bf16 v[32:35], a[132:135], a[4:7], v[32:35]\n"
    "v_add_f32 v80, v80, v2\n"
    "v_add_f32 v81, v81, v6\n"
    "v_mfma_f32_16x16x32_bf16 v[36:39], a[128:131], a[32:35], 0\n"
    "v_add_f32 v82, v82, v10\n"
    "v_add_f32 v83, v83, v14\n"
    "v_mfma_f32_16x16x32_bf16 v[36:39], a[132:135], a[36:39], v[36:39]\n"
    "v_add_f32 v80, v80, v3\n"
    "v_add_f32 v81, v81, v7\n"
    "v_mfma_f32_16x16x32_bf16 v[40:43], a[128:131], a[64:67], 0\n"
    "v_add_f32_e64 v82, v82, v11\n"
    "v_add_f32_e64 v83, v83, v15\n"
    "v_mfma_f32_16x16x32_bf16 v[40:43], a[132:135], a[68:71], v[40:43]\n"
    "v_cvt_pk_bf16_f32 v0, v0, v1\n"
    "v_cvt_pk_bf16_f32 v1, v2, v3\n"
    "v_mfma_f32_16x16x32_bf16 v[44:47], a[128:131], a[96:99], 0\n"
    "ds_read_b128 a[128:131], v88 offset:24576\n"
    "v_cvt_pk_bf16_f32 v4, v4, v5\n"
    "v_mfma_f32_16x16x32_bf16 v[44:47], a[132:135], a[100:103], v[44:47]\n"
    "s_waitcnt lgkmcnt(5)\n"
    "ds_read_b128 a[132:135], v89 offset:24576\n"
    "v_cvt_pk_bf16_f32 v5, v6, v7\n"
    "v_mfma_f32_16x16x32_bf16 v[32:35], a[136:139], a[8:11], v[32:35]\n"
    "s_nop 0\n"
    "v_cvt_pk_bf16_f32 v8, v8, v9\n"
    "v_cvt_pk_bf16_f32 v9, v10, v11\n"
    "v_mfma_f32_16x16x32_bf16 v[32:35], a[140:143], a[12:15], v[32:35]\n"
    "v_cvt_pk_bf16_f32 v12, v12, v13\n"
    "v_cvt_pk_bf16_f32 v13, v14, v15\n"
    "v_mfma_f32_16x16x32_bf16 v[36:39], a[136:139], a[40:43], v[36:39]\n"
    "v_fma_f32 v16, v16, s52, v84\n"
    "v_fma_f32 v18, v18, s52, v84\n"
    "v_mfma_f32_16x16x32_bf16 v[36:39], a[140:143], a[44:47], v[36:39]\n"
    "v_exp_f32_e64 v16, v16\n"
    "v_mfma_f32_16x16x32_bf16 v[40:43], a[136:139], a[72:75], v[40:43]\n"
    "v_exp_f32_e64 v18, v18\n"
    "v_mfma_f32_16x16x32_bf16 v[40:43], a[140:143], a[76:79], v[40:43]\n"
    "v_fma_f32 v20, v20, s52, v85\n"
    "v_fma_f32 v22, v22, s52, v85\n"
    "v_mfma_f32_16x16x32_bf16 v[44:47], a[136:139], a[104:107], v[44:47]\n"
    "s_nop 0\n"
    "ds_read_b128 a[136:139], v88 offset:25600\n"
    "v_fma_f32 v24, v24, s52, v86\n"
    "v_mfma_f32_16x16x32_bf16 v[44:47], a[140:143], a[108:111], v[44:47]\n"
    "s_waitcnt lgkmcnt(5)\n"
    "ds_read_b128 a[140:143], v89 offset:25600\n"
    "v_fma_f32 v26, v26, s52, v86\n"
    "v_mfma_f32_16x16x32_bf16 v[32:35], a[144:147], a[16:19], v[32:35]\n"
    "v_exp_f32_e64 v20, v20\n"
    "v_mfma_f32_16x16x32_bf16 v[32:35], a[148:151], a[20:23], v[32:35]\n"
    "v_exp_f32_e64 v24, v24\n"
    "v_mfma_f32_16x16x32_bf16 v[36:39], a[144:147], a[48:51], v[36:39]\n"
    "v_exp_f32_e64 v22, v22\n"
    "v_mfma_f32_16x16x32_bf16 v[36:39], a[148:151], a[52:55], v[36:39]\n"
    "v_exp_f32_e64 v26, v26\n"
    "v_mfma_f32_16x16x32_bf16 v[40:43], a[144:147], a[80:83], v[40:43]\n"
    "v_fma_f32 v28, v28, s52, v87\n"
    "v_fma_f32 v30, v30, s52, v87\n"
    "v_mfma_f32_16x16x32_bf16 v[40:43], a[148:151], a[84:87], v[40:43]\n"
    "v_exp_f32_e64 v28, v28\n"
    "v_mfma_f32_16x16x32_bf16 v[44:47], a[144:147], a[112:115], v[44:47]\n"
    "s_nop 0\n"
    "ds_read_b128 a[144:147], v88 offset:26624\n"
    "v_fma_f32 v17, v17, s52, v84\n"
    "v_mfma_f32_16x16x32_bf16 v[44:47], a[148:151], a[116:119], v[44:47]\n"
    "s_waitcnt lgkmcnt(5)\n"
    "ds_read_b128 a[148:151], v89 offset:26624\n"
    "v_fma_f32 v19, v19, s52, v84\n"
    "v_mfma_f32_16x16x32_bf16 v[32:35], a[152:155], a[24:27], v[32:35]\n"
    "v_exp_f32_e64 v17, v17\n"
    "v_mfma_f32_16x16x32_bf16 v[32:35], a[156:159], a[28:31], v[32:35]\n"
    "v_exp_f32_e64 v30, v30\n"
    "v_mfma_f32_16x16x32_bf16 v[36:39], a[152:155], a[56:59], v[36:39]\n"
    "v_exp_f32_e64 v19, v19\n"
    "v_mfma_f32_16x16x32_bf16 v[36:39], a[156:159], a[60:63], v[36:39]\n"
    "v_fma_f32 v21, v21, s52, v85\n"
    "v_fma_f32 v23, v23, s52, v85\n"
    "v_mfma_f32_16x16x32_bf16 v[40:43], a[152:155], a[88:91], v[40:43]\n"
    "v_exp_f32_e64 v21, v21\n"
    "v_mfma_f32_16x16x32_bf16 v[40:43], a[156:159], a[92:95], v[40:43]\n"
    "v_exp_f32_e64 v23, v23\n"
    "v_mfma_f32_16x16x32_bf16 v[44:47], a[152:155], a[120:123], v[44:47]\n"
    "s_nop 0\n"
    "ds_read_b128 a[152:155], v88 offset:27648\n"
    "v_fma_f32 v25, v25, s52, v86\n"
    "v_mfma_f32_16x16x32_bf16 v[44:47], a[156:159], a[124:127], v[44:47]\n"
    "s_waitcnt lgkmcnt(5)\n"
    "ds_read_b128 a[156:159], v89 offset:27648\n"
    "v_fma_f32 v27, v27, s52, v86\n"
    "v_mfma_f32_16x16x32_bf16 v[48:51], a[128:131], a[0:3], 0\n"
    "v_exp_f32_e64 v25, v25\n"
    "v_mfma_f32_16x16x32_bf16 v[48:51], a[132:135], a[4:7], v[48:51]\n"
    "v_exp_f32_e64 v27, v27\n"
    "v_mfma_f32_16x16x32_bf16 v[52:55], a[128:131], a[32:35], 0\n"
    "v_fma_f32 v29, v29, s52, v87\n"
    "v_fma_f32 v31, v31, s52, v87\n"
    "v_mfma_f32_16x16x32_bf16 v[52:55], a[132:135], a[36:39], v[52:55]\n"
    "v_exp_f32 v29, v29\n"
    "v_mfma_f32_16x16x32_bf16 v[56:59], a[128:131], a[64:67], 0\n"
    "v_exp_f32 v31, v31\n"
    "v_mfma_f32_16x16x32_bf16 v[56:59], a[132:135], a[68:71], v[56:59]\n"
    "v_add_f32 v80, v80, v16\n"
    "v_add_f32 v81, v81, v20\n"
    "v_mfma_f32_16x16x32_bf16 v[60:63], a[128:131], a[96:99], 0\n"
    "s_waitcnt vmcnt(12) lgkmcnt(0)\n"
    "v_add_f32 v82, v82, v24\n"
    "v_add_f32_e64 v83, v83, v28\n"
    "v_mfma_f32_16x16x32_bf16 v[60:63], a[132:135], a[100:103], v[60:63]\n"
    "s_barrier\n"
    "v_mfma_f32_16x16x32_bf16 v[48:51], a[136:139], a[8:11], v[48:51]\n"
    "s_mov_b32 m0, s60\n"
    "ds_read_b64_tr_b16 a[128:129], v90 offset:0\n"
    "ds_read_b64_tr_b16 a[130:131], v90 offset:8192\n"
    "v_mfma_f32_16x16x32_bf16 v[48:51], a[140:143], a[12:15], v[48:51]\n"
    "ds_read_b64_tr_b16 a[132:133], v90 offset:16384\n"
    "ds_read_b64_tr_b16 a[134:135], v90 offset:24576\n"
    "v_mfma_f32_16x16x32_bf16 v[52:55], a[136:139], a[40:43], v[52:55]\n"
    "buffer_load_dwordx4 v92, s[40:43], s55 offen offset:0 lds\n"
    "v_mfma_f32_16x16x32_bf16 v[52:55], a[140:143], a[44:47], v[52:55]\n"
    "s_add_u32 m0, m0, s97\n"
    "v_add_f32 v80, v80, v17\n"
    "v_add_f32 v81, v81, v21\n"
    "v_mfma_f32_16x16x32_bf16 v[56:59], a[136:139], a[72:75], v[56:59]\n"
    "v_add_f32 v82, v82, v25\n"
    "v_add_f32_e64 v83, v83, v29\n"
    "v_mfma_f32_16x16x32_bf16 v[56:59], a[140:143], a[76:79], v[56:59]\n"
    "v_add_f32 v80, v80, v18\n"
    "v_add_f32 v81, v81, v22\n"
    "v_mfma_f32_16x16x32_bf16 v[60:63], a[136:139], a[104:107], v[60:63]\n"
    "ds_read_b64_tr_b16 a[136:137], v90 offset:8\n"
    "ds_read_b64_tr_b16 a[138:139], v90 offset:8200\n"
    "v_mfma_f32_16x16x32_bf16 v[60:63], a[140:143], a[108:111], v[60:63]\n"
    "ds_read_b64_tr_b16 a[140:141], v90 offset:16392\n"
    "ds_read_b64_tr_b16 a[142:143], v90 offset:24584\n"
    "v_mfma_f32_16x16x32_bf16 v[48:51], a[144:147], a[16:19], v[48:51]\n"
    "buffer_load_dwordx4 v92, s[40:43], s55 offen offset:128 lds\n"
    "v_mfma_f32_16x16x32_bf16 v[48:51], a[148:151], a[20:23], v[48:51]\n"
    "s_add_u32 m0, m0, s97\n"
    "v_add_f32 v82, v82, v26\n"
    "v_add_f32 v83, v83, v30\n"
    "v_mfma_f32_16x16x32_bf16 v[52:55], a[144:147], a[48:51], v[52:55]\n"
    "v_add_f32 v80, v80, v19\n"
    "v_add_f32 v81, v81, v23\n"
    "v_mfma_f32_16x16x32_bf16 v[52:55], a[148:151], a[52:55], v[52:55]\n"
    "v_add_f32 v82, v82, v27\n"
    "v_add_f32_e64 v83, v83, v31\n"
    "v_mfma_f32_16x16x32_bf16 v[56:59], a[144:147], a[80:83], v[56:59]\n"
    "v_cvt_pk_bf16_f32 v2, v16, v17\n"
    "v_cvt_pk_bf16_f32 v3, v18, v19\n"
    "v_mfma_f32_16x16x32_bf16 v[56:59], a[148:151], a[84:87], v[56:59]\n"
    "v_cvt_pk_bf16_f32 v6, v20, v21\n"
    "v_cvt_pk_bf16_f32 v7, v22, v23\n"
    "v_mfma_f32_16x16x32_bf16 v[60:63], a[144:147], a[112:115], v[60:63]\n"
    "ds_read_b64_tr_b16 a[144:145], v91 offset:0\n"
    "ds_read_b64_tr_b16 a[146:147], v91 offset:8192\n"
    "v_mfma_f32_16x16x32_bf16 v[60:63], a[148:151], a[116:119], v[60:63]\n"
    "ds_read_b64_tr_b16 a[148:149], v91 offset:16384\n"
    "ds_read_b64_tr_b16 a[150:151], v91 offset:24576\n"
    "v_mfma_f32_16x16x32_bf16 v[48:51], a[152:155], a[24:27], v[48:51]\n"
    "buffer_load_dwordx4 v92, s[40:43], s55 offen offset:256 lds\n"
    "v_mfma_f32_16x16x32_bf16 v[48:51], a[156:159], a[28:31], v[48:51]\n"
    "s_add_u32 m0, m0, 896\n"
    "v_cvt_pk_bf16_f32 v10, v24, v25\n"
    "v_cvt_pk_bf16_f32 v11, v26, v27\n"
    "v_mfma_f32_16x16x32_bf16 v[52:55], a[152:155], a[56:59], v[52:55]\n"
    "v_cvt_pk_bf16_f32 v14, v28, v29\n"
    "v_cvt_pk_bf16_f32 v15, v30, v31\n"
    "v_mfma_f32_16x16x32_bf16 v[52:55], a[156:159], a[60:63], v[52:55]\n"
    "v_fma_f32 v32, v32, s52, v84\n"
    "v_fma_f32 v34, v34, s52, v84\n"
    "v_mfma_f32_16x16x32_bf16 v[56:59], a[152:155], a[88:91], v[56:59]\n"
    "v_exp_f32 v32, v32\n"
    "v_mfma_f32_16x16x32_bf16 v[56:59], a[156:159], a[92:95], v[56:59]\n"
    "v_exp_f32 v34, v34\n"
    "v_mfma_f32_16x16x32_bf16 v[60:63], a[152:155], a[120:123], v[60:63]\n"
    "ds_read_b64_tr_b16 a[152:153], v91 offset:8\n"
    "ds_read_b64_tr_b16 a[154:155], v91 offset:8200\n"
    "v_mfma_f32_16x16x32_bf16 v[60:63], a[156:159], a[124:127], v[60:63]\n"
    "s_waitcnt lgkmcnt(10)\n"
    "ds_read_b64_tr_b16 a[156:157], v91 offset:16392\n"
    "ds_read_b64_tr_b16 a[158:159], v91 offset:24584\n"
    "v_mfma_f32_16x16x32_bf16 v[96:99], a[128:131], v[64:67], v[96:99]\n"
    "s_nop 0\n"
    "buffer_load_dwordx4 v92, s[40:43], s55 offen offset:384 lds\n"
    "v_mfma_f32_16x16x32_bf16 v[96:99], a[132:135], v[0:3], v[96:99]\n"
    "s_add_u32 m0, m0, 1408\n"
    "v_fma_f32 v36, v36, s52, v85\n"
    "v_fma_f32 v38, v38, s52, v85\n"
    "v_mfma_f32_16x16x32_bf16 v[160:163], a[128:131], v[68:71], v[160:163]\n"
    "v_exp_f32_e64 v36, v36\n"
    "v_mfma_f32_16x16x32_bf16 v[160:163], a[132:135], v[4:7], v[160:163]\n"
    "s_nop 0\n"
    "v_exp_f32_e64 v38, v38\n"
    "v_mfma_f32_16x16x32_bf16 v[224:227], a[128:131], v[72:75], v[224:227]\n"
    "s_nop 0\n"
    "v_fma_f32 v40, v40, s52, v86\n"
    "v_fma_f32 v42, v42, s52, v86\n"
    "v_mfma_f32_16x16x32_bf16 v[224:227], a[132:135], v[8:11], v[224:227]\n"
    "v_exp_f32_e64 v40, v40\n"
    "v_mfma_f32_16x16x32_bf16 a[160:163], a[128:131], v[76:79], a[160:163]\n"
    "s_nop 0\n"
    "ds_read_b64_tr_b16 a[128:129], v90 offset:1024\n"
    "ds_read_b64_tr_b16 a[130:131], v90 offset:9216\n"
    "v_mfma_f32_16x16x32_bf16 a[160:163], a[132:135], v[12:15], a[160:163]\n"
    "s_waitcnt lgkmcnt(10)\n"
    "ds_read_b64_tr_b16 a[132:133], v90 offset:17408\n"
    "ds_read_b64_tr_b16 a[134:135], v90 offset:25600\n"
    "v_mfma_f32_16x16x32_bf16 v[100:103], a[136:139], v[64:67], v[100:103]\n"
    "buffer_load_dwordx4 v92, s[40:43], s56 offen offset:0 lds\n"
    "v_mfma_f32_16x16x32_bf16 v[100:103], a[140:143], v[0:3], v[100:103]\n"
    "s_add_u32 m0, m0, s97\n"
    "v_exp_f32 v42, v42\n"
    "v_mfma_f32_16x16x32_bf16 v[164:167], a[136:139], v[68:71], v[164:167]\n"
    "v_fma_f32 v44, v44, s52, v87\n"
    "v_fma_f32 v46, v46, s52, v87\n"
    "v_mfma_f32_16x16x32_bf16 v[164:167], a[140:143], v[4:7], v[164:167]\n"
    "v_exp_f32 v44, v44\n"
    "v_mfma_f32_16x16x32_bf16 v[228:231], a[136:139], v[72:75], v[228:231]\n"
    "v_exp_f32 v46, v46\n"
    "v_mfma_f32_16x16x32_bf16 v[228:231], a[140:143], v[8:11], v[228:231]\n"
    "v_fma_f32 v33, v33, s52, v84\n"
    "v_fma_f32 v35, v35, s52, v84\n"
    "v_mfma_f32_16x16x32_bf16 a[164:167], a[136:139], v[76:79], a[164:167]\n"
    "s_nop 0\n"
    "ds_read_b64_tr_b16 a[136:137], v90 offset:1032\n"
    "ds_read_b64_tr_b16 a[138:139], v90 offset:9224\n"
    "v_mfma_f32_16x16x32_bf16 a[164:167], a[140:143], v[12:15], a[164:167]\n"
    "s_waitcnt lgkmcnt(10)\n"
    "ds_read_b64_tr_b16 a[140:141], v90 offset:17416\n"
    "ds_read_b64_tr_b16 a[142:143], v90 offset:25608\n"
    "v_mfma_f32_16x16x32_bf16 v[104:107], a[144:147], v[64:67], v[104:107]\n"
    "buffer_load_dwordx4 v92, s[40:43], s56 offen offset:128 lds\n"
    "v_mfma_f32_16x16x32_bf16 v[104:107], a[148:151], v[0:3], v[104:107]\n"
    "s_add_u32 m0, m0, s97\n"
    "v_exp_f32 v33, v33\n"
    "v_mfma_f32_16x16x32_bf16 v[168:171], a[144:147], v[68:71], v[168:171]\n"
    "v_exp_f32_e64 v35, v35\n"
    "v_mfma_f32_16x16x32_bf16 v[168:171], a[148:151], v[4:7], v[168:171]\n"
    "v_fma_f32 v37, v37, s52, v85\n"
    "v_fma_f32 v39, v39, s52, v85\n"
    "v_mfma_f32_16x16x32_bf16 v[232:235], a[144:147], v[72:75], v[232:235]\n"
    "v_exp_f32_e64 v37, v37\n"
    "v_mfma_f32_16x16x32_bf16 v[232:235], a[148:151], v[8:11], v[232:235]\n"
    "v_exp_f32_e64 v39, v39\n"
    "v_mfma_f32_16x16x32_bf16 a[168:171], a[144:147], v[76:79], a[168:171]\n"
    "s_nop 0\n"
    "ds_read_b64_tr_b16 a[144:145], v91 offset:1024\n"
    "ds_read_b64_tr_b16 a[146:147], v91 offset:9216\n"
    "v_mfma_f32_16x16x32_bf16 a[168:171], a[148:151], v[12:15], a[168:171]\n"
    "s_waitcnt lgkmcnt(10)\n"
    "ds_read_b64_tr_b16 a[148:149], v91 offset:17408\n"
    "ds_read_b64_tr_b16 a[150:151], v91 offset:25600\n"
    "v_mfma_f32_16x16x32_bf16 v[108:111], a[152:155], v[64:67], v[108:111]\n"
    "buffer_load_dwordx4 v92, s[40:43], s56 offen offset:256 lds\n"
    "v_mfma_f32_16x16x32_bf16 v[108:111], a[156:159], v[0:3], v[108:111]\n"
    "s_add_u32 m0, m0, 896\n"
    "v_fma_f32 v41, v41, s52, v86\n"
    "v_fma_f32 v43, v43, s52, v86\n"
    "v_mfma_f32_16x16x32_bf16 v[172:175], a[152:155], v[68:71], v[172:175]\n"
    "v_exp_f32 v41, v41\n"
    "v_mfma_f32_16x16x32_bf16 v[172:175], a[156:159], v[4:7], v[172:175]\n"
    "v_exp_f32 v43, v43\n"
    "v_mfma_f32_16x16x32_bf16 v[236:239], a[152:155], v[72:75], v[236:239]\n"
    "v_fma_f32 v45, v45, s52, v87\n"
    "v_fma_f32 v47, v47, s52, v87\n"
    "v_mfma_f32_16x16x32_bf16 v[236:239], a[156:159], v[8:11], v[236:239]\n"
    "v_exp_f32_e64 v45, v45\n"
    "v_mfma_f32_16x16x32_bf16 a[172:175], a[152:155], v[76:79], a[172:175]\n"
    "ds_read_b64_tr_b16 a[152:153], v91 offset:1032\n"
    "ds_read_b64_tr_b16 a[154:155], v91 offset:9224\n"
    "v_mfma_f32_16x16x32_bf16 a[172:175], a[156:159], v[12:15], a[172:175]\n"
    "s_waitcnt lgkmcnt(10)\n"
    "ds_read_b64_tr_b16 a[156:157], v91 offset:17416\n"
    "ds_read_b64_tr_b16 a[158:159], v91 offset:25608\n"
    "v_mfma_f32_16x16x32_bf16 v[112:115], a[128:131], v[64:67], v[112:115]\n"
    "s_nop 0\n"
    "buffer_load_dwordx4 v92, s[40:43], s56 offen offset:384 lds\n"
    "v_mfma_f32_16x16x32_bf16 v[112:115], a[132:135], v[0:3], v[112:115]\n"
    "s_add_u32 s55, s55, 0x8000\n"
    "ds_read_b128 v[16:19], v93 offset:0\n"
    "ds_read_b128 v[20:23], v93 offset:1024\n"
    "v_mfma_f32_16x16x32_bf16 v[176:179], a[128:131], v[68:71], v[176:179]\n"
    "s_add_u32 s56, s56, s96\n"
    "v_exp_f32 v47, v47\n"
    "v_mfma_f32_16x16x32_bf16 v[176:179], a[132:135], v[4:7], v[176:179]\n"
    "v_add_f32_e64 v80, v80, v32\n"
    "v_add_f32_e64 v81, v81, v36\n"
    "v_mfma_f32_16x16x32_bf16 v[240:243], a[128:131], v[72:75], v[240:243]\n"
    "v_add_f32_e64 v82, v82, v40\n"
    "v_add_f32_e64 v83, v83, v44\n"
    "v_mfma_f32_16x16x32_bf16 v[240:243], a[132:135], v[8:11], v[240:243]\n"
    "v_add_f32_e64 v80, v80, v33\n"
    "v_add_f32_e64 v81, v81, v37\n"
    "v_mfma_f32_16x16x32_bf16 a[176:179], a[128:131], v[76:79], a[176:179]\n"
    "ds_read_b64_tr_b16 a[128:129], v90 offset:2048\n"
    "ds_read_b64_tr_b16 a[130:131], v90 offset:10240\n"
    "v_mfma_f32_16x16x32_bf16 a[176:179], a[132:135], v[12:15], a[176:179]\n"
    "s_waitcnt lgkmcnt(12)\n"
    "ds_read_b64_tr_b16 a[132:133], v90 offset:18432\n"
    "ds_read_b64_tr_b16 a[134:135], v90 offset:26624\n"
    "v_mfma_f32_16x16x32_bf16 v[116:119], a[136:139], v[64:67], v[116:119]\n"
    "v_add_f32 v82, v82, v41\n"
    "v_add_f32 v83, v83, v45\n"
    "v_mfma_f32_16x16x32_bf16 v[116:119], a[140:143], v[0:3], v[116:119]\n"
    "v_add_u32 v88, s62, v88\n"
    "v_add_u32 v89, s62, v89\n"
    "v_mfma_f32_16x16x32_bf16 v[180:183], a[136:139], v[68:71], v[180:183]\n"
    "v_add_f32 v80, v80, v34\n"
    "v_add_f32 v81, v81, v38\n"
    "v_mfma_f32_16x16x32_bf16 v[180:183], a[140:143], v[4:7], v[180:183]\n"
    "s_waitcnt lgkmcnt(4)\n"
    "buffer_store_dwordx4 v[16:19], v92, s[48:51], s98 offen offset:0 nt\n"
    "v_mfma_f32_16x16x32_bf16 v[244:247], a[136:139], v[72:75], v[244:247]\n"
    "v_add_f32 v82, v82, v42\n"
    "v_add_f32 v83, v83, v46\n"
    "v_mfma_f32_16x16x32_bf16 v[244:247], a[140:143], v[8:11], v[244:247]\n"
    "v_add_f32_e64 v80, v80, v35\n"
    "v_add_f32_e64 v81, v81, v39\n"
    "v_mfma_f32_16x16x32_bf16 a[180:183], a[136:139], v[76:79], a[180:183]\n"
    "ds_read_b64_tr_b16 a[136:137], v90 offset:2056\n"
    "ds_read_b64_tr_b16 a[138:139], v90 offset:10248\n"
    "v_mfma_f32_16x16x32_bf16 a[180:183], a[140:143], v[12:15], a[180:183]\n"
    "s_waitcnt lgkmcnt(12)\n"
    "ds_read_b64_tr_b16 a[140:141], v90 offset:18440\n"
    "ds_read_b64_tr_b16 a[142:143], v90 offset:26632\n"
    "v_mfma_f32_16x16x32_bf16 v[120:123], a[144:147], v[64:67], v[120:123]\n"
    "s_nop 0\n"
    "buffer_store_dwordx4 v[20:23], v92, s[48:51], s98 offen offset:128 nt\n"
    "v_mfma_f32_16x16x32_bf16 v[120:123], a[148:151], v[0:3], v[120:123]\n"
    "s_add_u32 s98, s98, s99\n"
    "v_add_f32 v82, v82, v43\n"
    "v_add_f32_e64 v83, v83, v47\n"
    "v_mfma_f32_16x16x32_bf16 v[184:187], a[144:147], v[68:71], v[184:187]\n"
    "s_xor_b32 s99, s99, 0xe00\n"
    "v_fma_f32 v48, v48, s52, v84\n"
    "v_fma_f32 v50, v50, s52, v84\n"
    "v_mfma_f32_16x16x32_bf16 v[184:187], a[148:151], v[4:7], v[184:187]\n"
    "v_exp_f32_e64 v48, v48\n"
    "v_mfma_f32_16x16x32_bf16 v[248:251], a[144:147], v[72:75], v[248:251]\n"
    "v_exp_f32_e64 v50, v50\n"
    "v_mfma_f32_16x16x32_bf16 v[248:251], a[148:151], v[8:11], v[248:251]\n"
    "v_fma_f32 v52, v52, s52, v85\n"
    "v_fma_f32 v54, v54, s52, v85\n"
    "v_mfma_f32_16x16x32_bf16 a[184:187], a[144:147], v[76:79], a[184:187]\n"
    "s_nop 0\n"
    "ds_read_b64_tr_b16 a[144:145], v91 offset:2048\n"
    "ds_read_b64_tr_b16 a[146:147], v91 offset:10240\n"
    "v_mfma_f32_16x16x32_bf16 a[184:187], a[148:151], v[12:15], a[184:187]\n"
    "s_waitcnt lgkmcnt(12)\n"
    "ds_read_b64_tr_b16 a[148:149], v91 offset:18432\n"
    "ds_read_b64_tr_b16 a[150:151], v91 offset:26624\n"
    "v_mfma_f32_16x16x32_bf16 v[124:127], a[152:155], v[64:67], v[124:127]\n"
    "v_exp_f32_e64 v52, v52\n"
    "v_mfma_f32_16x16x32_bf16 v[124:127], a[156:159], v[0:3], v[124:127]\n"
    "v_exp_f32_e64 v54, v54\n"
    "v_mfma_f32_16x16x32_bf16 v[188:191], a[152:155], v[68:71], v[188:191]\n"
    "v_fma_f32 v56, v56, s52, v86\n"
    "v_fma_f32 v58, v58, s52, v86\n"
    "v_mfma_f32_16x16x32_bf16 v[188:191], a[156:159], v[4:7], v[188:191]\n"
    "v_exp_f32_e64 v56, v56\n"
    "v_mfma_f32_16x16x32_bf16 v[252:255], a[152:155], v[72:75], v[252:255]\n"
    "v_exp_f32_e64 v58, v58\n"
    "v_mfma_f32_16x16x32_bf16 v[252:255], a[156:159], v[8:11], v[252:255]\n"
    "v_fma_f32 v60, v60, s52, v87\n"
    "v_fma_f32 v62, v62, s52, v87\n"
    "v_mfma_f32_16x16x32_bf16 a[188:191], a[152:155], v[76:79], a[188:191]\n"
    "ds_read_b64_tr_b16 a[152:153], v91 offset:2056\n"
    "ds_read_b64_tr_b16 a[154:155], v91 offset:10248\n"
    "v_mfma_f32_16x16x32_bf16 a[188:191], a[156:159], v[12:15], a[188:191]\n"
    "s_waitcnt lgkmcnt(10)\n"
    "ds_read_b64_tr_b16 a[156:157], v91 offset:18440\n"
    "ds_read_b64_tr_b16 a[158:159], v91 offset:26632\n"
    "v_mfma_f32_16x16x32_bf16 v[128:131], a[128:131], v[64:67], v[128:131]\n"
    "v_exp_f32 v60, v60\n"
    "v_mfma_f32_16x16x32_bf16 v[128:131], a[132:135], v[0:3], v[128:131]\n"
    "v_exp_f32_e64 v62, v62\n"
    "v_mfma_f32_16x16x32_bf16 v[192:195], a[128:131], v[68:71], v[192:195]\n"
    "v_fma_f32 v49, v49, s52, v84\n"
    "v_fma_f32 v51, v51, s52, v84\n"
    "v_mfma_f32_16x16x32_bf16 v[192:195], a[132:135], v[4:7], v[192:195]\n"
    "v_exp_f32_e64 v49, v49\n"
    "v_mfma_f32_16x16x32_bf16 a[224:227], a[128:131], v[72:75], a[224:227]\n"
    "v_exp_f32_e64 v51, v51\n"
    "v_mfma_f32_16x16x32_bf16 a[224:227], a[132:135], v[8:11], a[224:227]\n"
    "v_fma_f32 v53, v53, s52, v85\n"
    "v_fma_f32 v55, v55, s52, v85\n"
    "v_mfma_f32_16x16x32_bf16 a[192:195], a[128:131], v[76:79], a[192:195]\n"
    "ds_read_b64_tr_b16 a[128:129], v90 offset:3072\n"
    "ds_read_b64_tr_b16 a[130:131], v90 offset:11264\n"
    "v_mfma_f32_16x16x32_bf16 a[192:195], a[132:135], v[12:15], a[192:195]\n"
    "s_waitcnt lgkmcnt(10)\n"
    "ds_read_b64_tr_b16 a[132:133], v90 offset:19456\n"
    "ds_read_b64_tr_b16 a[134:135], v90 offset:27648\n"
    "v_mfma_f32_16x16x32_bf16 v[132:135], a[136:139], v[64:67], v[132:135]\n"
    "v_exp_f32 v53, v53\n"
    "v_mfma_f32_16x16x32_bf16 v[132:135], a[140:143], v[0:3], v[132:135]\n"
    "v_exp_f32_e64 v55, v55\n"
    "v_mfma_f32_16x16x32_bf16 v[196:199], a[136:139], v[68:71], v[196:199]\n"
    "v_fma_f32 v57, v57, s52, v86\n"
    "v_fma_f32 v59, v59, s52, v86\n"
    "v_mfma_f32_16x16x32_bf16 v[196:199], a[140:143], v[4:7], v[196:199]\n"
    "v_exp_f32_e64 v57, v57\n"
    "v_mfma_f32_16x16x32_bf16 a[228:231], a[136:139], v[72:75], a[228:231]\n"
    "v_exp_f32_e64 v59, v59\n"
    "v_mfma_f32_16x16x32_bf16 a[228:231], a[140:143], v[8:11], a[228:231]\n"
    "v_fma_f32 v61, v61, s52, v87\n"
    "v_fma_f32 v63, v63, s52, v87\n"
    "v_mfma_f32_16x16x32_bf16 a[196:199], a[136:139], v[76:79], a[196:199]\n"
    "ds_read_b64_tr_b16 a[136:137], v90 offset:3080\n"
    "ds_read_b64_tr_b16 a[138:139], v90 offset:11272\n"
    "v_mfma_f32_16x16x32_bf16 a[196:199], a[140:143], v[12:15], a[196:199]\n"
    "s_waitcnt lgkmcnt(10)\n"
    "ds_read_b64_tr_b16 a[140:141], v90 offset:19464\n"
    "ds_read_b64_tr_b16 a[142:143], v90 offset:27656\n"
    "v_mfma_f32_16x16x32_bf16 v[136:139], a[144:147], v[64:67], v[136:139]\n"
    "v_exp_f32 v61, v61\n"
    "v_mfma_f32_16x16x32_bf16 v[136:139], a[148:151], v[0:3], v[136:139]\n"
    "v_exp_f32 v63, v63\n"
    "v_mfma_f32_16x16x32_bf16 v[200:203], a[144:147], v[68:71], v[200:203]\n"
    "v_add_f32 v80, v80, v48\n"
    "v_add_f32_e64 v81, v81, v52\n"
    "v_mfma_f32_16x16x32_bf16 v[200:203], a[148:151], v[4:7], v[200:203]\n"
    "v_add_f32 v82, v82, v56\n"
    "v_add_f32 v83, v83, v60\n"
    "v_mfma_f32_16x16x32_bf16 a[232:235], a[144:147], v[72:75], a[232:235]\n"
    "v_add_f32 v80, v80, v49\n"
    "v_add_f32 v81, v81, v53\n"
    "v_mfma_f32_16x16x32_bf16 a[232:235], a[148:151], v[8:11], a[232:235]\n"
    "v_add_f32_e64 v82, v82, v57\n"
    "v_add_f32_e64 v83, v83, v61\n"
    "v_mfma_f32_16x16x32_bf16 a[200:203], a[144:147], v[76:79], a[200:203]\n"
    "s_nop 0\n"
    "ds_read_b64_tr_b16 a[144:145], v91 offset:3072\n"
    "ds_read_b64_tr_b16 a[146:147], v91 offset:11264\n"
    "v_mfma_f32_16x16x32_bf16 a[200:203], a[148:151], v[12:15], a[200:203]\n"
    "s_waitcnt lgkmcnt(10)\n"
    "ds_read_b64_tr_b16 a[148:149], v91 offset:19456\n"
    "ds_read_b64_tr_b16 a[150:151], v91 offset:27648\n"
    "v_mfma_f32_16x16x32_bf16 v[140:143], a[152:155], v[64:67], v[140:143]\n"
    "v_add_u32 v93, 2048, v93\n"
    "v_add_f32 v80, v80, v50\n"
    "v_mfma_f32_16x16x32_bf16 v[140:143], a[156:159], v[0:3], v[140:143]\n"
    "v_add_f32 v81, v81, v54\n"
    "v_add_f32_e64 v82, v82, v58\n"
    "v_mfma_f32_16x16x32_bf16 v[204:207], a[152:155], v[68:71], v[204:207]\n"
    "v_add_f32_e64 v83, v83, v62\n"
    "v_add_f32_e64 v80, v80, v51\n"
    "v_mfma_f32_16x16x32_bf16 v[204:207], a[156:159], v[4:7], v[204:207]\n"
    "v_add_f32_e64 v81, v81, v55\n"
    "v_add_f32_e64 v82, v82, v59\n"
    "v_mfma_f32_16x16x32_bf16 a[236:239], a[152:155], v[72:75], a[236:239]\n"
    "v_add_f32_e64 v83, v83, v63\n"
    "v_mfma_f32_16x16x32_bf16 a[236:239], a[156:159], v[8:11], a[236:239]\n"
    "v_mfma_f32_16x16x32_bf16 a[204:207], a[152:155], v[76:79], a[204:207]\n"
    "ds_read_b64_tr_b16 a[152:153], v91 offset:3080\n"
    "ds_read_b64_tr_b16 a[154:155], v91 offset:11272\n"
    "v_mfma_f32_16x16x32_bf16 a[204:207], a[156:159], v[12:15], a[204:207]\n"
    "s_waitcnt lgkmcnt(10)\n"
    "ds_read_b64_tr_b16 a[156:157], v91 offset:19464\n"
    "ds_read_b64_tr_b16 a[158:159], v91 offset:27656\n"
    "v_mfma_f32_16x16x32_bf16 v[144:147], a[128:131], v[64:67], v[144:147]\n"
    "s_nop 0\n"
    "v_mfma_f32_16x16x32_bf16 v[144:147], a[132:135], v[0:3], v[144:147]\n"
    "v_mfma_f32_16x16x32_bf16 v[208:211], a[128:131], v[68:71], v[208:211]\n"
    "v_mfma_f32_16x16x32_bf16 v[208:211], a[132:135], v[4:7], v[208:211]\n"
    "v_mfma_f32_16x16x32_bf16 a[240:243], a[128:131], v[72:75], a[240:243]\n"
    "v_mfma_f32_16x16x32_bf16 a[240:243], a[132:135], v[8:11], a[240:243]\n"
    "v_mfma_f32_16x16x32_bf16 a[208:211], a[128:131], v[76:79], a[208:211]\n"
    "s_waitcnt vmcnt(12) lgkmcnt(0)\n"
    "v_mfma_f32_16x16x32_bf16 a[208:211], a[132:135], v[12:15], a[208:211]\n"
    "s_barrier\n"
    "v_mfma_f32_16x16x32_bf16 v[148:151], a[136:139], v[64:67], v[148:151]\n"
    "ds_read_b128 a[128:131], v88 offset:0\n"
    "ds_read_b128 a[132:135], v89 offset:0\n"
    "v_mfma_f32_16x16x32_bf16 v[148:151], a[140:143], v[0:3], v[148:151]\n"
    "v_mfma_f32_16x16x32_bf16 v[212:215], a[136:139], v[68:71], v[212:215]\n"
    "v_mfma_f32_16x16x32_bf16 v[212:215], a[140:143], v[4:7], v[212:215]\n"
    "v_mfma_f32_16x16x32_bf16 a[244:247], a[136:139], v[72:75], a[244:247]\n"
    "v_mfma_f32_16x16x32_bf16 a[244:247], a[140:143], v[8:11], a[244:247]\n"
    "v_mfma_f32_16x16x32_bf16 a[212:215], a[136:139], v[76:79], a[212:215]\n"
    "ds_read_b128 a[136:139], v88 offset:1024\n"
    "v_mfma_f32_16x16x32_bf16 a[212:215], a[140:143], v[12:15], a[212:215]\n"
    "ds_read_b128 a[140:143], v89 offset:1024\n"
    "v_mfma_f32_16x16x32_bf16 v[152:155], a[144:147], v[64:67], v[152:155]\n"
    "v_mfma_f32_16x16x32_bf16 v[152:155], a[148:151], v[0:3], v[152:155]\n"
    "v_mfma_f32_16x16x32_bf16 v[216:219], a[144:147], v[68:71], v[216:219]\n"
    "v_mfma_f32_16x16x32_bf16 v[216:219], a[148:151], v[4:7], v[216:219]\n"
    "v_mfma_f32_16x16x32_bf16 a[248:251], a[144:147], v[72:75], a[248:251]\n"
    "v_mfma_f32_16x16x32_bf16 a[248:251], a[148:151], v[8:11], a[248:251]\n"
    "v_mfma_f32_16x16x32_bf16 a[216:219], a[144:147], v[76:79], a[216:219]\n"
    "ds_read_b128 a[144:147], v88 offset:2048\n"
    "v_mfma_f32_16x16x32_bf16 a[216:219], a[148:151], v[12:15], a[216:219]\n"
    "ds_read_b128 a[148:151], v89 offset:2048\n"
    "v_mfma_f32_16x16x32_bf16 v[156:159], a[152:155], v[64:67], v[156:159]\n"
    "v_mfma_f32_16x16x32_bf16 v[156:159], a[156:159], v[0:3], v[156:159]\n"
    "v_mfma_f32_16x16x32_bf16 v[220:223], a[152:155], v[68:71], v[220:223]\n"
    "v_mfma_f32_16x16x32_bf16 v[220:223], a[156:159], v[4:7], v[220:223]\n"
    "v_mfma_f32_16x16x32_bf16 a[252:255], a[152:155], v[72:75], a[252:255]\n"
    "v_mfma_f32_16x16x32_bf16 a[252:255], a[156:159], v[8:11], a[252:255]\n"
    "v_mfma_f32_16x16x32_bf16 a[220:223], a[152:155], v[76:79], a[220:223]\n"
    "s_sub_u32 s65, s65, 1\n"
    "ds_read_b128 a[152:155], v88 offset:3072\n"
    "v_mfma_f32_16x16x32_bf16 a[220:223], a[156:159], v[12:15], a[220:223]\n"
    "s_waitcnt lgkmcnt(5)\n"
    "ds_read_b128 a[156:159], v89 offset:3072\n"
    "s_cbranch_scc0 LQ4_%=\n"
    "v_mfma_f32_16x16x32_bf16 v[0:3], a[128:131], a[0:3], 0\n"
    "s_mov_b32 m0, s61\n"
    "v_cvt_pk_bf16_f32 v64, v32, v33\n"
    "v_cvt_pk_bf16_f32 v65, v34, v35\n"
    "v_mfma_f32_16x16x32_bf16 v[0:3], a[132:135], a[4:7], v[0:3]\n"
    "buffer_load_dwordx4 v92, s[40:43], s55 offen offset:0 lds\n"
    "v_mfma_f32_16x16x32_bf16 v[4:7], a[128:131], a[32:35], 0\n"
    "s_add_u32 m0, m0, s97\n"
    "v_cvt_pk_bf16_f32 v66, v48, v49\n"
    "v_cvt_pk_bf16_f32 v67, v50, v51\n"
    "v_mfma_f32_16x16x32_bf16 v[4:7], a[132:135], a[36:39], v[4:7]\n"
    "s_sub_u32 s64, s60, s61\n"
    "v_cvt_pk_bf16_f32 v68, v36, v37\n"
    "v_cvt_pk_bf16_f32 v69, v38, v39\n"
    "v_mfma_f32_16x16x32_bf16 v[8:11], a[128:131], a[64:67], 0\n"
    "s_mov_b32 s71, s61\n"
    "v_add_u32 v90, s64, v90\n"
    "v_add_u32 v91, s64, v91\n"
    "v_mfma_f32_16x16x32_bf16 v[8:11], a[132:135], a[68:71], v[8:11]\n"
    "buffer_load_dwordx4 v92, s[40:43], s55 offen offset:128 lds\n"
    "v_mfma_f32_16x16x32_bf16 v[12:15], a[128:131], a[96:99], 0\n"
    "s_add_u32 m0, m0, s97\n"
    "ds_read_b128 a[128:131], v88 offset:8192\n"
    "v_cvt_pk_bf16_f32 v70, v52, v53\n"
    "v_mfma_f32_16x16x32_bf16 v[12:15], a[132:135], a[100:103], v[12:15]\n"
    "s_waitcnt lgkmcnt(5)\n"
    "ds_read_b128 a[132:135], v89 offset:8192\n"
    "v_cvt_pk_bf16_f32 v71, v54, v55\n"
    "v_mfma_f32_16x16x32_bf16 v[0:3], a[136:139], a[8:11], v[0:3]\n"
    "s_mov_b32 s61, s60\n"
    "v_cvt_pk_bf16_f32 v72, v40, v41\n"
    "v_cvt_pk_bf16_f32 v73, v42, v43\n"
    "v_mfma_f32_16x16x32_bf16 v[0:3], a[140:143], a[12:15], v[0:3]\n"
    "buffer_load_dwordx4 v92, s[40:43], s55 offen offset:256 lds\n"
    "v_mfma_f32_16x16x32_bf16 v[4:7], a[136:139], a[40:43], v[4:7]\n"
    "s_add_u32 m0, m0, 896\n"
    "v_cvt_pk_bf16_f32 v74, v56, v57\n"
    "v_cvt_pk_bf16_f32 v75, v58, v59\n"
    "v_mfma_f32_16x16x32_bf16 v[4:7], a[140:143], a[44:47], v[4:7]\n"
    "s_mov_b32 s60, s63\n"
    "v_cvt_pk_bf16_f32 v76, v44, v45\n"
    "v_cvt_pk_bf16_f32 v77, v46, v47\n"
    "v_mfma_f32_16x16x32_bf16 v[8:11], a[136:139], a[72:75], v[8:11]\n"
    "s_mov_b32 s63, s71\n"
    "v_cvt_pk_bf16_f32 v78, v60, v61\n"
    "v_cvt_pk_bf16_f32 v79, v62, v63\n"
    "v_mfma_f32_16x16x32_bf16 v[8:11], a[140:143], a[76:79], v[8:11]\n"
    "buffer_load_dwordx4 v92, s[40:43], s55 offen offset:384 lds\n"
    "v_mfma_f32_16x16x32_bf16 v[12:15], a[136:139], a[104:107], v[12:15]\n"
    "s_add_u32 m0, m0, 1408\n"
    "ds_read_b128 a[136:139], v88 offset:9216\n"
    "v_add_u32 v93, 0xffffc000, v93\n"
    "v_mfma_f32_16x16x32_bf16 v[12:15], a[140:143], a[108:111], v[12:15]\n"
    "s_waitcnt lgkmcnt(5)\n"
    "ds_read_b128 a[140:143], v89 offset:9216\n"
    "v_mfma_f32_16x16x32_bf16 v[0:3], a[144:147], a[16:19], v[0:3]\n"
    "s_sub_u32 s62, s63, s60\n"
    "v_mfma_f32_16x16x32_bf16 v[0:3], a[148:151], a[20:23], v[0:3]\n"
    "buffer_load_dwordx4 v92, s[40:43], s56 offen offset:0 lds\n"
    "v_mfma_f32_16x16x32_bf16 v[4:7], a[144:147], a[48:51], v[4:7]\n"
    "s_add_u32 m0, m0, 896\n"
    "v_mfma_f32_16x16x32_bf16 v[4:7], a[148:151], a[52:55], v[4:7]\n"
    "v_mfma_f32_16x16x32_bf16 v[8:11], a[144:147], a[80:83], v[8:11]\n"
    "v_mfma_f32_16x16x32_bf16 v[8:11], a[148:151], a[84:87], v[8:11]\n"
    "buffer_load_dwordx4 v92, s[40:43], s56 offen offset:128 lds\n"
    "v_mfma_f32_16x16x32_bf16 v[12:15], a[144:147], a[112:115], v[12:15]\n"
    "s_add_u32 m0, m0, 896\n"
    "ds_read_b128 a[144:147], v88 offset:10240\n"
    "v_mfma_f32_16x16x32_bf16 v[12:15], a[148:151], a[116:119], v[12:15]\n"
    "s_waitcnt lgkmcnt(5)\n"
    "ds_read_b128 a[148:151], v89 offset:10240\n"
    "v_mfma_f32_16x16x32_bf16 v[0:3], a[152:155], a[24:27], v[0:3]\n"
    "v_mfma_f32_16x16x32_bf16 v[0:3], a[156:159], a[28:31], v[0:3]\n"
    "s_nop 0\n"
    "buffer_load_dwordx4 v92, s[40:43], s56 offen offset:256 lds\n"
    "v_mfma_f32_16x16x32_bf16 v[4:7], a[152:155], a[56:59], v[4:7]\n"
    "s_add_u32 m0, m0, 896\n"
    "v_mfma_f32_16x16x32_bf16 v[4:7], a[156:159], a[60:63], v[4:7]\n"
    "v_mfma_f32_16x16x32_bf16 v[8:11], a[152:155], a[88:91], v[8:11]\n"
    "v_mfma_f32_16x16x32_bf16 v[8:11], a[156:159], a[92:95], v[8:11]\n"
    "buffer_load_dwordx4 v92, s[40:43], s56 offen offset:384 lds\n"
    "v_mfma_f32_16x16x32_bf16 v[12:15], a[152:155], a[120:123], v[12:15]\n"
    "s_add_u32 s55, s55, s96\n"
    "ds_read_b128 a[152:155], v88 offset:11264\n"
    "v_fma_f32 v0, v0, s52, v84\n"
    "v_mfma_f32_16x16x32_bf16 v[12:15], a[156:159], a[124:127], v[12:15]\n"
    "s_waitcnt lgkmcnt(5)\n"
    "ds_read_b128 a[156:159], v89 offset:11264\n"
    "v_fma_f32 v2, v2, s52, v84\n"
    "v_mfma_f32_16x16x32_bf16 v[16:19], a[128:131], a[0:3], 0\n"
    "s_add_u32 s56, s56, s96\n"
    "v_exp_f32 v0, v0\n"
    "v_mfma_f32_16x16x32_bf16 v[16:19], a[132:135], a[4:7], v[16:19]\n"
    "s_mov_b32 m0, s59\n"
    "v_exp_f32 v2, v2\n"
    "v_mfma_f32_16x16x32_bf16 v[20:23], a[128:131], a[32:35], 0\n"
    "buffer_load_dwordx4 v92, s[44:47], s57 offen offset:0 nt lds\n"
    "v_mfma_f32_16x16x32_bf16 v[20:23], a[132:135], a[36:39], v[20:23]\n"
    "s_add_u32 m0, m0, 896\n"
    "v_fma_f32 v4, v4, s52, v85\n"
    "v_fma_f32 v6, v6, s52, v85\n"
    "v_mfma_f32_16x16x32_bf16 v[24:27], a[128:131], a[64:67], 0\n"
    "s_mov_b32 s50, 434176\n"
    "v_exp_f32_e64 v4, v4\n"
    "v_mfma_f32_16x16x32_bf16 v[24:27], a[132:135], a[68:71], v[24:27]\n"
    "buffer_load_dwordx4 v92, s[44:47], s57 offen offset:128 nt lds\n"
    "v_mfma_f32_16x16x32_bf16 v[28:31], a[128:131], a[96:99], 0\n"
    "s_add_u32 s57, s57, s58\n"
    "ds_read_b128 a[128:131], v88 offset:16384\n"
    "v_fma_f32 v8, v8, s52, v86\n"
    "v_mfma_f32_16x16x32_bf16 v[28:31], a[132:135], a[100:103], v[28:31]\n"
    "s_waitcnt lgkmcnt(5)\n"
    "ds_read_b128 a[132:135], v89 offset:16384\n"
    "v_fma_f32 v10, v10, s52, v86\n"
    "v_mfma_f32_16x16x32_bf16 v[16:19], a[136:139], a[8:11], v[16:19]\n"
    "s_xor_b32 s58, s58, 0xe00\n"
    "v_exp_f32_e64 v8, v8\n"
    "v_mfma_f32_16x16x32_bf16 v[16:19], a[140:143], a[12:15], v[16:19]\n"
    "s_add_u32 s59, s59, 0x800\n"
    "v_exp_f32_e64 v6, v6\n"
    "v_mfma_f32_16x16x32_bf16 v[20:23], a[136:139], a[40:43], v[20:23]\n"
    "s_add_u32 s48, s48, 434176\n"
    "v_exp_f32 v10, v10\n"
    "v_mfma_f32_16x16x32_bf16 v[20:23], a[140:143], a[44:47], v[20:23]\n"
    "s_addc_u32 s49, s49, 0\n"
    "v_fma_f32 v12, v12, s52, v87\n"
    "v_fma_f32 v14, v14, s52, v87\n"
    "v_mfma_f32_16x16x32_bf16 v[24:27], a[136:139], a[72:75], v[24:27]\n"
    "s_add_u32 s66, s66, 1\n"
    "v_exp_f32 v12, v12\n"
    "v_mfma_f32_16x16x32_bf16 v[24:27], a[140:143], a[76:79], v[24:27]\n"
    "v_exp_f32_e64 v14, v14\n"
    "v_mfma_f32_16x16x32_bf16 v[28:31], a[136:139], a[104:107], v[28:31]\n"
    "ds_read_b128 a[136:139], v88 offset:17408\n"
    "v_fma_f32 v1, v1, s52, v84\n"
    "v_mfma_f32_16x16x32_bf16 v[28:31], a[140:143], a[108:111], v[28:31]\n"
    "s_waitcnt lgkmcnt(5)\n"
    "ds_read_b128 a[140:143], v89 offset:17408\n"
    "v_fma_f32 v3, v3, s52, v84\n"
    "v_mfma_f32_16x16x32_bf16 v[16:19], a[144:147], a[16:19], v[16:19]\n"
    "v_exp_f32 v1, v1\n"
    "v_mfma_f32_16x16x32_bf16 v[16:19], a[148:151], a[20:23], v[16:19]\n"
    "v_exp_f32_e64 v3, v3\n"
    "v_mfma_f32_16x16x32_bf16 v[20:23], a[144:147], a[48:51], v[20:23]\n"
    "v_fma_f32 v5, v5, s52, v85\n"
    "v_fma_f32 v7, v7, s52, v85\n"
    "v_mfma_f32_16x16x32_bf16 v[20:23], a[148:151], a[52:55], v[20:23]\n"
    "v_exp_f32_e64 v5, v5\n"
    "v_mfma_f32_16x16x32_bf16 v[24:27], a[144:147], a[80:83], v[24:27]\n"
    "v_exp_f32_e64 v7, v7\n"
    "v_mfma_f32_16x16x32_bf16 v[24:27], a[148:151], a[84:87], v[24:27]\n"
    "v_fma_f32 v9, v9, s52, v86\n"
    "v_fma_f32 v11, v11, s52, v86\n"
    "v_mfma_f32_16x16x32_bf16 v[28:31], a[144:147], a[112:115], v[28:31]\n"
    "ds_read_b128 a[144:147], v88 offset:18432\n"
    "v_fma_f32 v13, v13, s52, v87\n"
    "v_mfma_f32_16x16x32_bf16 v[28:31], a[148:151], a[116:119], v[28:31]\n"
    "s_waitcnt lgkmcnt(5)\n"
    "ds_read_b128 a[148:151], v89 offset:18432\n"
    "v_fma_f32 v15, v15, s52, v87\n"
    "v_mfma_f32_16x16x32_bf16 v[16:19], a[152:155], a[24:27], v[16:19]\n"
    "v_exp_f32 v9, v9\n"
    "v_mfma_f32_16x16x32_bf16 v[16:19], a[156:159], a[28:31], v[16:19]\n"
    "v_exp_f32 v13, v13\n"
    "v_mfma_f32_16x16x32_bf16 v[20:23], a[152:155], a[56:59], v[20:23]\n"
    "v_exp_f32 v11, v11\n"
    "v_mfma_f32_16x16x32_bf16 v[20:23], a[156:159], a[60:63], v[20:23]\n"
    "v_exp_f32 v15, v15\n"
    "v_mfma_f32_16x16x32_bf16 v[24:27], a[152:155], a[88:91], v[24:27]\n"
    "v_add_f32 v80, v80, v0\n"
    "v_add_f32 v81, v81, v4\n"
    "v_mfma_f32_16x16x32_bf16 v[24:27], a[156:159], a[92:95], v[24:27]\n"
    "v_add_f32 v82, v82, v8\n"
    "v_add_f32 v83, v83, v12\n"
    "v_mfma_f32_16x16x32_bf16 v[28:31], a[152:155], a[120:123], v[28:31]\n"
    "ds_read_b128 a[152:155], v88 offset:19456\n"
    "v_add_f32 v80, v80, v1\n"
    "v_mfma_f32_16x16x32_bf16 v[28:31], a[156:159], a[124:127], v[28:31]\n"
    "s_waitcnt lgkmcnt(5)\n"
    "ds_read_b128 a[156:159], v89 offset:19456\n"
    "v_add_f32 v81, v81, v5\n"
    "v_mfma_f32_16x16x32_bf16 v[32:35], a[128:131], a[0:3], 0\n"
    "v_add_f32 v82, v82, v9\n"
    "v_add_f32 v83, v83, v13\n"
    "v_mfma_f32_16x16x32_bf16 v[32:35], a[132:135], a[4:7], v[32:35]\n"
    "v_add_f32 v80, v80, v2\n"
    "v_add_f32 v81, v81, v6\n"
    "v_mfma_f32_16x16x32_bf16 v[36:39], a[128:131], a[32:35], 0\n"
    "v_add_f32 v82, v82, v10\n"
    "v_add_f32 v83, v83, v14\n"
    "v_mfma_f32_16x16x32_bf16 v[36:39], a[132:135], a[36:39], v[36:39]\n"
    "v_add_f32 v80, v80, v3\n"
    "v_add_f32 v81, v81, v7\n"
    "v_mfma_f32_16x16x32_bf16 v[40:43], a[128:131], a[64:67], 0\n"
    "v_add_f32_e64 v82, v82, v11\n"
    "v_add_f32_e64 v83, v83, v15\n"
    "v_mfma_f32_16x16x32_bf16 v[40:43], a[132:135], a[68:71], v[40:43]\n"
    "v_cvt_pk_bf16_f32 v0, v0, v1\n"
    "v_cvt_pk_bf16_f32 v1, v2, v3\n"
    "v_mfma_f32_16x16x32_bf16 v[44:47], a[128:131], a[96:99], 0\n"
    "ds_read_b128 a[128:131], v88 offset:24576\n"
    "v_cvt_pk_bf16_f32 v4, v4, v5\n"
    "v_mfma_f32_16x16x32_bf16 v[44:47], a[132:135], a[100:103], v[44:47]\n"
    "s_waitcnt lgkmcnt(5)\n"
    "ds_read_b128 a[132:135], v89 offset:24576\n"
    "v_cvt_pk_bf16_f32 v5, v6, v7\n"
    "v_mfma_f32_16x16x32_bf16 v[32:35], a[136:139], a[8:11], v[32:35]\n"
    "s_nop 0\n"
    "v_cvt_pk_bf16_f32 v8, v8, v9\n"
    "v_cvt_pk_bf16_f32 v9, v10, v11\n"
    "v_mfma_f32_16x16x32_bf16 v[32:35], a[140:143], a[12:15], v[32:35]\n"
    "v_cvt_pk_bf16_f32 v12, v12, v13\n"
    "v_cvt_pk_bf16_f32 v13, v14, v15\n"
    "v_mfma_f32_16x16x32_bf16 v[36:39], a[136:139], a[40:43], v[36:39]\n"
    "v_fma_f32 v16, v16, s52, v84\n"
    "v_fma_f32 v18, v18, s52, v84\n"
    "v_mfma_f32_16x16x32_bf16 v[36:39], a[140:143], a[44:47], v[36:39]\n"
    "v_exp_f32_e64 v16, v16\n"
    "v_mfma_f32_16x16x32_bf16 v[40:43], a[136:139], a[72:75], v[40:43]\n"
    "v_exp_f32_e64 v18, v18\n"
    "v_mfma_f32_16x16x32_bf16 v[40:43], a[140:143], a[76:79], v[40:43]\n"
    "v_fma_f32 v20, v20, s52, v85\n"
    "v_fma_f32 v22, v22, s52, v85\n"
    "v_mfma_f32_16x16x32_bf16 v[44:47], a[136:139], a[104:107], v[44:47]\n"
    "s_nop 0\n"
    "ds_read_b128 a[136:139], v88 offset:25600\n"
    "v_fma_f32 v24, v24, s52, v86\n"
    "v_mfma_f32_16x16x32_bf16 v[44:47], a[140:143], a[108:111], v[44:47]\n"
    "s_waitcnt lgkmcnt(5)\n"
    "ds_read_b128 a[140:143], v89 offset:25600\n"
    "v_fma_f32 v26, v26, s52, v86\n"
    "v_mfma_f32_16x16x32_bf16 v[32:35], a[144:147], a[16:19], v[32:35]\n"
    "v_exp_f32_e64 v20, v20\n"
    "v_mfma_f32_16x16x32_bf16 v[32:35], a[148:151], a[20:23], v[32:35]\n"
    "v_exp_f32_e64 v24, v24\n"
    "v_mfma_f32_16x16x32_bf16 v[36:39], a[144:147], a[48:51], v[36:39]\n"
    "v_exp_f32_e64 v22, v22\n"
    "v_mfma_f32_16x16x32_bf16 v[36:39], a[148:151], a[52:55], v[36:39]\n"
    "v_exp_f32_e64 v26, v26\n"
    "v_mfma_f32_16x16x32_bf16 v[40:43], a[144:147], a[80:83], v[40:43]\n"
    "v_fma_f32 v28, v28, s52, v87\n"
    "v_fma_f32 v30, v30, s52, v87\n"
    "v_mfma_f32_16x16x32_bf16 v[40:43], a[148:151], a[84:87], v[40:43]\n"
    "v_exp_f32_e64 v28, v28\n"
    "v_mfma_f32_16x16x32_bf16 v[44:47], a[144:147], a[112:115], v[44:47]\n"
    "s_nop 0\n"
    "ds_read_b128 a[144:147], v88 offset:26624\n"
    "v_fma_f32 v17, v17, s52, v84\n"
    "v_mfma_f32_16x16x32_bf16 v[44:47], a[148:151], a[116:119], v[44:47]\n"
    "s_waitcnt lgkmcnt(5)\n"
    "ds_read_b128 a[148:151], v89 offset:26624\n"
    "v_fma_f32 v19, v19, s52, v84\n"
    "v_mfma_f32_16x16x32_bf16 v[32:35], a[152:155], a[24:27], v[32:35]\n"
    "v_exp_f32_e64 v17, v17\n"
    "v_mfma_f32_16x16x32_bf16 v[32:35], a[156:159], a[28:31], v[32:35]\n"
    "v_exp_f32_e64 v30, v30\n"
    "v_mfma_f32_16x16x32_bf16 v[36:39], a[152:155], a[56:59], v[36:39]\n"
    "v_exp_f32_e64 v19, v19\n"
    "v_mfma_f32_16x16x32_bf16 v[36:39], a[156:159], a[60:63], v[36:39]\n"
    "v_fma_f32 v21, v21, s52, v85\n"
    "v_fma_f32 v23, v23, s52, v85\n"
    "v_mfma_f32_16x16x32_bf16 v[40:43], a[152:155], a[88:91], v[40:43]\n"
    "v_exp_f32_e64 v21, v21\n"
    "v_mfma_f32_16x16x32_bf16 v[40:43], a[156:159], a[92:95], v[40:43]\n"
    "v_exp_f32_e64 v23, v23\n"
    "v_mfma_f32_16x16x32_bf16 v[44:47], a[152:155], a[120:123], v[44:47]\n"
    "s_nop 0\n"
    "ds_read_b128 a[152:155], v88 offset:27648\n"
    "v_fma_f32 v25, v25, s52, v86\n"
    "v_mfma_f32_16x16x32_bf16 v[44:47], a[156:159], a[124:127], v[44:47]\n"
    "s_waitcnt lgkmcnt(5)\n"
    "ds_read_b128 a[156:159], v89 offset:27648\n"
    "v_fma_f32 v27, v27, s52, v86\n"
    "v_mfma_f32_16x16x32_bf16 v[48:51], a[128:131], a[0:3], 0\n"
    "v_exp_f32_e64 v25, v25\n"
    "v_mfma_f32_16x16x32_bf16 v[48:51], a[132:135], a[4:7], v[48:51]\n"
    "v_exp_f32_e64 v27, v27\n"
    "v_mfma_f32_16x16x32_bf16 v[52:55], a[128:131], a[32:35], 0\n"
    "v_fma_f32 v29, v29, s52, v87\n"
    "v_fma_f32 v31, v31, s52, v87\n"
    "v_mfma_f32_16x16x32_bf16 v[52:55], a[132:135], a[36:39], v[52:55]\n"
    "v_exp_f32 v29, v29\n"
    "v_mfma_f32_16x16x32_bf16 v[56:59], a[128:131], a[64:67], 0\n"
    "v_exp_f32 v31, v31\n"
    "v_mfma_f32_16x16x32_bf16 v[56:59], a[132:135], a[68:71], v[56:59]\n"
    "v_add_f32 v80, v80, v16\n"
    "v_add_f32_e64 v81, v81, v20\n"
    "v_mfma_f32_16x16x32_bf16 v[60:63], a[128:131], a[96:99], 0\n"
    "s_waitcnt vmcnt(12) lgkmcnt(0)\n"
    "v_add_f32 v82, v82, v24\n"
    "v_add_f32_e64 v83, v83, v28\n"
    "v_mfma_f32_16x16x32_bf16 v[60:63], a[132:135], a[100:103], v[60:63]\n"
    "s_barrier\n"
    "v_mfma_f32_16x16x32_bf16 v[48:51], a[136:139], a[8:11], v[48:51]\n"
    "s_mov_b32 m0, s60\n"
    "ds_read_b64_tr_b16 a[128:129], v90 offset:0\n"
    "ds_read_b64_tr_b16 a[130:131], v90 offset:8192\n"
    "v_mfma_f32_16x16x32_bf16 v[48:51], a[140:143], a[12:15], v[48:51]\n"
    "s_sub_u32 s64, s63, s61\n"
    "ds_read_b64_tr_b16 a[132:133], v90 offset:16384\n"
    "ds_read_b64_tr_b16 a[134:135], v90 offset:24576\n"
    "v_mfma_f32_16x16x32_bf16 v[52:55], a[136:139], a[40:43], v[52:55]\n"
    "buffer_load_dwordx4 v92, s[36:39], s53 offen offset:0 lds\n"
    "v_mfma_f32_16x16x32_bf16 v[52:55], a[140:143], a[44:47], v[52:55]\n"
    "s_add_u32 m0, m0, s97\n"
    "v_add_f32 v80, v80, v17\n"
    "v_add_f32 v81, v81, v21\n"
    "v_mfma_f32_16x16x32_bf16 v[56:59], a[136:139], a[72:75], v[56:59]\n"
    "s_lshl_b32 s72, s69, 12\n"
    "v_add_f32 v82, v82, v25\n"
    "v_add_f32_e64 v83, v83, v29\n"
    "v_mfma_f32_16x16x32_bf16 v[56:59], a[140:143], a[76:79], v[56:59]\n"
    "s_sub_u32 s72, s63, s72\n"
    "v_add_f32_e64 v80, v80, v18\n"
    "v_add_f32 v81, v81, v22\n"
    "v_mfma_f32_16x16x32_bf16 v[60:63], a[136:139], a[104:107], v[60:63]\n"
    "s_mov_b32 s71, s61\n"
    "ds_read_b64_tr_b16 a[136:137], v90 offset:8\n"
    "ds_read_b64_tr_b16 a[138:139], v90 offset:8200\n"
    "v_mfma_f32_16x16x32_bf16 v[60:63], a[140:143], a[108:111], v[60:63]\n"
    "ds_read_b64_tr_b16 a[140:141], v90 offset:16392\n"
    "ds_read_b64_tr_b16 a[142:143], v90 offset:24584\n"
    "v_mfma_f32_16x16x32_bf16 v[48:51], a[144:147], a[16:19], v[48:51]\n"
    "buffer_load_dwordx4 v92, s[36:39], s53 offen offset:128 lds\n"
    "v_mfma_f32_16x16x32_bf16 v[48:51], a[148:151], a[20:23], v[48:51]\n"
    "s_add_u32 m0, m0, s97\n"
    "v_add_f32 v82, v82, v26\n"
    "v_add_f32_e64 v83, v83, v30\n"
    "v_mfma_f32_16x16x32_bf16 v[52:55], a[144:147], a[48:51], v[52:55]\n"
    "s_mov_b32 s61, s63\n"
    "v_add_f32 v80, v80, v19\n"
    "v_add_f32 v81, v81, v23\n"
    "v_mfma_f32_16x16x32_bf16 v[52:55], a[148:151], a[52:55], v[52:55]\n"
    "s_mov_b32 s63, s71\n"
    "v_add_f32 v82, v82, v27\n"
    "v_add_f32 v83, v83, v31\n"
    "v_mfma_f32_16x16x32_bf16 v[56:59], a[144:147], a[80:83], v[56:59]\n"
    "buffer_load_dwordx4 v92, s[36:39], s53 offen offset:256 lds\n"
    "v_mfma_f32_16x16x32_bf16 v[56:59], a[148:151], a[84:87], v[56:59]\n"
    "s_add_u32 m0, m0, s97\n"
    "v_cvt_pk_bf16_f32 v2, v16, v17\n"
    "v_cvt_pk_bf16_f32 v3, v18, v19\n"
    "v_mfma_f32_16x16x32_bf16 v[60:63], a[144:147], a[112:115], v[60:63]\n"
    "s_sub_u32 s62, s63, s60\n"
    "ds_read_b64_tr_b16 a[144:145], v91 offset:0\n"
    "ds_read_b64_tr_b16 a[146:147], v91 offset:8192\n"
    "v_mfma_f32_16x16x32_bf16 v[60:63], a[148:151], a[116:119], v[60:63]\n"
    "ds_read_b64_tr_b16 a[148:149], v91 offset:16384\n"
    "ds_read_b64_tr_b16 a[150:151], v91 offset:24576\n"
    "v_mfma_f32_16x16x32_bf16 v[48:51], a[152:155], a[24:27], v[48:51]\n"
    "buffer_load_dwordx4 v92, s[36:39], s53 offen offset:384 lds\n"
    "v_mfma_f32_16x16x32_bf16 v[48:51], a[156:159], a[28:31], v[48:51]\n"
    "s_add_u32 m0, m0, 1408\n"
    "v_cvt_pk_bf16_f32 v6, v20, v21\n"
    "v_cvt_pk_bf16_f32 v7, v22, v23\n"
    "v_mfma_f32_16x16x32_bf16 v[52:55], a[152:155], a[56:59], v[52:55]\n"
    "s_mov_b32 s57, s78\n"
    "v_cvt_pk_bf16_f32 v10, v24, v25\n"
    "v_cvt_pk_bf16_f32 v11, v26, v27\n"
    "v_mfma_f32_16x16x32_bf16 v[52:55], a[156:159], a[60:63], v[52:55]\n"
    "s_nop 0\n"
    "s_mov_b32 s58, 0x100\n"
    "v_cvt_pk_bf16_f32 v14, v28, v29\n"
    "v_cvt_pk_bf16_f32 v15, v30, v31\n"
    "v_mfma_f32_16x16x32_bf16 v[56:59], a[152:155], a[88:91], v[56:59]\n"
    "buffer_load_dwordx4 v92, s[36:39], s54 offen offset:0 lds\n"
    "v_mfma_f32_16x16x32_bf16 v[56:59], a[156:159], a[92:95], v[56:59]\n"
    "s_add_u32 m0, m0, 896\n"
    "v_fma_f32 v32, v32, s52, v84\n"
    "v_fma_f32 v34, v34, s52, v84\n"
    "v_mfma_f32_16x16x32_bf16 v[60:63], a[152:155], a[120:123], v[60:63]\n"
    "s_mov_b32 s59, s70\n"
    "ds_read_b64_tr_b16 a[152:153], v91 offset:8\n"
    "ds_read_b64_tr_b16 a[154:155], v91 offset:8200\n"
    "v_mfma_f32_16x16x32_bf16 v[60:63], a[156:159], a[124:127], v[60:63]\n"
    "s_waitcnt lgkmcnt(10)\n"
    "ds_read_b64_tr_b16 a[156:157], v91 offset:16392\n"
    "ds_read_b64_tr_b16 a[158:159], v91 offset:24584\n"
    "v_mfma_f32_16x16x32_bf16 v[96:99], a[128:131], v[64:67], v[96:99]\n"
    "buffer_load_dwordx4 v92, s[36:39], s54 offen offset:128 lds\n"
    "v_mfma_f32_16x16x32_bf16 v[96:99], a[132:135], v[0:3], v[96:99]\n"
    "s_add_u32 m0, m0, s97\n"
    "v_exp_f32 v32, v32\n"
    "v_mfma_f32_16x16x32_bf16 v[160:163], a[128:131], v[68:71], v[160:163]\n"
    "s_mov_b32 s98, s78\n"
    "v_exp_f32 v34, v34\n"
    "v_mfma_f32_16x16x32_bf16 v[160:163], a[132:135], v[4:7], v[160:163]\n"
    "s_mov_b32 s99, 0x100\n"
    "v_fma_f32 v36, v36, s52, v85\n"
    "v_fma_f32 v38, v38, s52, v85\n"
    "v_mfma_f32_16x16x32_bf16 v[224:227], a[128:131], v[72:75], v[224:227]\n"
    "buffer_load_dwordx4 v92, s[36:39], s54 offen offset:256 lds\n"
    "v_mfma_f32_16x16x32_bf16 v[224:227], a[132:135], v[8:11], v[224:227]\n"
    "s_add_u32 m0, m0, 896\n"
    "v_exp_f32_e64 v36, v36\n"
    "v_mfma_f32_16x16x32_bf16 a[160:163], a[128:131], v[76:79], a[160:163]\n"
    "s_mov_b32 s65, 5\n"
    "ds_read_b64_tr_b16 a[128:129], v90 offset:1024\n"
    "ds_read_b64_tr_b16 a[130:131], v90 offset:9216\n"
    "v_mfma_f32_16x16x32_bf16 a[160:163], a[132:135], v[12:15], a[160:163]\n"
    "s_waitcnt lgkmcnt(10)\n"
    "ds_read_b64_tr_b16 a[132:133], v90 offset:17408\n"
    "ds_read_b64_tr_b16 a[134:135], v90 offset:25600\n"
    "v_mfma_f32_16x16x32_bf16 v[100:103], a[136:139], v[64:67], v[100:103]\n"
    "buffer_load_dwordx4 v92, s[36:39], s54 offen offset:384 lds\n"
    "v_mfma_f32_16x16x32_bf16 v[100:103], a[140:143], v[0:3], v[100:103]\n"
    "s_add_u32 s53, s53, 0x8000\n"
    "v_exp_f32_e64 v38, v38\n"
    "v_mfma_f32_16x16x32_bf16 v[164:167], a[136:139], v[68:71], v[164:167]\n"
    "s_add_u32 s54, s54, s96\n"
    "v_fma_f32 v40, v40, s52, v86\n"
    "v_fma_f32 v42, v42, s52, v86\n"
    "v_mfma_f32_16x16x32_bf16 v[164:167], a[140:143], v[4:7], v[164:167]\n"
    "s_waitcnt vmcnt(8)\n"
    "ds_read_b128 a[0:3], v94 offset:0\n"
    "ds_read_b128 a[4:7], v95 offset:0\n"
    "v_mfma_f32_16x16x32_bf16 v[228:231], a[136:139], v[72:75], v[228:231]\n"
    "ds_read_b128 a[8:11], v94 offset:1024\n"
    "ds_read_b128 a[12:15], v95 offset:1024\n"
    "v_mfma_f32_16x16x32_bf16 v[228:231], a[140:143], v[8:11], v[228:231]\n"
    "ds_read_b128 a[16:19], v94 offset:2048\n"
    "ds_read_b128 a[20:23], v95 offset:2048\n"
    "v_mfma_f32_16x16x32_bf16 a[164:167], a[136:139], v[76:79], a[164:167]\n"
    "ds_read_b64_tr_b16 a[136:137], v90 offset:1032\n"
    "ds_read_b64_tr_b16 a[138:139], v90 offset:9224\n"
    "v_mfma_f32_16x16x32_bf16 a[164:167], a[140:143], v[12:15], a[164:167]\n"
    "s_waitcnt lgkmcnt(15)\n"
    "ds_read_b64_tr_b16 a[140:141], v90 offset:17416\n"
    "ds_read_b64_tr_b16 a[142:143], v90 offset:25608\n"
    "v_mfma_f32_16x16x32_bf16 v[104:107], a[144:147], v[64:67], v[104:107]\n"
    "s_nop 0\n"
    "ds_read_b128 a[24:27], v94 offset:3072\n"
    "ds_read_b128 a[28:31], v95 offset:3072\n"
    "v_mfma_f32_16x16x32_bf16 v[104:107], a[148:151], v[0:3], v[104:107]\n"
    "ds_read_b128 a[32:35], v94 offset:8192\n"
    "ds_read_b128 a[36:39], v95 offset:8192\n"
    "v_mfma_f32_16x16x32_bf16 v[168:171], a[144:147], v[68:71], v[168:171]\n"
    "ds_read_b128 a[40:43], v94 offset:9216\n"
    "ds_read_b128 a[44:47], v95 offset:9216\n"
    "v_mfma_f32_16x16x32_bf16 v[168:171], a[148:151], v[4:7], v[168:171]\n"
    "ds_read_b128 a[48:51], v94 offset:10240\n"
    "ds_read_b128 a[52:55], v95 offset:10240\n"
    "v_mfma_f32_16x16x32_bf16 v[232:235], a[144:147], v[72:75], v[232:235]\n"
    "ds_read_b128 a[56:59], v94 offset:11264\n"
    "ds_read_b128 a[60:63], v95 offset:11264\n"
    "v_mfma_f32_16x16x32_bf16 v[232:235], a[148:151], v[8:11], v[232:235]\n"
    "v_exp_f32_e64 v40, v40\n"
    "v_mfma_f32_16x16x32_bf16 a[168:171], a[144:147], v[76:79], a[168:171]\n"
    "ds_read_b64_tr_b16 a[144:145], v91 offset:1024\n"
    "ds_read_b64_tr_b16 a[146:147], v91 offset:9216\n"
    "v_mfma_f32_16x16x32_bf16 a[168:171], a[148:151], v[12:15], a[168:171]\n"
    "s_waitcnt lgkmcnt(15)\n"
    "ds_read_b64_tr_b16 a[148:149], v91 offset:17408\n"
    "ds_read_b64_tr_b16 a[150:151], v91 offset:25600\n"
    "v_mfma_f32_16x16x32_bf16 v[108:111], a[152:155], v[64:67], v[108:111]\n"
    "v_exp_f32 v42, v42\n"
    "v_mfma_f32_16x16x32_bf16 v[108:111], a[156:159], v[0:3], v[108:111]\n"
    "v_fma_f32 v44, v44, s52, v87\n"
    "v_fma_f32 v46, v46, s52, v87\n"
    "v_mfma_f32_16x16x32_bf16 v[172:175], a[152:155], v[68:71], v[172:175]\n"
    "v_exp_f32 v44, v44\n"
    "v_mfma_f32_16x16x32_bf16 v[172:175], a[156:159], v[4:7], v[172:175]\n"
    "v_exp_f32_e64 v46, v46\n"
    "v_mfma_f32_16x16x32_bf16 v[236:239], a[152:155], v[72:75], v[236:239]\n"
    "s_waitcnt lgkmcnt(4)\n"
    "v_fma_f32 v33, v33, s52, v84\n"
    "v_fma_f32 v35, v35, s52, v84\n"
    "v_mfma_f32_16x16x32_bf16 v[236:239], a[156:159], v[8:11], v[236:239]\n"
    "s_add_u32 s73, s79, 16384\n"
    "v_exp_f32_e64 v33, v33\n"
    "v_mfma_f32_16x16x32_bf16 a[172:175], a[152:155], v[76:79], a[172:175]\n"
    "s_add_u32 s74, s79, 20480\n"
    "ds_read_b64_tr_b16 a[152:153], v91 offset:1032\n"
    "ds_read_b64_tr_b16 a[154:155], v91 offset:9224\n"
    "v_mfma_f32_16x16x32_bf16 a[172:175], a[156:159], v[12:15], a[172:175]\n"
    "s_waitcnt lgkmcnt(15)\n"
    "ds_read_b64_tr_b16 a[156:157], v91 offset:17416\n"
    "s_nop 0\n"
    "ds_read_b64_tr_b16 a[158:159], v91 offset:25608\n"
    "v_mfma_f32_16x16x32_bf16 v[112:115], a[128:131], v[64:67], v[112:115]\n"
    "s_add_u32 m0, s70, 0\n"
    "v_exp_f32 v35, v35\n"
    "v_mfma_f32_16x16x32_bf16 v[112:115], a[132:135], v[0:3], v[112:115]\n"
    "buffer_load_dwordx4 v92, s[44:47], s73 offen offset:0 nt lds\n"
    "v_mfma_f32_16x16x32_bf16 v[176:179], a[128:131], v[68:71], v[176:179]\n"
    "s_add_u32 m0, m0, 896\n"
    "v_fma_f32 v37, v37, s52, v85\n"
    "v_fma_f32 v39, v39, s52, v85\n"
    "v_mfma_f32_16x16x32_bf16 v[176:179], a[132:135], v[4:7], v[176:179]\n"
    "v_exp_f32_e64 v37, v37\n"
    "v_mfma_f32_16x16x32_bf16 v[240:243], a[128:131], v[72:75], v[240:243]\n"
    "v_exp_f32_e64 v39, v39\n"
    "v_mfma_f32_16x16x32_bf16 v[240:243], a[132:135], v[8:11], v[240:243]\n"
    "buffer_load_dwordx4 v92, s[44:47], s73 offen offset:128 nt lds\n"
    "v_mfma_f32_16x16x32_bf16 a[176:179], a[128:131], v[76:79], a[176:179]\n"
    "s_add_u32 m0, m0, 896\n"
    "ds_read_b64_tr_b16 a[128:129], v90 offset:2048\n"
    "ds_read_b64_tr_b16 a[130:131], v90 offset:10240\n"
    "v_mfma_f32_16x16x32_bf16 a[176:179], a[132:135], v[12:15], a[176:179]\n"
    "s_waitcnt lgkmcnt(15)\n"
    "ds_read_b64_tr_b16 a[132:133], v90 offset:18432\n"
    "ds_read_b64_tr_b16 a[134:135], v90 offset:26624\n"
    "v_mfma_f32_16x16x32_bf16 v[116:119], a[136:139], v[64:67], v[116:119]\n"
    "s_nop 0\n"
    "v_fma_f32 v41, v41, s52, v86\n"
    "v_fma_f32 v43, v43, s52, v86\n"
    "v_mfma_f32_16x16x32_bf16 v[116:119], a[140:143], v[0:3], v[116:119]\n"
    "buffer_load_dwordx4 v92, s[44:47], s73 offen offset:256 nt lds\n"
    "v_mfma_f32_16x16x32_bf16 v[180:183], a[136:139], v[68:71], v[180:183]\n"
    "s_add_u32 m0, m0, 896\n"
    "v_exp_f32_e64 v41, v41\n"
    "v_mfma_f32_16x16x32_bf16 v[180:183], a[140:143], v[4:7], v[180:183]\n"
    "s_nop 0\n"
    "v_exp_f32_e64 v43, v43\n"
    "v_mfma_f32_16x16x32_bf16 v[244:247], a[136:139], v[72:75], v[244:247]\n"
    "s_nop 0\n"
    "v_fma_f32 v45, v45, s52, v87\n"
    "v_fma_f32 v47, v47, s52, v87\n"
    "v_mfma_f32_16x16x32_bf16 v[244:247], a[140:143], v[8:11], v[244:247]\n"
    "buffer_load_dwordx4 v92, s[44:47], s73 offen offset:384 nt lds\n"
    "v_mfma_f32_16x16x32_bf16 a[180:183], a[136:139], v[76:79], a[180:183]\n"
    "s_add_u32 m0, m0, 1408\n"
    "ds_read_b64_tr_b16 a[136:137], v90 offset:2056\n"
    "ds_read_b64_tr_b16 a[138:139], v90 offset:10248\n"
    "v_mfma_f32_16x16x32_bf16 a[180:183], a[140:143], v[12:15], a[180:183]\n"
    "s_waitcnt lgkmcnt(10)\n"
    "ds_read_b64_tr_b16 a[140:141], v90 offset:18440\n"
    "ds_read_b64_tr_b16 a[142:143], v90 offset:26632\n"
    "v_mfma_f32_16x16x32_bf16 v[120:123], a[144:147], v[64:67], v[120:123]\n"
    "v_exp_f32 v45, v45\n"
    "v_mfma_f32_16x16x32_bf16 v[120:123], a[148:151], v[0:3], v[120:123]\n"
    "buffer_load_dwordx4 v92, s[44:47], s74 offen offset:0 nt lds\n"
    "v_mfma_f32_16x16x32_bf16 v[184:187], a[144:147], v[68:71], v[184:187]\n"
    "s_add_u32 m0, m0, s97\n"
    "v_exp_f32 v47, v47\n"
    "v_mfma_f32_16x16x32_bf16 v[184:187], a[148:151], v[4:7], v[184:187]\n"
    "v_add_f32 v80, v80, v32\n"
    "v_add_f32 v81, v81, v36\n"
    "v_mfma_f32_16x16x32_bf16 v[248:251], a[144:147], v[72:75], v[248:251]\n"
    "v_add_f32_e64 v82, v82, v40\n"
    "v_add_f32_e64 v83, v83, v44\n"
    "v_mfma_f32_16x16x32_bf16 v[248:251], a[148:151], v[8:11], v[248:251]\n"
    "buffer_load_dwordx4 v92, s[44:47], s74 offen offset:128 nt lds\n"
    "v_mfma_f32_16x16x32_bf16 a[184:187], a[144:147], v[76:79], a[184:187]\n"
    "s_add_u32 m0, m0, s97\n"
    "ds_read_b64_tr_b16 a[144:145], v91 offset:2048\n"
    "ds_read_b64_tr_b16 a[146:147], v91 offset:10240\n"
    "v_mfma_f32_16x16x32_bf16 a[184:187], a[148:151], v[12:15], a[184:187]\n"
    "s_waitcnt lgkmcnt(10)\n"
    "ds_read_b64_tr_b16 a[148:149], v91 offset:18432\n"
    "ds_read_b64_tr_b16 a[150:151], v91 offset:26624\n"
    "v_mfma_f32_16x16x32_bf16 v[124:127], a[152:155], v[64:67], v[124:127]\n"
    "v_add_f32 v80, v80, v33\n"
    "v_add_f32 v81, v81, v37\n"
    "v_mfma_f32_16x16x32_bf16 v[124:127], a[156:159], v[0:3], v[124:127]\n"
    "buffer_load_dwordx4 v92, s[44:47], s74 offen offset:256 nt lds\n"
    "v_mfma_f32_16x16x32_bf16 v[188:191], a[152:155], v[68:71], v[188:191]\n"
    "s_add_u32 m0, m0, s97\n"
    "v_add_f32 v82, v82, v41\n"
    "v_add_f32 v83, v83, v45\n"
    "v_mfma_f32_16x16x32_bf16 v[188:191], a[156:159], v[4:7], v[188:191]\n"
    "v_add_f32 v80, v80, v34\n"
    "v_add_f32 v81, v81, v38\n"
    "v_mfma_f32_16x16x32_bf16 v[252:255], a[152:155], v[72:75], v[252:255]\n"
    "v_add_f32 v82, v82, v42\n"
    "v_add_f32_e64 v83, v83, v46\n"
    "v_mfma_f32_16x16x32_bf16 v[252:255], a[156:159], v[8:11], v[252:255]\n"
    "buffer_load_dwordx4 v92, s[44:47], s74 offen offset:384 nt lds\n"
    "v_mfma_f32_16x16x32_bf16 a[188:191], a[152:155], v[76:79], a[188:191]\n"
    "s_add_u32 s44, s44, 434176\n"
    "ds_read_b64_tr_b16 a[152:153], v91 offset:2056\n"
    "ds_read_b64_tr_b16 a[154:155], v91 offset:10248\n"
    "v_mfma_f32_16x16x32_bf16 a[188:191], a[156:159], v[12:15], a[188:191]\n"
    "s_waitcnt lgkmcnt(10)\n"
    "ds_read_b64_tr_b16 a[156:157], v91 offset:18440\n"
    "ds_read_b64_tr_b16 a[158:159], v91 offset:26632\n"
    "v_mfma_f32_16x16x32_bf16 v[128:131], a[128:131], v[64:67], v[128:131]\n"
    "s_addc_u32 s45, s45, 0\n"
    "v_add_f32 v80, v80, v35\n"
    "v_add_f32_e64 v81, v81, v39\n"
    "v_mfma_f32_16x16x32_bf16 v[128:131], a[132:135], v[0:3], v[128:131]\n"
    "s_cmp_ge_u32 s66, 3\n"
    "v_add_f32_e64 v82, v82, v43\n"
    "v_add_f32 v83, v83, v47\n"
    "v_mfma_f32_16x16x32_bf16 v[192:195], a[128:131], v[68:71], v[192:195]\n"
    "s_cselect_b32 s46, 0, s46\n"
    "v_cvt_pk_bf16_f32 v32, v32, v33\n"
    "v_cvt_pk_bf16_f32 v33, v34, v35\n"
    "v_mfma_f32_16x16x32_bf16 v[192:195], a[132:135], v[4:7], v[192:195]\n"
    "v_cvt_pk_bf16_f32 v36, v36, v37\n"
    "v_cvt_pk_bf16_f32 v37, v38, v39\n"
    "v_mfma_f32_16x16x32_bf16 a[224:227], a[128:131], v[72:75], a[224:227]\n"
    "v_cvt_pk_bf16_f32 v40, v40, v41\n"
    "v_cvt_pk_bf16_f32 v41, v42, v43\n"
    "v_mfma_f32_16x16x32_bf16 a[224:227], a[132:135], v[8:11], a[224:227]\n"
    "v_cvt_pk_bf16_f32 v44, v44, v45\n"
    "v_cvt_pk_bf16_f32 v45, v46, v47\n"
    "v_mfma_f32_16x16x32_bf16 a[192:195], a[128:131], v[76:79], a[192:195]\n"
    "ds_read_b64_tr_b16 a[128:129], v90 offset:3072\n"
    "ds_read_b64_tr_b16 a[130:131], v90 offset:11264\n"
    "v_mfma_f32_16x16x32_bf16 a[192:195], a[132:135], v[12:15], a[192:195]\n"
    "s_waitcnt lgkmcnt(10)\n"
    "ds_read_b64_tr_b16 a[132:133], v90 offset:19456\n"
    "ds_read_b64_tr_b16 a[134:135], v90 offset:27648\n"
    "s_nop 0\n"
    "v_mfma_f32_16x16x32_bf16 v[132:135], a[136:139], v[64:67], v[132:135]\n"
    "v_fma_f32 v48, v48, s52, v84\n"
    "v_fma_f32 v50, v50, s52, v84\n"
    "v_mfma_f32_16x16x32_bf16 v[132:135], a[140:143], v[0:3], v[132:135]\n"
    "v_exp_f32 v48, v48\n"
    "v_mfma_f32_16x16x32_bf16 v[196:199], a[136:139], v[68:71], v[196:199]\n"
    "v_exp_f32 v50, v50\n"
    "v_mfma_f32_16x16x32_bf16 v[196:199], a[140:143], v[4:7], v[196:199]\n"
    "v_fma_f32 v52, v52, s52, v85\n"
    "v_fma_f32 v54, v54, s52, v85\n"
    "v_mfma_f32_16x16x32_bf16 a[228:231], a[136:139], v[72:75], a[228:231]\n"
    "v_exp_f32 v52, v52\n"
    "v_mfma_f32_16x16x32_bf16 a[228:231], a[140:143], v[8:11], a[228:231]\n"
    "v_exp_f32 v54, v54\n"
    "v_mfma_f32_16x16x32_bf16 a[196:199], a[136:139], v[76:79], a[196:199]\n"
    "ds_read_b64_tr_b16 a[136:137], v90 offset:3080\n"
    "ds_read_b64_tr_b16 a[138:139], v90 offset:11272\n"
    "v_mfma_f32_16x16x32_bf16 a[196:199], a[140:143], v[12:15], a[196:199]\n"
    "s_waitcnt lgkmcnt(10)\n"
    "ds_read_b64_tr_b16 a[140:141], v90 offset:19464\n"
    "ds_read_b64_tr_b16 a[142:143], v90 offset:27656\n"
    "v_mfma_f32_16x16x32_bf16 v[136:139], a[144:147], v[64:67], v[136:139]\n"
    "s_nop 0\n"
    "v_fma_f32 v56, v56, s52, v86\n"
    "v_fma_f32 v58, v58, s52, v86\n"
    "v_mfma_f32_16x16x32_bf16 v[136:139], a[148:151], v[0:3], v[136:139]\n"
    "v_exp_f32_e64 v56, v56\n"
    "v_mfma_f32_16x16x32_bf16 v[200:203], a[144:147], v[68:71], v[200:203]\n"
    "v_exp_f32_e64 v58, v58\n"
    "v_mfma_f32_16x16x32_bf16 v[200:203], a[148:151], v[4:7], v[200:203]\n"
    "v_fma_f32 v60, v60, s52, v87\n"
    "v_fma_f32 v62, v62, s52, v87\n"
    "v_mfma_f32_16x16x32_bf16 a[232:235], a[144:147], v[72:75], a[232:235]\n"
    "v_exp_f32 v60, v60\n"
    "v_mfma_f32_16x16x32_bf16 a[232:235], a[148:151], v[8:11], a[232:235]\n"
    "v_exp_f32 v62, v62\n"
    "v_mfma_f32_16x16x32_bf16 a[200:203], a[144:147], v[76:79], a[200:203]\n"
    "ds_read_b64_tr_b16 a[144:145], v91 offset:3072\n"
    "ds_read_b64_tr_b16 a[146:147], v91 offset:11264\n"
    "v_mfma_f32_16x16x32_bf16 a[200:203], a[148:151], v[12:15], a[200:203]\n"
    "s_waitcnt lgkmcnt(10)\n"
    "ds_read_b64_tr_b16 a[148:149], v91 offset:19456\n"
    "ds_read_b64_tr_b16 a[150:151], v91 offset:27648\n"
    "v_mfma_f32_16x16x32_bf16 v[140:143], a[152:155], v[64:67], v[140:143]\n"
    "s_nop 0\n"
    "v_fma_f32 v49, v49, s52, v84\n"
    "v_fma_f32 v51, v51, s52, v84\n"
    "v_mfma_f32_16x16x32_bf16 v[140:143], a[156:159], v[0:3], v[140:143]\n"
    "v_exp_f32_e64 v49, v49\n"
    "v_mfma_f32_16x16x32_bf16 v[204:207], a[152:155], v[68:71], v[204:207]\n"
    "v_exp_f32_e64 v51, v51\n"
    "v_mfma_f32_16x16x32_bf16 v[204:207], a[156:159], v[4:7], v[204:207]\n"
    "v_fma_f32 v53, v53, s52, v85\n"
    "v_fma_f32 v55, v55, s52, v85\n"
    "v_mfma_f32_16x16x32_bf16 a[236:239], a[152:155], v[72:75], a[236:239]\n"
    "v_exp_f32 v53, v53\n"
    "v_mfma_f32_16x16x32_bf16 a[236:239], a[156:159], v[8:11], a[236:239]\n"
    "v_exp_f32 v55, v55\n"
    "v_mfma_f32_16x16x32_bf16 a[204:207], a[152:155], v[76:79], a[204:207]\n"
    "ds_read_b64_tr_b16 a[152:153], v91 offset:3080\n"
    "ds_read_b64_tr_b16 a[154:155], v91 offset:11272\n"
    "v_mfma_f32_16x16x32_bf16 a[204:207], a[156:159], v[12:15], a[204:207]\n"
    "s_waitcnt lgkmcnt(10)\n"
    "ds_read_b64_tr_b16 a[156:157], v91 offset:19464\n"
    "ds_read_b64_tr_b16 a[158:159], v91 offset:27656\n"
    "v_mfma_f32_16x16x32_bf16 v[144:147], a[128:131], v[64:67], v[144:147]\n"
    "v_add_u32 v90, s64, v90\n"
    "v_add_u32_e64 v91, s64, v91\n"
    "v_mfma_f32_16x16x32_bf16 v[144:147], a[132:135], v[0:3], v[144:147]\n"
    "v_fma_f32 v57, v57, s52, v86\n"
    "v_fma_f32 v59, v59, s52, v86\n"
    "v_mfma_f32_16x16x32_bf16 v[208:211], a[128:131], v[68:71], v[208:211]\n"
    "v_exp_f32 v57, v57\n"
    "v_mfma_f32_16x16x32_bf16 v[208:211], a[132:135], v[4:7], v[208:211]\n"
    "v_exp_f32 v59, v59\n"
    "v_mfma_f32_16x16x32_bf16 a[240:243], a[128:131], v[72:75], a[240:243]\n"
    "v_fma_f32 v61, v61, s52, v87\n"
    "v_fma_f32 v63, v63, s52, v87\n"
    "v_mfma_f32_16x16x32_bf16 a[240:243], a[132:135], v[8:11], a[240:243]\n"
    "v_exp_f32_e64 v61, v61\n"
    "v_mfma_f32_16x16x32_bf16 a[208:211], a[128:131], v[76:79], a[208:211]\n"
    "s_waitcnt vmcnt(18) lgkmcnt(0)\n"
    "v_exp_f32_e64 v63, v63\n"
    "v_mfma_f32_16x16x32_bf16 a[208:211], a[132:135], v[12:15], a[208:211]\n"
    "s_barrier\n"
    "v_mfma_f32_16x16x32_bf16 v[148:151], a[136:139], v[64:67], v[148:151]\n"
    "ds_read_b64_tr_b16 a[128:129], v90 offset:0\n"
    "ds_read_b64_tr_b16 a[130:131], v90 offset:8192\n"
    "v_mfma_f32_16x16x32_bf16 v[148:151], a[140:143], v[0:3], v[148:151]\n"
    "ds_read_b64_tr_b16 a[132:133], v90 offset:8\n"
    "ds_read_b64_tr_b16 a[134:135], v90 offset:8200\n"
    "v_mfma_f32_16x16x32_bf16 v[212:215], a[136:139], v[68:71], v[212:215]\n"
    "v_add_f32 v80, v80, v48\n"
    "v_add_f32 v81, v81, v52\n"
    "v_mfma_f32_16x16x32_bf16 v[212:215], a[140:143], v[4:7], v[212:215]\n"
    "v_add_f32 v82, v82, v56\n"
    "v_add_f32 v83, v83, v60\n"
    "v_mfma_f32_16x16x32_bf16 a[244:247], a[136:139], v[72:75], a[244:247]\n"
    "v_add_f32 v80, v80, v49\n"
    "v_add_f32 v81, v81, v53\n"
    "v_mfma_f32_16x16x32_bf16 a[244:247], a[140:143], v[8:11], a[244:247]\n"
    "v_add_f32 v82, v82, v57\n"
    "v_add_f32 v83, v83, v61\n"
    "v_mfma_f32_16x16x32_bf16 a[212:215], a[136:139], v[76:79], a[212:215]\n"
    "ds_read_b64_tr_b16 a[136:137], v91 offset:0\n"
    "ds_read_b64_tr_b16 a[138:139], v91 offset:8192\n"
    "v_mfma_f32_16x16x32_bf16 a[212:215], a[140:143], v[12:15], a[212:215]\n"
    "ds_read_b64_tr_b16 a[140:141], v91 offset:8\n"
    "ds_read_b64_tr_b16 a[142:143], v91 offset:8200\n"
    "v_mfma_f32_16x16x32_bf16 v[152:155], a[144:147], v[64:67], v[152:155]\n"
    "v_add_f32 v80, v80, v50\n"
    "v_add_f32 v81, v81, v54\n"
    "v_mfma_f32_16x16x32_bf16 v[152:155], a[148:151], v[0:3], v[152:155]\n"
    "v_add_f32 v82, v82, v58\n"
    "v_add_f32 v83, v83, v62\n"
    "v_mfma_f32_16x16x32_bf16 v[216:219], a[144:147], v[68:71], v[216:219]\n"
    "v_cvt_pk_bf16_f32 v34, v48, v49\n"
    "v_add_f32_e64 v80, v80, v51\n"
    "v_mfma_f32_16x16x32_bf16 v[216:219], a[148:151], v[4:7], v[216:219]\n"
    "v_cvt_pk_bf16_f32 v35, v50, v51\n"
    "v_add_f32 v81, v81, v55\n"
    "v_mfma_f32_16x16x32_bf16 a[248:251], a[144:147], v[72:75], a[248:251]\n"
    "v_add_f32 v82, v82, v59\n"
    "v_add_f32_e64 v83, v83, v63\n"
    "v_mfma_f32_16x16x32_bf16 a[248:251], a[148:151], v[8:11], a[248:251]\n"
    "v_cvt_pk_bf16_f32 v38, v52, v53\n"
    "v_cvt_pk_bf16_f32 v39, v54, v55\n"
    "v_mfma_f32_16x16x32_bf16 a[216:219], a[144:147], v[76:79], a[216:219]\n"
    "ds_read_b64_tr_b16 a[144:145], v90 offset:1024\n"
    "ds_read_b64_tr_b16 a[146:147], v90 offset:9216\n"
    "v_mfma_f32_16x16x32_bf16 a[216:219], a[148:151], v[12:15], a[216:219]\n"
    "ds_read_b64_tr_b16 a[148:149], v90 offset:1032\n"
    "ds_read_b64_tr_b16 a[150:151], v90 offset:9224\n"
    "v_mfma_f32_16x16x32_bf16 v[156:159], a[152:155], v[64:67], v[156:159]\n"
    "v_cvt_pk_bf16_f32 v42, v56, v57\n"
    "v_cvt_pk_bf16_f32 v43, v58, v59\n"
    "v_mfma_f32_16x16x32_bf16 v[156:159], a[156:159], v[0:3], v[156:159]\n"
    "v_cvt_pk_bf16_f32 v46, v60, v61\n"
    "v_cvt_pk_bf16_f32 v47, v62, v63\n"
    "v_mfma_f32_16x16x32_bf16 v[220:223], a[152:155], v[68:71], v[220:223]\n"
    "v_mov_b32_e64 v20, v80\n"
    "v_mov_b32_e64 v21, v81\n"
    "v_mfma_f32_16x16x32_bf16 v[220:223], a[156:159], v[4:7], v[220:223]\n"
    "v_mov_b32_e64 v22, v82\n"
    "v_mov_b32_e64 v23, v83\n"
    "v_mfma_f32_16x16x32_bf16 a[252:255], a[152:155], v[72:75], a[252:255]\n"
    "v_permlane32_swap_b32 v80, v20\n"
    "v_mfma_f32_16x16x32_bf16 a[252:255], a[156:159], v[8:11], a[252:255]\n"
    "v_permlane32_swap_b32 v81, v21\n"
    "v_mfma_f32_16x16x32_bf16 a[220:223], a[152:155], v[76:79], a[220:223]\n"
    "ds_read_b64_tr_b16 a[152:153], v91 offset:1024\n"
    "ds_read_b64_tr_b16 a[154:155], v91 offset:9216\n"
    "v_mfma_f32_16x16x32_bf16 a[220:223], a[156:159], v[12:15], a[220:223]\n"
    "s_waitcnt lgkmcnt(10)\n"
    "ds_read_b64_tr_b16 a[156:157], v91 offset:1032\n"
    "ds_read_b64_tr_b16 a[158:159], v91 offset:9224\n"
    "v_mfma_f32_16x16x32_bf16 v[96:99], a[128:131], v[32:35], v[96:99]\n"
    "v_permlane32_swap_b32 v82, v22\n"
    "v_mfma_f32_16x16x32_bf16 v[100:103], a[132:135], v[32:35], v[100:103]\n"
    "v_permlane32_swap_b32 v83, v23\n"
    "v_mfma_f32_16x16x32_bf16 v[160:163], a[128:131], v[36:39], v[160:163]\n"
    "v_add_f32 v80, v80, v20\n"
    "v_add_f32_e64 v81, v81, v21\n"
    "v_mfma_f32_16x16x32_bf16 v[164:167], a[132:135], v[36:39], v[164:167]\n"
    "v_add_f32 v82, v82, v22\n"
    "v_add_f32 v83, v83, v23\n"
    "v_mfma_f32_16x16x32_bf16 v[224:227], a[128:131], v[40:43], v[224:227]\n"
    "v_mov_b32_e64 v20, v80\n"
    "v_mov_b32_e64 v21, v81\n"
    "v_mfma_f32_16x16x32_bf16 v[228:231], a[132:135], v[40:43], v[228:231]\n"
    "v_mov_b32_e64 v22, v82\n"
    "v_mov_b32_e64 v23, v83\n"
    "v_mfma_f32_16x16x32_bf16 a[160:163], a[128:131], v[44:47], a[160:163]\n"
    "ds_read_b64_tr_b16 a[128:129], v90 offset:2048\n"
    "ds_read_b64_tr_b16 a[130:131], v90 offset:10240\n"
    "v_mfma_f32_16x16x32_bf16 a[164:167], a[132:135], v[44:47], a[164:167]\n"
    "s_waitcnt lgkmcnt(10)\n"
    "ds_read_b64_tr_b16 a[132:133], v90 offset:2056\n"
    "ds_read_b64_tr_b16 a[134:135], v90 offset:10248\n"
    "v_mfma_f32_16x16x32_bf16 v[104:107], a[136:139], v[32:35], v[104:107]\n"
    "v_permlane16_swap_b32 v80, v20\n"
    "v_mfma_f32_16x16x32_bf16 v[108:111], a[140:143], v[32:35], v[108:111]\n"
    "v_permlane16_swap_b32 v81, v21\n"
    "v_mfma_f32_16x16x32_bf16 v[168:171], a[136:139], v[36:39], v[168:171]\n"
    "v_permlane16_swap_b32 v82, v22\n"
    "v_mfma_f32_16x16x32_bf16 v[172:175], a[140:143], v[36:39], v[172:175]\n"
    "v_permlane16_swap_b32 v83, v23\n"
    "v_mfma_f32_16x16x32_bf16 v[232:235], a[136:139], v[40:43], v[232:235]\n"
    "s_nop 0\n"
    "v_add_f32_e64 v80, v80, v20\n"
    "v_add_f32_e64 v81, v81, v21\n"
    "v_mfma_f32_16x16x32_bf16 v[236:239], a[140:143], v[40:43], v[236:239]\n"
    "v_add_f32_e64 v82, v82, v22\n"
    "v_add_f32_e64 v83, v83, v23\n"
    "v_mfma_f32_16x16x32_bf16 a[168:171], a[136:139], v[44:47], a[168:171]\n"
    "s_nop 0\n"
    "ds_read_b64_tr_b16 a[136:137], v91 offset:2048\n"
    "ds_read_b64_tr_b16 a[138:139], v91 offset:10240\n"
    "v_mfma_f32_16x16x32_bf16 a[172:175], a[140:143], v[44:47], a[172:175]\n"
    "s_waitcnt lgkmcnt(10)\n"
    "ds_read_b64_tr_b16 a[140:141], v91 offset:2056\n"
    "ds_read_b64_tr_b16 a[142:143], v91 offset:10248\n"
    "v_mfma_f32_16x16x32_bf16 v[112:115], a[144:147], v[32:35], v[112:115]\n"
    "v_add_f32 v24, v80, v81\n"
    "v_add_f32 v24, v24, v82\n"
    "v_mfma_f32_16x16x32_bf16 v[116:119], a[148:151], v[32:35], v[116:119]\n"
    "v_add_f32_e64 v24, v24, v83\n"
    "v_mbcnt_lo_u32_b32 v25, -1, 0\n"
    "v_mfma_f32_16x16x32_bf16 v[176:179], a[144:147], v[36:39], v[176:179]\n"
    "v_rcp_f32_e64 v80, v80\n"
    "v_mfma_f32_16x16x32_bf16 v[180:183], a[148:151], v[36:39], v[180:183]\n"
    "v_rcp_f32_e64 v81, v81\n"
    "v_mfma_f32_16x16x32_bf16 v[240:243], a[144:147], v[40:43], v[240:243]\n"
    "v_rcp_f32_e64 v82, v82\n"
    "v_mfma_f32_16x16x32_bf16 v[244:247], a[148:151], v[40:43], v[244:247]\n"
    "v_rcp_f32_e64 v83, v83\n"
    "v_mfma_f32_16x16x32_bf16 a[176:179], a[144:147], v[44:47], a[176:179]\n"
    "s_nop 0\n"
    "ds_read_b64_tr_b16 a[144:145], v90 offset:3072\n"
    "ds_read_b64_tr_b16 a[146:147], v90 offset:11264\n"
    "v_mfma_f32_16x16x32_bf16 a[180:183], a[148:151], v[44:47], a[180:183]\n"
    "s_waitcnt lgkmcnt(10)\n"
    "ds_read_b64_tr_b16 a[148:149], v90 offset:3080\n"
    "ds_read_b64_tr_b16 a[150:151], v90 offset:11272\n"
    "v_mfma_f32_16x16x32_bf16 v[120:123], a[152:155], v[32:35], v[120:123]\n"
    "v_mbcnt_hi_u32_b32 v25, -1, v25\n"
    "v_lshrrev_b32 v26, 3, v25\n"
    "v_mfma_f32_16x16x32_bf16 v[124:127], a[156:159], v[32:35], v[124:127]\n"
    "v_and_b32 v27, 7, v25\n"
    "v_xor_b32 v19, v26, v27\n"
    "v_mfma_f32_16x16x32_bf16 v[184:187], a[152:155], v[36:39], v[184:187]\n"
    "v_lshlrev_b32 v19, 4, v19\n"
    "v_lshl_add_u32 v19, v26, 9, v19\n"
    "v_mfma_f32_16x16x32_bf16 v[188:191], a[156:159], v[36:39], v[188:191]\n"
    "v_and_b32 v26, 1, v26\n"
    "v_lshlrev_b32 v26, 10, v26\n"
    "v_mfma_f32_16x16x32_bf16 v[248:251], a[152:155], v[40:43], v[248:251]\n"
    "v_lshl_add_u32 v26, v27, 7, v26\n"
    "v_lshrrev_b32 v16, 4, v25\n"
    "v_mfma_f32_16x16x32_bf16 v[252:255], a[156:159], v[40:43], v[252:255]\n"
    "v_xor_b32 v16, v16, v27\n"
    "v_lshl_add_u32 v16, v16, 4, v26\n"
    "v_mfma_f32_16x16x32_bf16 a[184:187], a[152:155], v[44:47], a[184:187]\n"
    "ds_read_b64_tr_b16 a[152:153], v91 offset:3072\n"
    "ds_read_b64_tr_b16 a[154:155], v91 offset:11264\n"
    "v_mfma_f32_16x16x32_bf16 a[188:191], a[156:159], v[44:47], a[188:191]\n"
    "s_waitcnt lgkmcnt(10)\n"
    "ds_read_b64_tr_b16 a[156:157], v91 offset:3080\n"
    "ds_read_b64_tr_b16 a[158:159], v91 offset:11272\n"
    "v_mfma_f32_16x16x32_bf16 v[128:131], a[128:131], v[32:35], v[128:131]\n"
    "v_lshlrev_b32 v18, 4, v25\n"
    "v_cmp_nge_f32 vcc, s75, v24\n"
    "v_mfma_f32_16x16x32_bf16 v[132:135], a[132:135], v[32:35], v[132:135]\n"
    "s_cmp_lg_u64 vcc, 0\n"
    "v_add_u32 v16, s72, v16\n"
    "v_xor_b32 v17, 64, v16\n"
    "v_mfma_f32_16x16x32_bf16 v[192:195], a[128:131], v[36:39], v[192:195]\n"
    "s_cselect_b32 s71, s68, 0\n"
    "v_add_u32 v18, s72, v18\n"
    "v_mul_f32 v96, v96, v80\n"
    "v_mfma_f32_16x16x32_bf16 v[196:199], a[132:135], v[36:39], v[196:199]\n"
    "s_or_b32 s67, s67, s71\n"
    "v_mul_f32_e64 v97, v97, v80\n"
    "v_mul_f32 v98, v98, v80\n"
    "v_mfma_f32_16x16x32_bf16 a[224:227], a[128:131], v[40:43], a[224:227]\n"
    "s_lshl_b32 s68, s68, 1\n"
    "v_mul_f32_e64 v99, v99, v80\n"
    "v_mul_f32_e64 v100, v100, v80\n"
    "v_mfma_f32_16x16x32_bf16 a[228:231], a[132:135], v[40:43], a[228:231]\n"
    "v_mul_f32 v101, v101, v80\n"
    "v_mul_f32_e64 v102, v102, v80\n"
    "v_mfma_f32_16x16x32_bf16 a[192:195], a[128:131], v[44:47], a[192:195]\n"
    "s_waitcnt vmcnt(8) lgkmcnt(0)\n"
    "v_mul_f32 v103, v103, v80\n"
    "v_cvt_pk_bf16_f32 v96, v96, v97\n"
    "v_mfma_f32_16x16x32_bf16 a[196:199], a[132:135], v[44:47], a[196:199]\n"
    "s_barrier\n"
    "v_mfma_f32_16x16x32_bf16 v[136:139], a[136:139], v[32:35], v[136:139]\n"
    "v_cvt_pk_bf16_f32 v97, v98, v99\n"
    "v_cvt_pk_bf16_f32 v98, v100, v101\n"
    "v_mfma_f32_16x16x32_bf16 v[140:143], a[140:143], v[32:35], v[140:143]\n"
    "v_cvt_pk_bf16_f32 v99, v102, v103\n"
    "v_mul_f32 v160, v160, v81\n"
    "v_mfma_f32_16x16x32_bf16 v[200:203], a[136:139], v[36:39], v[200:203]\n"
    "v_mul_f32 v161, v161, v81\n"
    "v_mul_f32 v162, v162, v81\n"
    "v_mfma_f32_16x16x32_bf16 v[204:207], a[140:143], v[36:39], v[204:207]\n"
    "v_mul_f32 v163, v163, v81\n"
    "v_mul_f32 v164, v164, v81\n"
    "v_mfma_f32_16x16x32_bf16 a[232:235], a[136:139], v[40:43], a[232:235]\n"
    "v_mul_f32 v165, v165, v81\n"
    "v_mul_f32 v166, v166, v81\n"
    "v_mfma_f32_16x16x32_bf16 a[236:239], a[140:143], v[40:43], a[236:239]\n"
    "v_mul_f32 v167, v167, v81\n"
    "v_cvt_pk_bf16_f32 v160, v160, v161\n"
    "v_mfma_f32_16x16x32_bf16 a[200:203], a[136:139], v[44:47], a[200:203]\n"
    "v_cvt_pk_bf16_f32 v161, v162, v163\n"
    "v_cvt_pk_bf16_f32 v162, v164, v165\n"
    "v_mfma_f32_16x16x32_bf16 a[204:207], a[140:143], v[44:47], a[204:207]\n"
    "v_cvt_pk_bf16_f32 v163, v166, v167\n"
    "v_mul_f32 v104, v104, v80\n"
    "v_mfma_f32_16x16x32_bf16 v[144:147], a[144:147], v[32:35], v[144:147]\n"
    "v_mul_f32 v105, v105, v80\n"
    "v_mul_f32 v106, v106, v80\n"
    "v_mfma_f32_16x16x32_bf16 v[148:151], a[148:151], v[32:35], v[148:151]\n"
    "v_mul_f32 v107, v107, v80\n"
    "v_mul_f32 v108, v108, v80\n"
    "v_mfma_f32_16x16x32_bf16 v[208:211], a[144:147], v[36:39], v[208:211]\n"
    "v_mul_f32 v109, v109, v80\n"
    "v_mul_f32 v110, v110, v80\n"
    "v_mfma_f32_16x16x32_bf16 v[212:215], a[148:151], v[36:39], v[212:215]\n"
    "v_mul_f32 v111, v111, v80\n"
    "v_cvt_pk_bf16_f32 v104, v104, v105\n"
    "v_mfma_f32_16x16x32_bf16 a[240:243], a[144:147], v[40:43], a[240:243]\n"
    "v_cvt_pk_bf16_f32 v105, v106, v107\n"
    "v_cvt_pk_bf16_f32 v106, v108, v109\n"
    "v_mfma_f32_16x16x32_bf16 a[244:247], a[148:151], v[40:43], a[244:247]\n"
    "v_cvt_pk_bf16_f32 v107, v110, v111\n"
    "v_mul_f32 v168, v168, v81\n"
    "v_mfma_f32_16x16x32_bf16 a[208:211], a[144:147], v[44:47], a[208:211]\n"
    "v_mul_f32 v169, v169, v81\n"
    "v_mul_f32 v170, v170, v81\n"
    "v_mfma_f32_16x16x32_bf16 a[212:215], a[148:151], v[44:47], a[212:215]\n"
    "v_mul_f32 v171, v171, v81\n"
    "v_mul_f32 v172, v172, v81\n"
    "v_mfma_f32_16x16x32_bf16 v[152:155], a[152:155], v[32:35], v[152:155]\n"
    "v_mul_f32 v173, v173, v81\n"
    "v_mul_f32 v174, v174, v81\n"
    "v_mfma_f32_16x16x32_bf16 v[156:159], a[156:159], v[32:35], v[156:159]\n"
    "v_mul_f32 v175, v175, v81\n"
    "v_cvt_pk_bf16_f32 v168, v168, v169\n"
    "v_mfma_f32_16x16x32_bf16 v[216:219], a[152:155], v[36:39], v[216:219]\n"
    "v_cvt_pk_bf16_f32 v169, v170, v171\n"
    "v_cvt_pk_bf16_f32 v170, v172, v173\n"
    "v_mfma_f32_16x16x32_bf16 v[220:223], a[156:159], v[36:39], v[220:223]\n"
    "v_cvt_pk_bf16_f32 v171, v174, v175\n"
    "v_mul_f32 v112, v112, v80\n"
    "v_mfma_f32_16x16x32_bf16 a[248:251], a[152:155], v[40:43], a[248:251]\n"
    "v_mul_f32 v113, v113, v80\n"
    "v_mul_f32_e64 v114, v114, v80\n"
    "v_mfma_f32_16x16x32_bf16 a[252:255], a[156:159], v[40:43], a[252:255]\n"
    "v_mul_f32_e64 v115, v115, v80\n"
    "v_mul_f32_e64 v116, v116, v80\n"
    "v_mfma_f32_16x16x32_bf16 a[216:219], a[152:155], v[44:47], a[216:219]\n"
    "v_mul_f32_e64 v117, v117, v80\n"
    "v_mul_f32_e64 v118, v118, v80\n"
    "v_mfma_f32_16x16x32_bf16 a[220:223], a[156:159], v[44:47], a[220:223]\n"
    "s_cmp_eq_u32 s66, 4\n"
    "v_mul_f32_e64 v119, v119, v80\n"
    "v_cvt_pk_bf16_f32 v112, v112, v113\n"
    "s_cbranch_scc1 LEND4_%=\n"
    "ds_read_b128 a[128:131], v88 offset:0\n"
    "ds_read_b128 a[132:135], v89 offset:0\n"
    "ds_read_b128 a[136:139], v88 offset:1024\n"
    "ds_read_b128 a[140:143], v89 offset:1024\n"
    "ds_read_b128 a[144:147], v88 offset:2048\n"
    "ds_read_b128 a[148:151], v89 offset:2048\n"
    "ds_read_b128 a[152:155], v88 offset:3072\n"
    "ds_read_b128 a[156:159], v89 offset:3072\n"
    "s_cmp_lt_u32 s69, 1\n"
    "s_cbranch_scc1 LXS4_%=\n"
    "s_nop 3\n"
    "s_cmp_lt_u32 s69, 2\n"
    "s_cbranch_scc1 LXS4_%=\n"
    "s_nop 3\n"
    "s_cmp_lt_u32 s69, 3\n"
    "s_cbranch_scc1 LXS4_%=\n"
    "s_nop 3\n"
    "s_nop 0\n"
    "LXS4_%=:\n"
    "v_cvt_pk_bf16_f32 v113, v114, v115\n"
    "v_cvt_pk_bf16_f32 v114, v116, v117\n"
    "v_cvt_pk_bf16_f32 v115, v118, v119\n"
    "v_pk_mul_f32 v[176:177], v[176:177], v[80:81] op_sel:[0,1] op_sel_hi:[1,1]\n"
    "v_pk_mul_f32 v[178:179], v[178:179], v[80:81] op_sel:[0,1] op_sel_hi:[1,1]\n"
    "v_pk_mul_f32 v[180:181], v[180:181], v[80:81] op_sel:[0,1] op_sel_hi:[1,1]\n"
    "v_pk_mul_f32 v[182:183], v[182:183], v[80:81] op_sel:[0,1] op_sel_hi:[1,1]\n"
    "v_cvt_pk_bf16_f32 v176, v176, v177\n"
    "v_cvt_pk_bf16_f32 v177, v178, v179\n"
    "v_cvt_pk_bf16_f32 v178, v180, v181\n"
    "v_cvt_pk_bf16_f32 v179, v182, v183\n"
    "v_pk_mul_f32 v[120:121], v[120:121], v[80:81] op_sel_hi:[1,0]\n"
    "v_pk_mul_f32 v[122:123], v[122:123], v[80:81] op_sel_hi:[1,0]\n"
    "v_pk_mul_f32 v[124:125], v[124:125], v[80:81] op_sel_hi:[1,0]\n"
    "v_pk_mul_f32 v[126:127], v[126:127], v[80:81] op_sel_hi:[1,0]\n"
    "v_cvt_pk_bf16_f32 v120, v120, v121\n"
    "v_pk_mul_f32 v[224:225], v[224:225], v[82:83] op_sel_hi:[1,0]\n"
    "v_pk_mul_f32 v[226:227], v[226:227], v[82:83] op_sel_hi:[1,0]\n"
    "v_pk_mul_f32 v[228:229], v[228:229], v[82:83] op_sel_hi:[1,0]\n"
    "v_pk_mul_f32 v[230:231], v[230:231], v[82:83] op_sel_hi:[1,0]\n"
    "v_pk_mul_f32 v[232:233], v[232:233], v[82:83] op_sel_hi:[1,0]\n"
    "v_pk_mul_f32 v[234:235], v[234:235], v[82:83] op_sel_hi:[1,0]\n"
    "v_pk_mul_f32 v[236:237], v[236:237], v[82:83] op_sel_hi:[1,0]\n"
    "v_pk_mul_f32 v[238:239], v[238:239], v[82:83] op_sel_hi:[1,0]\n"
    "v_cvt_pk_bf16_f32 v224, v224, v225\n"
    "v_cvt_pk_bf16_f32 v225, v226, v227\n"
    "v_cvt_pk_bf16_f32 v226, v228, v229\n"
    "v_cvt_pk_bf16_f32 v227, v230, v231\n"
    "v_cvt_pk_bf16_f32 v232, v232, v233\n"
    "v_cvt_pk_bf16_f32 v233, v234, v235\n"
    "v_cvt_pk_bf16_f32 v234, v236, v237\n"
    "v_cvt_pk_bf16_f32 v235, v238, v239\n"
    "ds_write_b128 v16, v[224:227] offset:0\n"
    "ds_write_b128 v17, v[232:235] offset:0\n"
    "v_cvt_pk_bf16_f32 v121, v122, v123\n"
    "v_cvt_pk_bf16_f32 v122, v124, v125\n"
    "v_cvt_pk_bf16_f32 v123, v126, v127\n"
    "v_pk_mul_f32 v[184:185], v[184:185], v[80:81] op_sel:[0,1] op_sel_hi:[1,1]\n"
    "v_pk_mul_f32 v[186:187], v[186:187], v[80:81] op_sel:[0,1] op_sel_hi:[1,1]\n"
    "v_pk_mul_f32 v[188:189], v[188:189], v[80:81] op_sel:[0,1] op_sel_hi:[1,1]\n"
    "v_pk_mul_f32 v[190:191], v[190:191], v[80:81] op_sel:[0,1] op_sel_hi:[1,1]\n"
    "v_cvt_pk_bf16_f32 v184, v184, v185\n"
    "v_cvt_pk_bf16_f32 v185, v186, v187\n"
    "v_cvt_pk_bf16_f32 v186, v188, v189\n"
    "v_cvt_pk_bf16_f32 v187, v190, v191\n"
    "v_pk_mul_f32 v[128:129], v[128:129], v[80:81] op_sel_hi:[1,0]\n"
    "v_pk_mul_f32 v[130:131], v[130:131], v[80:81] op_sel_hi:[1,0]\n"
    "v_pk_mul_f32 v[132:133], v[132:133], v[80:81] op_sel_hi:[1,0]\n"
    "v_pk_mul_f32 v[134:135], v[134:135], v[80:81] op_sel_hi:[1,0]\n"
    "v_cvt_pk_bf16_f32 v128, v128, v129\n"
    "ds_read_b128 v[64:67], v18 offset:0\n"
    "ds_read_b128 v[68:71], v18 offset:1024\n"
    "v_pk_mul_f32 v[240:241], v[240:241], v[82:83] op_sel_hi:[1,0]\n"
    "v_pk_mul_f32 v[242:243], v[242:243], v[82:83] op_sel_hi:[1,0]\n"
    "v_pk_mul_f32 v[244:245], v[244:245], v[82:83] op_sel_hi:[1,0]\n"
    "v_pk_mul_f32 v[246:247], v[246:247], v[82:83] op_sel_hi:[1,0]\n"
    "v_pk_mul_f32 v[248:249], v[248:249], v[82:83] op_sel_hi:[1,0]\n"
    "v_pk_mul_f32 v[250:251], v[250:251], v[82:83] op_sel_hi:[1,0]\n"
    "v_pk_mul_f32 v[252:253], v[252:253], v[82:83] op_sel_hi:[1,0]\n"
    "v_pk_mul_f32 v[254:255], v[254:255], v[82:83] op_sel_hi:[1,0]\n"
    "v_cvt_pk_bf16_f32 v240, v240, v241\n"
    "v_cvt_pk_bf16_f32 v241, v242, v243\n"
    "v_cvt_pk_bf16_f32 v242, v244, v245\n"
    "v_cvt_pk_bf16_f32 v243, v246, v247\n"
    "v_cvt_pk_bf16_f32 v248, v248, v249\n"
    "v_cvt_pk_bf16_f32 v249, v250, v251\n"
    "v_cvt_pk_bf16_f32 v250, v252, v253\n"
    "v_cvt_pk_bf16_f32 v251, v254, v255\n"
    "ds_write_b128 v16, v[240:243] offset:2048\n"
    "ds_write_b128 v17, v[248:251] offset:2048\n"
    "v_accvgpr_read_b32 v32, a224\n"
    "v_accvgpr_read_b32 v33, a225\n"
    "v_accvgpr_read_b32 v34, a226\n"
    "v_accvgpr_read_b32 v35, a227\n"
    "v_accvgpr_read_b32 v36, a228\n"
    "v_accvgpr_read_b32 v37, a229\n"
    "v_accvgpr_read_b32 v38, a230\n"
    "v_accvgpr_read_b32 v39, a231\n"
    "v_accvgpr_read_b32 v40, a232\n"
    "v_accvgpr_read_b32 v41, a233\n"
    "v_accvgpr_read_b32 v42, a234\n"
    "v_accvgpr_read_b32 v43, a235\n"
    "v_accvgpr_read_b32 v44, a236\n"
    "v_accvgpr_read_b32 v45, a237\n"
    "v_accvgpr_read_b32 v46, a238\n"
    "v_accvgpr_read_b32 v47, a239\n"
    "ds_read_b128 v[72:75], v18 offset:2048\n"
    "ds_read_b128 v[76:79], v18 offset:3072\n"
    "v_pk_mul_f32 v[32:33], v[32:33], v[82:83] op_sel_hi:[1,0]\n"
    "v_pk_mul_f32 v[34:35], v[34:35], v[82:83] op_sel_hi:[1,0]\n"
    "v_pk_mul_f32 v[36:37], v[36:37], v[82:83] op_sel_hi:[1,0]\n"
    "v_pk_mul_f32 v[38:39], v[38:39], v[82:83] op_sel_hi:[1,0]\n"
    "v_pk_mul_f32 v[40:41], v[40:41], v[82:83] op_sel_hi:[1,0]\n"
    "v_pk_mul_f32 v[42:43], v[42:43], v[82:83] op_sel_hi:[1,0]\n"
    "v_pk_mul_f32 v[44:45], v[44:45], v[82:83] op_sel_hi:[1,0]\n"
    "v_pk_mul_f32 v[46:47], v[46:47], v[82:83] op_sel_hi:[1,0]\n"
    "v_cvt_pk_bf16_f32 v32, v32, v33\n"
    "v_cvt_pk_bf16_f32 v33, v34, v35\n"
    "v_cvt_pk_bf16_f32 v34, v36, v37\n"
    "v_cvt_pk_bf16_f32 v35, v38, v39\n"
    "v_cvt_pk_bf16_f32 v40, v40, v41\n"
    "v_cvt_pk_bf16_f32 v41, v42, v43\n"
    "v_cvt_pk_bf16_f32 v42, v44, v45\n"
    "v_cvt_pk_bf16_f32 v43, v46, v47\n"
    "s_waitcnt lgkmcnt(4)\n"
    "s_add_u32 s76, s78, 16384\n"
    "s_add_u32 s77, s78, 20480\n"
    "buffer_store_dwordx4 v[64:67], v19, s[48:51], s76 offen offset:0\n"
    "s_nop 0\n"
    "buffer_store_dwordx4 v[68:71], v19, s[48:51], s77 offen offset:0\n"
    "ds_write_b128 v16, v[32:35] offset:0\n"
    "ds_write_b128 v17, v[40:43] offset:0\n"
    "v_accvgpr_read_b32 v48, a240\n"
    "v_accvgpr_read_b32 v49, a241\n"
    "v_accvgpr_read_b32 v50, a242\n"
    "v_accvgpr_read_b32 v51, a243\n"
    "v_accvgpr_read_b32 v52, a244\n"
    "v_accvgpr_read_b32 v53, a245\n"
    "v_accvgpr_read_b32 v54, a246\n"
    "v_accvgpr_read_b32 v55, a247\n"
    "v_accvgpr_read_b32 v56, a248\n"
    "v_accvgpr_read_b32 v57, a249\n"
    "v_accvgpr_read_b32 v58, a250\n"
    "v_accvgpr_read_b32 v59, a251\n"
    "v_accvgpr_read_b32 v60, a252\n"
    "v_accvgpr_read_b32 v61, a253\n"
    "v_accvgpr_read_b32 v62, a254\n"
    "v_accvgpr_read_b32 v63, a255\n"
    "ds_read_b128 v[64:67], v18 offset:0\n"
    "ds_read_b128 v[68:71], v18 offset:1024\n"
    "v_pk_mul_f32 v[48:49], v[48:49], v[82:83] op_sel_hi:[1,0]\n"
    "v_pk_mul_f32 v[50:51], v[50:51], v[82:83] op_sel_hi:[1,0]\n"
    "v_pk_mul_f32 v[52:53], v[52:53], v[82:83] op_sel_hi:[1,0]\n"
    "v_pk_mul_f32 v[54:55], v[54:55], v[82:83] op_sel_hi:[1,0]\n"
    "v_pk_mul_f32 v[56:57], v[56:57], v[82:83] op_sel_hi:[1,0]\n"
    "v_pk_mul_f32 v[58:59], v[58:59], v[82:83] op_sel_hi:[1,0]\n"
    "v_pk_mul_f32 v[60:61], v[60:61], v[82:83] op_sel_hi:[1,0]\n"
    "v_pk_mul_f32 v[62:63], v[62:63], v[82:83] op_sel_hi:[1,0]\n"
    "v_cvt_pk_bf16_f32 v48, v48, v49\n"
    "v_cvt_pk_bf16_f32 v49, v50, v51\n"
    "v_cvt_pk_bf16_f32 v50, v52, v53\n"
    "v_cvt_pk_bf16_f32 v51, v54, v55\n"
    "v_cvt_pk_bf16_f32 v56, v56, v57\n"
    "v_cvt_pk_bf16_f32 v57, v58, v59\n"
    "v_cvt_pk_bf16_f32 v58, v60, v61\n"
    "v_cvt_pk_bf16_f32 v59, v62, v63\n"
    "s_waitcnt lgkmcnt(4)\n"
    "buffer_store_dwordx4 v[72:75], v19, s[48:51], s76 offen offset:128\n"
    "buffer_store_dwordx4 v[76:79], v19, s[48:51], s77 offen offset:128\n"
    "s_nop 0\n"
    "ds_write_b128 v16, v[48:51] offset:2048\n"
    "ds_write_b128 v17, v[56:59] offset:2048\n"
    "v_accvgpr_read_b32 v32, a160\n"
    "v_accvgpr_read_b32 v33, a161\n"
    "v_accvgpr_read_b32 v34, a162\n"
    "v_accvgpr_read_b32 v35, a163\n"
    "v_accvgpr_read_b32 v36, a164\n"
    "v_accvgpr_read_b32 v37, a165\n"
    "v_accvgpr_read_b32 v38, a166\n"
    "v_accvgpr_read_b32 v39, a167\n"
    "v_accvgpr_read_b32 v40, a168\n"
    "v_accvgpr_read_b32 v41, a169\n"
    "v_accvgpr_read_b32 v42, a170\n"
    "v_accvgpr_read_b32 v43, a171\n"
    "v_accvgpr_read_b32 v44, a172\n"
    "v_accvgpr_read_b32 v45, a173\n"
    "v_accvgpr_read_b32 v46, a174\n"
    "v_accvgpr_read_b32 v47, a175\n"
    "ds_read_b128 v[72:75], v18 offset:2048\n"
    "ds_read_b128 v[76:79], v18 offset:3072\n"
    "v_pk_mul_f32 v[32:33], v[32:33], v[82:83] op_sel:[0,1] op_sel_hi:[1,1]\n"
    "v_pk_mul_f32 v[34:35], v[34:35], v[82:83] op_sel:[0,1] op_sel_hi:[1,1]\n"
    "v_pk_mul_f32 v[36:37], v[36:37], v[82:83] op_sel:[0,1] op_sel_hi:[1,1]\n"
    "v_pk_mul_f32 v[38:39], v[38:39], v[82:83] op_sel:[0,1] op_sel_hi:[1,1]\n"
    "v_pk_mul_f32 v[40:41], v[40:41], v[82:83] op_sel:[0,1] op_sel_hi:[1,1]\n"
    "v_pk_mul_f32 v[42:43], v[42:43], v[82:83] op_sel:[0,1] op_sel_hi:[1,1]\n"
    "v_pk_mul_f32 v[44:45], v[44:45], v[82:83] op_sel:[0,1] op_sel_hi:[1,1]\n"
    "v_pk_mul_f32 v[46:47], v[46:47], v[82:83] op_sel:[0,1] op_sel_hi:[1,1]\n"
    "v_cvt_pk_bf16_f32 v32, v32, v33\n"
    "v_cvt_pk_bf16_f32 v33, v34, v35\n"
    "v_cvt_pk_bf16_f32 v34, v36, v37\n"
    "v_cvt_pk_bf16_f32 v35, v38, v39\n"
    "v_cvt_pk_bf16_f32 v40, v40, v41\n"
    "v_cvt_pk_bf16_f32 v41, v42, v43\n"
    "v_cvt_pk_bf16_f32 v42, v44, v45\n"
    "v_cvt_pk_bf16_f32 v43, v46, v47\n"
    "s_waitcnt lgkmcnt(4)\n"
    "buffer_store_dwordx4 v[64:67], v19, s[48:51], s76 offen offset:256\n"
    "buffer_store_dwordx4 v[68:71], v19, s[48:51], s77 offen offset:256\n"
    "ds_write_b128 v16, v[32:35] offset:0\n"
    "s_nop 0\n"
    "ds_write_b128 v17, v[40:43] offset:0\n"
    "v_accvgpr_read_b32 v48, a176\n"
    "v_accvgpr_read_b32 v49, a177\n"
    "v_accvgpr_read_b32 v50, a178\n"
    "v_accvgpr_read_b32 v51, a179\n"
    "v_accvgpr_read_b32 v52, a180\n"
    "v_accvgpr_read_b32 v53, a181\n"
    "v_accvgpr_read_b32 v54, a182\n"
    "v_accvgpr_read_b32 v55, a183\n"
    "v_accvgpr_read_b32 v56, a184\n"
    "v_accvgpr_read_b32 v57, a185\n"
    "v_accvgpr_read_b32 v58, a186\n"
    "v_accvgpr_read_b32 v59, a187\n"
    "v_accvgpr_read_b32 v60, a188\n"
    "v_accvgpr_read_b32 v61, a189\n"
    "v_accvgpr_read_b32 v62, a190\n"
    "v_accvgpr_read_b32 v63, a191\n"
    "ds_read_b128 v[64:67], v18 offset:0\n"
    "ds_read_b128 v[68:71], v18 offset:1024\n"
    "v_pk_mul_f32 v[48:49], v[48:49], v[82:83] op_sel:[0,1] op_sel_hi:[1,1]\n"
    "v_pk_mul_f32 v[50:51], v[50:51], v[82:83] op_sel:[0,1] op_sel_hi:[1,1]\n"
    "v_pk_mul_f32 v[52:53], v[52:53], v[82:83] op_sel:[0,1] op_sel_hi:[1,1]\n"
    "v_pk_mul_f32 v[54:55], v[54:55], v[82:83] op_sel:[0,1] op_sel_hi:[1,1]\n"
    "v_pk_mul_f32 v[56:57], v[56:57], v[82:83] op_sel:[0,1] op_sel_hi:[1,1]\n"
    "v_pk_mul_f32 v[58:59], v[58:59], v[82:83] op_sel:[0,1] op_sel_hi:[1,1]\n"
    "v_pk_mul_f32 v[60:61], v[60:61], v[82:83] op_sel:[0,1] op_sel_hi:[1,1]\n"
    "v_pk_mul_f32 v[62:63], v[62:63], v[82:83] op_sel:[0,1] op_sel_hi:[1,1]\n"
    "v_cvt_pk_bf16_f32 v48, v48, v49\n"
    "v_cvt_pk_bf16_f32 v49, v50, v51\n"
    "v_cvt_pk_bf16_f32 v50, v52, v53\n"
    "v_cvt_pk_bf16_f32 v51, v54, v55\n"
    "v_cvt_pk_bf16_f32 v56, v56, v57\n"
    "v_cvt_pk_bf16_f32 v57, v58, v59\n"
    "v_cvt_pk_bf16_f32 v58, v60, v61\n"
    "v_cvt_pk_bf16_f32 v59, v62, v63\n"
    "s_waitcnt lgkmcnt(4)\n"
    "s_nop 0\n"
    "buffer_store_dwordx4 v[72:75], v19, s[48:51], s76 offen offset:384\n"
    "buffer_store_dwordx4 v[76:79], v19, s[48:51], s77 offen offset:384\n"
    "ds_write_b128 v16, v[48:51] offset:2048\n"
    "ds_write_b128 v17, v[56:59] offset:2048\n"
    "v_accvgpr_read_b32 v32, a192\n"
    "v_accvgpr_read_b32 v33, a193\n"
    "v_accvgpr_read_b32 v34, a194\n"
    "v_accvgpr_read_b32 v35, a195\n"
    "v_accvgpr_read_b32 v36, a196\n"
    "v_accvgpr_read_b32 v37, a197\n"
    "v_accvgpr_read_b32 v38, a198\n"
    "v_accvgpr_read_b32 v39, a199\n"
    "v_accvgpr_read_b32 v40, a200\n"
    "v_accvgpr_read_b32 v41, a201\n"
    "v_accvgpr_read_b32 v42, a202\n"
    "v_accvgpr_read_b32 v43, a203\n"
    "v_accvgpr_read_b32 v44, a204\n"
    "v_accvgpr_read_b32 v45, a205\n"
    "v_accvgpr_read_b32 v46, a206\n"
    "v_accvgpr_read_b32 v47, a207\n"
    "ds_read_b128 v[72:75], v18 offset:2048\n"
    "ds_read_b128 v[76:79], v18 offset:3072\n"
    "v_pk_mul_f32 v[32:33], v[32:33], v[82:83] op_sel:[0,1] op_sel_hi:[1,1]\n"
    "v_pk_mul_f32 v[34:35], v[34:35], v[82:83] op_sel:[0,1] op_sel_hi:[1,1]\n"
    "v_pk_mul_f32 v[36:37], v[36:37], v[82:83] op_sel:[0,1] op_sel_hi:[1,1]\n"
    "v_pk_mul_f32 v[38:39], v[38:39], v[82:83] op_sel:[0,1] op_sel_hi:[1,1]\n"
    "v_pk_mul_f32 v[40:41], v[40:41], v[82:83] op_sel:[0,1] op_sel_hi:[1,1]\n"
    "v_pk_mul_f32 v[42:43], v[42:43], v[82:83] op_sel:[0,1] op_sel_hi:[1,1]\n"
    "v_pk_mul_f32 v[44:45], v[44:45], v[82:83] op_sel:[0,1] op_sel_hi:[1,1]\n"
    "v_pk_mul_f32 v[46:47], v[46:47], v[82:83] op_sel:[0,1] op_sel_hi:[1,1]\n"
    "v_cvt_pk_bf16_f32 v32, v32, v33\n"
    "v_cvt_pk_bf16_f32 v33, v34, v35\n"
    "v_cvt_pk_bf16_f32 v34, v36, v37\n"
    "v_cvt_pk_bf16_f32 v35, v38, v39\n"
    "v_cvt_pk_bf16_f32 v40, v40, v41\n"
    "v_cvt_pk_bf16_f32 v41, v42, v43\n"
    "v_cvt_pk_bf16_f32 v42, v44, v45\n"
    "v_cvt_pk_bf16_f32 v43, v46, v47\n"
    "s_waitcnt lgkmcnt(4)\n"
    "s_add_u32 s76, s78, 24576\n"
    "s_nop 0\n"
    "s_add_u32 s77, s78, 28672\n"
    "buffer_store_dwordx4 v[64:67], v19, s[48:51], s76 offen offset:0\n"
    "buffer_store_dwordx4 v[68:71], v19, s[48:51], s77 offen offset:0\n"
    "ds_write_b128 v16, v[32:35] offset:0\n"
    "ds_write_b128 v17, v[40:43] offset:0\n"
    "v_accvgpr_read_b32 v48, a208\n"
    "v_accvgpr_read_b32 v49, a209\n"
    "v_accvgpr_read_b32 v50, a210\n"
    "v_accvgpr_read_b32 v51, a211\n"
    "v_accvgpr_read_b32 v52, a212\n"
    "v_accvgpr_read_b32 v53, a213\n"
    "v_accvgpr_read_b32 v54, a214\n"
    "v_accvgpr_read_b32 v55, a215\n"
    "v_accvgpr_read_b32 v56, a216\n"
    "v_accvgpr_read_b32 v57, a217\n"
    "v_accvgpr_read_b32 v58, a218\n"
    "v_accvgpr_read_b32 v59, a219\n"
    "v_accvgpr_read_b32 v60, a220\n"
    "v_accvgpr_read_b32 v61, a221\n"
    "v_accvgpr_read_b32 v62, a222\n"
    "v_accvgpr_read_b32 v63, a223\n"
    "ds_read_b128 v[64:67], v18 offset:0\n"
    "ds_read_b128 v[68:71], v18 offset:1024\n"
    "v_pk_mul_f32 v[48:49], v[48:49], v[82:83] op_sel:[0,1] op_sel_hi:[1,1]\n"
    "v_pk_mul_f32 v[50:51], v[50:51], v[82:83] op_sel:[0,1] op_sel_hi:[1,1]\n"
    "v_pk_mul_f32 v[52:53], v[52:53], v[82:83] op_sel:[0,1] op_sel_hi:[1,1]\n"
    "v_pk_mul_f32 v[54:55], v[54:55], v[82:83] op_sel:[0,1] op_sel_hi:[1,1]\n"
    "v_pk_mul_f32 v[56:57], v[56:57], v[82:83] op_sel:[0,1] op_sel_hi:[1,1]\n"
    "v_pk_mul_f32 v[58:59], v[58:59], v[82:83] op_sel:[0,1] op_sel_hi:[1,1]\n"
    "v_pk_mul_f32 v[60:61], v[60:61], v[82:83] op_sel:[0,1] op_sel_hi:[1,1]\n"
    "v_pk_mul_f32 v[62:63], v[62:63], v[82:83] op_sel:[0,1] op_sel_hi:[1,1]\n"
    "v_cvt_pk_bf16_f32 v48, v48, v49\n"
    "v_cvt_pk_bf16_f32 v49, v50, v51\n"
    "v_cvt_pk_bf16_f32 v50, v52, v53\n"
    "v_cvt_pk_bf16_f32 v51, v54, v55\n"
    "v_cvt_pk_bf16_f32 v56, v56, v57\n"
    "v_cvt_pk_bf16_f32 v57, v58, v59\n"
    "v_cvt_pk_bf16_f32 v58, v60, v61\n"
    "v_cvt_pk_bf16_f32 v59, v62, v63\n"
    "s_waitcnt lgkmcnt(4)\n"
    "s_nop 0\n"
    "buffer_store_dwordx4 v[72:75], v19, s[48:51], s76 offen offset:128\n"
    "buffer_store_dwordx4 v[76:79], v19, s[48:51], s77 offen offset:128\n"
    "ds_write_b128 v16, v[48:51] offset:2048\n"
    "ds_write_b128 v17, v[56:59] offset:2048\n"
    "v_cvt_pk_bf16_f32 v129, v130, v131\n"
    "v_cvt_pk_bf16_f32 v130, v132, v133\n"
    "v_cvt_pk_bf16_f32 v131, v134, v135\n"
    "v_pk_mul_f32 v[192:193], v[192:193], v[80:81] op_sel:[0,1] op_sel_hi:[1,1]\n"
    "v_pk_mul_f32 v[194:195], v[194:195], v[80:81] op_sel:[0,1] op_sel_hi:[1,1]\n"
    "v_pk_mul_f32 v[196:197], v[196:197], v[80:81] op_sel:[0,1] op_sel_hi:[1,1]\n"
    "v_pk_mul_f32 v[198:199], v[198:199], v[80:81] op_sel:[0,1] op_sel_hi:[1,1]\n"
    "v_cvt_pk_bf16_f32 v192, v192, v193\n"
    "v_cvt_pk_bf16_f32 v193, v194, v195\n"
    "v_cvt_pk_bf16_f32 v194, v196, v197\n"
    "v_cvt_pk_bf16_f32 v195, v198, v199\n"
    "v_pk_mul_f32 v[136:137], v[136:137], v[80:81] op_sel_hi:[1,0]\n"
    "v_pk_mul_f32 v[138:139], v[138:139], v[80:81] op_sel_hi:[1,0]\n"
    "v_pk_mul_f32 v[140:141], v[140:141], v[80:81] op_sel_hi:[1,0]\n"
    "v_pk_mul_f32 v[142:143], v[142:143], v[80:81] op_sel_hi:[1,0]\n"
    "v_cvt_pk_bf16_f32 v136, v136, v137\n"
    "ds_read_b128 v[72:75], v18 offset:2048\n"
    "ds_read_b128 v[76:79], v18 offset:3072\n"
    "v_cvt_pk_bf16_f32 v137, v138, v139\n"
    "v_cvt_pk_bf16_f32 v138, v140, v141\n"
    "v_cvt_pk_bf16_f32 v139, v142, v143\n"
    "v_pk_mul_f32 v[200:201], v[200:201], v[80:81] op_sel:[0,1] op_sel_hi:[1,1]\n"
    "v_pk_mul_f32 v[202:203], v[202:203], v[80:81] op_sel:[0,1] op_sel_hi:[1,1]\n"
    "v_pk_mul_f32 v[204:205], v[204:205], v[80:81] op_sel:[0,1] op_sel_hi:[1,1]\n"
    "v_pk_mul_f32 v[206:207], v[206:207], v[80:81] op_sel:[0,1] op_sel_hi:[1,1]\n"
    "v_cvt_pk_bf16_f32 v200, v200, v201\n"
    "v_cvt_pk_bf16_f32 v201, v202, v203\n"
    "v_cvt_pk_bf16_f32 v202, v204, v205\n"
    "v_cvt_pk_bf16_f32 v203, v206, v207\n"
    "v_pk_mul_f32 v[144:145], v[144:145], v[80:81] op_sel_hi:[1,0]\n"
    "v_pk_mul_f32 v[146:147], v[146:147], v[80:81] op_sel_hi:[1,0]\n"
    "v_pk_mul_f32 v[148:149], v[148:149], v[80:81] op_sel_hi:[1,0]\n"
    "v_pk_mul_f32 v[150:151], v[150:151], v[80:81] op_sel_hi:[1,0]\n"
    "v_cvt_pk_bf16_f32 v144, v144, v145\n"
    "s_waitcnt lgkmcnt(4)\n"
    "buffer_store_dwordx4 v[64:67], v19, s[48:51], s76 offen offset:256\n"
    "s_nop 0\n"
    "buffer_store_dwordx4 v[68:71], v19, s[48:51], s77 offen offset:256\n"
    "v_cvt_pk_bf16_f32 v145, v146, v147\n"
    "v_cvt_pk_bf16_f32 v146, v148, v149\n"
    "v_cvt_pk_bf16_f32 v147, v150, v151\n"
    "v_pk_mul_f32 v[208:209], v[208:209], v[80:81] op_sel:[0,1] op_sel_hi:[1,1]\n"
    "v_pk_mul_f32 v[210:211], v[210:211], v[80:81] op_sel:[0,1] op_sel_hi:[1,1]\n"
    "v_pk_mul_f32 v[212:213], v[212:213], v[80:81] op_sel:[0,1] op_sel_hi:[1,1]\n"
    "v_pk_mul_f32 v[214:215], v[214:215], v[80:81] op_sel:[0,1] op_sel_hi:[1,1]\n"
    "v_cvt_pk_bf16_f32 v208, v208, v209\n"
    "v_cvt_pk_bf16_f32 v209, v210, v211\n"
    "v_cvt_pk_bf16_f32 v210, v212, v213\n"
    "v_cvt_pk_bf16_f32 v211, v214, v215\n"
    "v_pk_mul_f32 v[152:153], v[152:153], v[80:81] op_sel_hi:[1,0]\n"
    "v_pk_mul_f32 v[154:155], v[154:155], v[80:81] op_sel_hi:[1,0]\n"
    "s_waitcnt vmcnt(14)\n"
    "ds_read_b128 a[64:67], v94 offset:0\n"
    "s_nop 0\n"
    "ds_read_b128 a[68:71], v95 offset:0\n"
    "ds_read_b128 a[72:75], v94 offset:1024\n"
    "ds_read_b128 a[76:79], v95 offset:1024\n"
    "ds_read_b128 a[80:83], v94 offset:2048\n"
    "ds_read_b128 a[84:87], v95 offset:2048\n"
    "ds_read_b128 a[88:91], v94 offset:3072\n"
    "ds_read_b128 a[92:95], v95 offset:3072\n"
    "v_pk_mul_f32 v[156:157], v[156:157], v[80:81] op_sel_hi:[1,0]\n"
    "v_pk_mul_f32 v[158:159], v[158:159], v[80:81] op_sel_hi:[1,0]\n"
    "v_cvt_pk_bf16_f32 v152, v152, v153\n"
    "v_cvt_pk_bf16_f32 v153, v154, v155\n"
    "v_cvt_pk_bf16_f32 v154, v156, v157\n"
    "v_cvt_pk_bf16_f32 v155, v158, v159\n"
    "v_pk_mul_f32 v[216:217], v[216:217], v[80:81] op_sel:[0,1] op_sel_hi:[1,1]\n"
    "v_pk_mul_f32 v[218:219], v[218:219], v[80:81] op_sel:[0,1] op_sel_hi:[1,1]\n"
    "v_pk_mul_f32 v[220:221], v[220:221], v[80:81] op_sel:[0,1] op_sel_hi:[1,1]\n"
    "v_pk_mul_f32 v[222:223], v[222:223], v[80:81] op_sel:[0,1] op_sel_hi:[1,1]\n"
    "v_cvt_pk_bf16_f32 v216, v216, v217\n"
    "v_cvt_pk_bf16_f32 v217, v218, v219\n"
    "v_cvt_pk_bf16_f32 v218, v220, v221\n"
    "v_cvt_pk_bf16_f32 v219, v222, v223\n"
    "s_waitcnt lgkmcnt(8)\n"
    "buffer_store_dwordx4 v[72:75], v19, s[48:51], s76 offen offset:384\n"
    "buffer_store_dwordx4 v[76:79], v19, s[48:51], s77 offen offset:384\n"
    "LSP3_%=:\n"
    "s_cmp_eq_u32 s80, 1\n"
    "s_cbranch_scc1 GLSP3_%=\n"
    "s_waitcnt lgkmcnt(6)\n"
    "v_mfma_f32_16x16x32_bf16 v[0:3], a[128:131], a[0:3], 0\n"
    "ds_write_b128 v94, v[96:99] offset:0\n"
    "v_mfma_f32_16x16x32_bf16 v[0:3], a[132:135], a[4:7], v[0:3]\n"
    "v_mfma_f32_16x16x32_bf16 v[4:7], a[128:131], a[32:35], 0\n"
    "ds_write_b128 v95, v[104:107] offset:0\n"
    "v_mfma_f32_16x16x32_bf16 v[4:7], a[132:135], a[36:39], v[4:7]\n"
    "v_mfma_f32_16x16x32_bf16 v[8:11], a[128:131], a[64:67], 0\n"
    "s_nop 0\n"
    "ds_read_b128 a[128:131], v88 offset:8192\n"
    "v_mfma_f32_16x16x32_bf16 v[8:11], a[132:135], a[68:71], v[8:11]\n"
    "s_waitcnt lgkmcnt(7)\n"
    "ds_read_b128 a[132:135], v89 offset:8192\n"
    "v_mfma_f32_16x16x32_bf16 v[0:3], a[136:139], a[8:11], v[0:3]\n"
    "ds_write_b128 v94, v[112:115] offset:1024\n"
    "v_mfma_f32_16x16x32_bf16 v[0:3], a[140:143], a[12:15], v[0:3]\n"
    "v_mfma_f32_16x16x32_bf16 v[4:7], a[136:139], a[40:43], v[4:7]\n"
    "ds_write_b128 v95, v[120:123] offset:1024\n"
    "v_mfma_f32_16x16x32_bf16 v[4:7], a[140:143], a[44:47], v[4:7]\n"
    "v_mfma_f32_16x16x32_bf16 v[8:11], a[136:139], a[72:75], v[8:11]\n"
    "ds_read_b128 a[136:139], v88 offset:9216\n"
    "v_mfma_f32_16x16x32_bf16 v[8:11], a[140:143], a[76:79], v[8:11]\n"
    "s_waitcnt lgkmcnt(9)\n"
    "ds_read_b128 a[140:143], v89 offset:9216\n"
    "v_mfma_f32_16x16x32_bf16 v[0:3], a[144:147], a[16:19], v[0:3]\n"
    "s_nop 0\n"
    "ds_write_b128 v94, v[128:131] offset:2048\n"
    "v_mfma_f32_16x16x32_bf16 v[0:3], a[148:151], a[20:23], v[0:3]\n"
    "v_mfma_f32_16x16x32_bf16 v[4:7], a[144:147], a[48:51], v[4:7]\n"
    "ds_write_b128 v95, v[136:139] offset:2048\n"
    "v_mfma_f32_16x16x32_bf16 v[4:7], a[148:151], a[52:55], v[4:7]\n"
    "v_mfma_f32_16x16x32_bf16 v[8:11], a[144:147], a[80:83], v[8:11]\n"
    "ds_read_b128 a[144:147], v88 offset:10240\n"
    "v_mfma_f32_16x16x32_bf16 v[8:11], a[148:151], a[84:87], v[8:11]\n"
    "s_waitcnt lgkmcnt(11)\n"
    "ds_read_b128 a[148:151], v89 offset:10240\n"
    "v_mfma_f32_16x16x32_bf16 v[0:3], a[152:155], a[24:27], v[0:3]\n"
    "s_nop 0\n"
    "ds_write_b128 v94, v[144:147] offset:3072\n"
    "v_mfma_f32_16x16x32_bf16 v[0:3], a[156:159], a[28:31], v[0:3]\n"
    "v_mfma_f32_16x16x32_bf16 v[4:7], a[152:155], a[56:59], v[4:7]\n"
    "ds_write_b128 v95, v[152:155] offset:3072\n"
    "v_mfma_f32_16x16x32_bf16 v[4:7], a[156:159], a[60:63], v[4:7]\n"
    "v_mfma_f32_16x16x32_bf16 v[8:11], a[152:155], a[88:91], v[8:11]\n"
    "ds_read_b128 a[152:155], v88 offset:11264\n"
    "ds_read_b128 v[48:51], v93 offset:0\n"
    "v_mfma_f32_16x16x32_bf16 v[8:11], a[156:159], a[92:95], v[8:11]\n"
    "s_waitcnt lgkmcnt(12)\n"
    "ds_read_b128 a[156:159], v89 offset:11264\n"
    "ds_read_b128 v[52:55], v93 offset:1024\n"
    "v_mfma_f32_16x16x32_bf16 v[16:19], a[128:131], a[0:3], 0\n"
    "s_nop 0\n"
    "ds_write_b128 v94, v[160:163] offset:8192\n"
    "v_mfma_f32_16x16x32_bf16 v[16:19], a[132:135], a[4:7], v[16:19]\n"
    "ds_read_b128 v[56:59], v93 offset:2048\n"
    "ds_read_b128 v[60:63], v93 offset:3072\n"
    "v_mfma_f32_16x16x32_bf16 v[20:23], a[128:131], a[32:35], 0\n"
    "s_nop 0\n"
    "ds_write_b128 v95, v[168:171] offset:8192\n"
    "v_mfma_f32_16x16x32_bf16 v[20:23], a[132:135], a[36:39], v[20:23]\n"
    "s_nop 0\n"
    "v_max3_f32 v64, v0, v1, v2\n"
    "v_max_f32_e64 v64, v64, v3\n"
    "v_mfma_f32_16x16x32_bf16 v[24:27], a[128:131], a[64:67], 0\n"
    "ds_read_b128 a[128:131], v88 offset:16384\n"
    "v_mov_b32 v65, v64\n"
    "v_mfma_f32_16x16x32_bf16 v[24:27], a[132:135], a[68:71], v[24:27]\n"
    "s_waitcnt lgkmcnt(15)\n"
    "ds_read_b128 a[132:135], v89 offset:16384\n"
    "v_max3_f32 v66, v4, v5, v6\n"
    "v_mfma_f32_16x16x32_bf16 v[16:19], a[136:139], a[8:11], v[16:19]\n"
    "s_nop 0\n"
    "ds_write_b128 v94, v[176:179] offset:9216\n"
    "v_mfma_f32_16x16x32_bf16 v[16:19], a[140:143], a[12:15], v[16:19]\n"
    "s_waitcnt lgkmcnt(4)\n"
    "buffer_store_dwordx4 v[48:51], v92, s[48:51], s98 offen offset:0 nt\n"
    "v_mfma_f32_16x16x32_bf16 v[20:23], a[136:139], a[40:43], v[20:23]\n"
    "ds_write_b128 v95, v[184:187] offset:9216\n"
    "v_mfma_f32_16x16x32_bf16 v[20:23], a[140:143], a[44:47], v[20:23]\n"
    "buffer_store_dwordx4 v[52:55], v92, s[48:51], s98 offen offset:128 nt\n"
    "v_mfma_f32_16x16x32_bf16 v[24:27], a[136:139], a[72:75], v[24:27]\n"
    "ds_read_b128 a[136:139], v88 offset:17408\n"
    "v_max_f32_e64 v66, v66, v7\n"
    "v_mfma_f32_16x16x32_bf16 v[24:27], a[140:143], a[76:79], v[24:27]\n"
    "s_waitcnt lgkmcnt(15)\n"
    "ds_read_b128 a[140:143], v89 offset:17408\n"
    "v_permlane32_swap_b32 v64, v65\n"
    "v_mov_b32_e64 v67, v66\n"
    "v_mfma_f32_16x16x32_bf16 v[16:19], a[144:147], a[16:19], v[16:19]\n"
    "ds_write_b128 v94, v[192:195] offset:10240\n"
    "v_max_f32 v64, v64, v65\n"
    "v_mov_b32 v65, v64\n"
    "v_max3_f32 v68, v8, v9, v10\n"
    "v_max_f32 v68, v68, v11\n"
    "v_mfma_f32_16x16x32_bf16 v[16:19], a[148:151], a[20:23], v[16:19]\n"
    "buffer_store_dwordx4 v[56:59], v92, s[48:51], s98 offen offset:256 nt\n"
    "v_permlane32_swap_b32 v66, v67\n"
    "v_mov_b32_e64 v69, v68\n"
    "v_mfma_f32_16x16x32_bf16 v[20:23], a[144:147], a[48:51], v[20:23]\n"
    "ds_write_b128 v95, v[200:203] offset:10240\n"
    "v_permlane16_swap_b32 v64, v65\n"
    "v_max_f32 v66, v66, v67\n"
    "v_mov_b32_e64 v67, v66\n"
    "v_mfma_f32_16x16x32_bf16 v[20:23], a[148:151], a[52:55], v[20:23]\n"
    "buffer_store_dwordx4 v[60:63], v92, s[48:51], s98 offen offset:384 nt\n"
    "v_max_f32 v64, v64, v65\n"
    "v_permlane32_swap_b32 v68, v69\n"
    "v_mfma_f32_16x16x32_bf16 v[24:27], a[144:147], a[80:83], v[24:27]\n"
    "s_add_u32 s98, s98, 0x1000\n"
    "ds_read_b128 a[144:147], v88 offset:18432\n"
    "v_mul_f32 v84, s81, v64\n"
    "v_permlane16_swap_b32 v66, v67\n"
    "v_max_f32 v68, v68, v69\n"
    "v_mov_b32 v69, v68\n"
    "v_mfma_f32_16x16x32_bf16 v[24:27], a[148:151], a[84:87], v[24:27]\n"
    "s_waitcnt lgkmcnt(14)\n"
    "ds_read_b128 a[148:151], v89 offset:18432\n"
    "v_max_f32 v66, v66, v67\n"
    "v_mfma_f32_16x16x32_bf16 v[16:19], a[152:155], a[24:27], v[16:19]\n"
    "s_mov_b32 m0, s63\n"
    "ds_write_b128 v94, v[208:211] offset:11264\n"
    "v_mul_f32_e64 v85, s81, v66\n"
    "v_permlane16_swap_b32 v68, v69\n"
    "v_mfma_f32_16x16x32_bf16 v[16:19], a[156:159], a[28:31], v[16:19]\n"
    "buffer_load_dwordx4 v92, s[36:39], s53 offen offset:0 lds\n"
    "v_max_f32 v68, v68, v69\n"
    "v_mfma_f32_16x16x32_bf16 v[20:23], a[152:155], a[56:59], v[20:23]\n"
    "s_add_u32 m0, m0, s97\n"
    "ds_write_b128 v95, v[216:219] offset:11264\n"
    "v_mul_f32_e64 v86, s81, v68\n"
    "v_mfma_f32_16x16x32_bf16 v[20:23], a[156:159], a[60:63], v[20:23]\n"
    "v_fma_f32 v0, v0, s52, v84\n"
    "v_fma_f32 v2, v2, s52, v84\n"
    "v_mfma_f32_16x16x32_bf16 v[24:27], a[152:155], a[88:91], v[24:27]\n"
    "ds_read_b128 a[152:155], v88 offset:19456\n"
    "v_fma_f32 v4, v4, s52, v85\n"
    "v_mfma_f32_16x16x32_bf16 v[24:27], a[156:159], a[92:95], v[24:27]\n"
    "s_waitcnt lgkmcnt(11)\n"
    "ds_read_b128 a[156:159], v89 offset:19456\n"
    "v_fma_f32 v6, v6, s52, v85\n"
    "v_mfma_f32_16x16x32_bf16 v[32:35], a[128:131], a[0:3], 0\n"
    "s_nop 0\n"
    "buffer_load_dwordx4 v92, s[36:39], s53 offen offset:128 lds\n"
    "v_mfma_f32_16x16x32_bf16 v[32:35], a[132:135], a[4:7], v[32:35]\n"
    "s_add_u32 m0, m0, s97\n"
    "v_exp_f32 v0, v0\n"
    "v_mfma_f32_16x16x32_bf16 v[36:39], a[128:131], a[32:35], 0\n"
    "v_exp_f32_e64 v2, v2\n"
    "v_mfma_f32_16x16x32_bf16 v[36:39], a[132:135], a[36:39], v[36:39]\n"
    "buffer_load_dwordx4 v92, s[36:39], s53 offen offset:256 lds\n"
    "v_mfma_f32_16x16x32_bf16 v[40:43], a[128:131], a[64:67], 0\n"
    "s_add_u32 m0, m0, s97\n"
    "ds_read_b128 a[128:131], v88 offset:24576\n"
    "v_fma_f32 v8, v8, s52, v86\n"
    "v_mfma_f32_16x16x32_bf16 v[40:43], a[132:135], a[68:71], v[40:43]\n"
    "s_waitcnt lgkmcnt(9)\n"
    "ds_read_b128 a[132:135], v89 offset:24576\n"
    "v_fma_f32 v10, v10, s52, v86\n"
    "v_mfma_f32_16x16x32_bf16 v[32:35], a[136:139], a[8:11], v[32:35]\n"
    "buffer_load_dwordx4 v92, s[36:39], s53 offen offset:384 lds\n"
    "v_mfma_f32_16x16x32_bf16 v[32:35], a[140:143], a[12:15], v[32:35]\n"
    "s_add_u32 m0, m0, 1408\n"
    "v_exp_f32_e64 v4, v4\n"
    "v_mfma_f32_16x16x32_bf16 v[36:39], a[136:139], a[40:43], v[36:39]\n"
    "v_exp_f32_e64 v6, v6\n"
    "v_mfma_f32_16x16x32_bf16 v[36:39], a[140:143], a[44:47], v[36:39]\n"
    "buffer_load_dwordx4 v92, s[36:39], s54 offen offset:0 lds\n"
    "v_mfma_f32_16x16x32_bf16 v[40:43], a[136:139], a[72:75], v[40:43]\n"
    "s_add_u32 m0, m0, s97\n"
    "ds_read_b128 a[136:139], v88 offset:25600\n"
    "v_fma_f32 v1, v1, s52, v84\n"
    "v_mfma_f32_16x16x32_bf16 v[40:43], a[140:143], a[76:79], v[40:43]\n"
    "s_waitcnt lgkmcnt(7)\n"
    "ds_read_b128 a[140:143], v89 offset:25600\n"
    "v_fma_f32 v3, v3, s52, v84\n"
    "v_mfma_f32_16x16x32_bf16 v[32:35], a[144:147], a[16:19], v[32:35]\n"
    "buffer_load_dwordx4 v92, s[36:39], s54 offen offset:128 lds\n"
    "v_mfma_f32_16x16x32_bf16 v[32:35], a[148:151], a[20:23], v[32:35]\n"
    "s_add_u32 m0, m0, 896\n"
    "v_exp_f32_e64 v1, v1\n"
    "v_mfma_f32_16x16x32_bf16 v[36:39], a[144:147], a[48:51], v[36:39]\n"
    "v_exp_f32_e64 v8, v8\n"
    "v_mfma_f32_16x16x32_bf16 v[36:39], a[148:151], a[52:55], v[36:39]\n"
    "buffer_load_dwordx4 v92, s[36:39], s54 offen offset:256 lds\n"
    "v_mfma_f32_16x16x32_bf16 v[40:43], a[144:147], a[80:83], v[40:43]\n"
    "s_add_u32 m0, m0, s97\n"
    "ds_read_b128 a[144:147], v88 offset:26624\n"
    "v_fma_f32 v5, v5, s52, v85\n"
    "v_mfma_f32_16x16x32_bf16 v[40:43], a[148:151], a[84:87], v[40:43]\n"
    "s_waitcnt lgkmcnt(5)\n"
    "ds_read_b128 a[148:151], v89 offset:26624\n"
    "v_fma_f32 v7, v7, s52, v85\n"
    "v_mfma_f32_16x16x32_bf16 v[32:35], a[152:155], a[24:27], v[32:35]\n"
    "buffer_load_dwordx4 v92, s[36:39], s54 offen offset:384 lds\n"
    "v_mfma_f32_16x16x32_bf16 v[32:35], a[156:159], a[28:31], v[32:35]\n"
    "s_add_u32 s53, s53, 0x8000\n"
    "v_exp_f32_e64 v5, v5\n"
    "v_mfma_f32_16x16x32_bf16 v[36:39], a[152:155], a[56:59], v[36:39]\n"
    "s_add_u32 s54, s54, 0x8000\n"
    "v_exp_f32_e64 v10, v10\n"
    "v_mfma_f32_16x16x32_bf16 v[36:39], a[156:159], a[60:63], v[36:39]\n"
    "v_exp_f32_e64 v3, v3\n"
    "v_mfma_f32_16x16x32_bf16 v[40:43], a[152:155], a[88:91], v[40:43]\n"
    "ds_read_b128 a[152:155], v88 offset:27648\n"
    "v_fma_f32 v9, v9, s52, v86\n"
    "v_mfma_f32_16x16x32_bf16 v[40:43], a[156:159], a[92:95], v[40:43]\n"
    "s_waitcnt lgkmcnt(5)\n"
    "ds_read_b128 a[156:159], v89 offset:27648\n"
    "v_fma_f32 v11, v11, s52, v86\n"
    "v_mfma_f32_16x16x32_bf16 v[48:51], a[128:131], a[0:3], 0\n"
    "v_exp_f32 v9, v9\n"
    "v_mfma_f32_16x16x32_bf16 v[48:51], a[132:135], a[4:7], v[48:51]\n"
    "v_exp_f32 v7, v7\n"
    "v_mfma_f32_16x16x32_bf16 v[52:55], a[128:131], a[32:35], 0\n"
    "v_exp_f32 v11, v11\n"
    "v_mfma_f32_16x16x32_bf16 v[52:55], a[132:135], a[36:39], v[52:55]\n"
    "v_add_f32 v80, v0, v1\n"
    "v_add_f32 v81, v4, v5\n"
    "v_add_f32 v82, v8, v9\n"
    "v_mfma_f32_16x16x32_bf16 v[56:59], a[128:131], a[64:67], 0\n"
    "s_waitcnt lgkmcnt(0)\n"
    "v_add_f32 v80, v80, v2\n"
    "v_add_f32 v81, v81, v6\n"
    "v_add_f32 v82, v82, v10\n"
    "v_mfma_f32_16x16x32_bf16 v[56:59], a[132:135], a[68:71], v[56:59]\n"
    "s_barrier\n"
    "v_add_f32_e64 v80, v80, v3\n"
    "v_add_f32_e64 v81, v81, v7\n"
    "v_add_f32 v82, v82, v11\n"
    "v_mfma_f32_16x16x32_bf16 v[48:51], a[136:139], a[8:11], v[48:51]\n"
    "s_mov_b32 m0, s60\n"
    "ds_read_b64_tr_b16 a[128:129], v90 offset:16384\n"
    "ds_read_b64_tr_b16 a[130:131], v90 offset:24576\n"
    "v_mfma_f32_16x16x32_bf16 v[48:51], a[140:143], a[12:15], v[48:51]\n"
    "ds_read_b64_tr_b16 a[132:133], v90 offset:16392\n"
    "ds_read_b64_tr_b16 a[134:135], v90 offset:24584\n"
    "v_mfma_f32_16x16x32_bf16 v[52:55], a[136:139], a[40:43], v[52:55]\n"
    "buffer_load_dwordx4 v92, s[40:43], s55 offen offset:0 lds\n"
    "v_mfma_f32_16x16x32_bf16 v[52:55], a[140:143], a[44:47], v[52:55]\n"
    "s_add_u32 m0, m0, 896\n"
    "v_cvt_pk_bf16_f32 v0, v0, v1\n"
    "v_cvt_pk_bf16_f32 v1, v2, v3\n"
    "v_mfma_f32_16x16x32_bf16 v[56:59], a[136:139], a[72:75], v[56:59]\n"
    "ds_read_b64_tr_b16 a[136:137], v91 offset:16384\n"
    "ds_read_b64_tr_b16 a[138:139], v91 offset:24576\n"
    "v_mfma_f32_16x16x32_bf16 v[56:59], a[140:143], a[76:79], v[56:59]\n"
    "ds_read_b64_tr_b16 a[140:141], v91 offset:16392\n"
    "ds_read_b64_tr_b16 a[142:143], v91 offset:24584\n"
    "v_mfma_f32_16x16x32_bf16 v[48:51], a[144:147], a[16:19], v[48:51]\n"
    "buffer_load_dwordx4 v92, s[40:43], s55 offen offset:128 lds\n"
    "v_cvt_pk_bf16_f32 v4, v4, v5\n"
    "v_cvt_pk_bf16_f32 v5, v6, v7\n"
    "v_cvt_pk_bf16_f32 v8, v8, v9\n"
    "v_cvt_pk_bf16_f32 v9, v10, v11\n"
    "v_mfma_f32_16x16x32_bf16 v[48:51], a[148:151], a[20:23], v[48:51]\n"
    "s_add_u32 m0, m0, 896\n"
    "v_fma_f32 v16, v16, s52, v84\n"
    "v_fma_f32 v18, v18, s52, v84\n"
    "v_mfma_f32_16x16x32_bf16 v[52:55], a[144:147], a[48:51], v[52:55]\n"
    "v_exp_f32 v16, v16\n"
    "v_mfma_f32_16x16x32_bf16 v[52:55], a[148:151], a[52:55], v[52:55]\n"
    "v_exp_f32 v18, v18\n"
    "v_mfma_f32_16x16x32_bf16 v[56:59], a[144:147], a[80:83], v[56:59]\n"
    "ds_read_b64_tr_b16 a[144:145], v90 offset:17408\n"
    "ds_read_b64_tr_b16 a[146:147], v90 offset:25600\n"
    "v_fma_f32 v20, v20, s52, v85\n"
    "v_fma_f32 v22, v22, s52, v85\n"
    "v_fma_f32 v24, v24, s52, v86\n"
    "v_fma_f32 v26, v26, s52, v86\n"
    "v_mfma_f32_16x16x32_bf16 v[56:59], a[148:151], a[84:87], v[56:59]\n"
    "ds_read_b64_tr_b16 a[148:149], v90 offset:17416\n"
    "ds_read_b64_tr_b16 a[150:151], v90 offset:25608\n"
    "v_fma_f32 v17, v17, s52, v84\n"
    "v_fma_f32 v19, v19, s52, v84\n"
    "v_fma_f32 v21, v21, s52, v85\n"
    "v_exp_f32 v20, v20\n"
    "v_fma_f32 v23, v23, s52, v85\n"
    "v_fma_f32 v25, v25, s52, v86\n"
    "v_exp_f32 v24, v24\n"
    "v_fma_f32 v27, v27, s52, v86\n"
    "v_mfma_f32_16x16x32_bf16 v[48:51], a[152:155], a[24:27], v[48:51]\n"
    "buffer_load_dwordx4 v92, s[40:43], s55 offen offset:256 lds\n"
    "v_exp_f32 v17, v17\n"
    "v_add_f32 v80, v80, v16\n"
    "v_exp_f32 v21, v21\n"
    "v_add_f32 v81, v81, v20\n"
    "v_exp_f32 v22, v22\n"
    "v_exp_f32 v25, v25\n"
    "v_add_f32 v82, v82, v24\n"
    "v_exp_f32 v26, v26\n"
    "v_mfma_f32_16x16x32_bf16 v[48:51], a[156:159], a[28:31], v[48:51]\n"
    "s_add_u32 m0, m0, s97\n"
    "v_add_f32 v80, v80, v17\n"
    "v_exp_f32 v19, v19\n"
    "v_add_f32 v81, v81, v21\n"
    "v_exp_f32_e64 v23, v23\n"
    "v_add_f32_e64 v82, v82, v25\n"
    "v_exp_f32_e64 v27, v27\n"
    "v_mfma_f32_16x16x32_bf16 v[52:55], a[152:155], a[56:59], v[52:55]\n"
    "v_add_f32_e64 v80, v80, v18\n"
    "v_add_f32_e64 v81, v81, v22\n"
    "v_add_f32_e64 v82, v82, v26\n"
    "v_mfma_f32_16x16x32_bf16 v[52:55], a[156:159], a[60:63], v[52:55]\n"
    "v_cvt_pk_bf16_f32 v2, v16, v17\n"
    "v_add_f32_e64 v80, v80, v19\n"
    "v_cvt_pk_bf16_f32 v3, v18, v19\n"
    "v_add_f32_e64 v81, v81, v23\n"
    "v_add_f32_e64 v82, v82, v27\n"
    "v_mfma_f32_16x16x32_bf16 v[56:59], a[152:155], a[88:91], v[56:59]\n"
    "s_nop 0\n"
    "ds_read_b64_tr_b16 a[152:153], v91 offset:17408\n"
    "ds_read_b64_tr_b16 a[154:155], v91 offset:25600\n"
    "v_mfma_f32_16x16x32_bf16 v[56:59], a[156:159], a[92:95], v[56:59]\n"
    "s_waitcnt lgkmcnt(10)\n"
    "ds_read_b64_tr_b16 a[156:157], v91 offset:17416\n"
    "ds_read_b64_tr_b16 a[158:159], v91 offset:25608\n"
    "v_cvt_pk_bf16_f32 v6, v20, v21\n"
    "v_cvt_pk_bf16_f32 v7, v22, v23\n"
    "v_mfma_f32_16x16x32_bf16 v[96:99], a[128:131], v[0:3], 0\n"
    "buffer_load_dwordx4 v92, s[40:43], s55 offen offset:384 lds\n"
    "v_mfma_f32_16x16x32_bf16 v[100:103], a[132:135], v[0:3], 0\n"
    "s_add_u32 m0, m0, 1408\n"
    "v_cvt_pk_bf16_f32 v10, v24, v25\n"
    "v_cvt_pk_bf16_f32 v11, v26, v27\n"
    "v_mfma_f32_16x16x32_bf16 v[160:163], a[128:131], v[4:7], 0\n"
    "v_fma_f32 v32, v32, s52, v84\n"
    "v_fma_f32 v34, v34, s52, v84\n"
    "v_mfma_f32_16x16x32_bf16 v[164:167], a[132:135], v[4:7], 0\n"
    "v_exp_f32_e64 v32, v32\n"
    "v_mfma_f32_16x16x32_bf16 v[224:227], a[128:131], v[8:11], 0\n"
    "s_nop 0\n"
    "ds_read_b64_tr_b16 a[128:129], v90 offset:18432\n"
    "ds_read_b64_tr_b16 a[130:131], v90 offset:26624\n"
    "v_mfma_f32_16x16x32_bf16 v[228:231], a[132:135], v[8:11], 0\n"
    "s_waitcnt lgkmcnt(10)\n"
    "ds_read_b64_tr_b16 a[132:133], v90 offset:18440\n"
    "ds_read_b64_tr_b16 a[134:135], v90 offset:26632\n"
    "v_mfma_f32_16x16x32_bf16 v[104:107], a[136:139], v[0:3], 0\n"
    "buffer_load_dwordx4 v92, s[40:43], s56 offen offset:0 lds\n"
    "v_mfma_f32_16x16x32_bf16 v[108:111], a[140:143], v[0:3], 0\n"
    "s_add_u32 m0, m0, s97\n"
    "v_exp_f32 v34, v34\n"
    "v_mfma_f32_16x16x32_bf16 v[168:171], a[136:139], v[4:7], 0\n"
    "v_fma_f32 v36, v36, s52, v85\n"
    "v_fma_f32 v38, v38, s52, v85\n"
    "v_mfma_f32_16x16x32_bf16 v[172:175], a[140:143], v[4:7], 0\n"
    "v_exp_f32_e64 v36, v36\n"
    "v_mfma_f32_16x16x32_bf16 v[232:235], a[136:139], v[8:11], 0\n"
    "s_nop 0\n"
    "ds_read_b64_tr_b16 a[136:137], v91 offset:18432\n"
    "ds_read_b64_tr_b16 a[138:139], v91 offset:26624\n"
    "v_mfma_f32_16x16x32_bf16 v[236:239], a[140:143], v[8:11], 0\n"
    "s_waitcnt lgkmcnt(10)\n"
    "ds_read_b64_tr_b16 a[140:141], v91 offset:18440\n"
    "ds_read_b64_tr_b16 a[142:143], v91 offset:26632\n"
    "v_mfma_f32_16x16x32_bf16 v[112:115], a[144:147], v[0:3], 0\n"
    "buffer_load_dwordx4 v92, s[40:43], s56 offen offset:128 lds\n"
    "v_mfma_f32_16x16x32_bf16 v[116:119], a[148:151], v[0:3], 0\n"
    "s_add_u32 m0, m0, s97\n"
    "v_exp_f32 v38, v38\n"
    "v_mfma_f32_16x16x32_bf16 v[176:179], a[144:147], v[4:7], 0\n"
    "v_fma_f32 v40, v40, s52, v86\n"
    "v_fma_f32 v42, v42, s52, v86\n"
    "v_mfma_f32_16x16x32_bf16 v[180:183], a[148:151], v[4:7], 0\n"
    "v_exp_f32_e64 v40, v40\n"
    "v_mfma_f32_16x16x32_bf16 v[240:243], a[144:147], v[8:11], 0\n"
    "s_nop 0\n"
    "ds_read_b64_tr_b16 a[144:145], v90 offset:19456\n"
    "ds_read_b64_tr_b16 a[146:147], v90 offset:27648\n"
    "v_mfma_f32_16x16x32_bf16 v[244:247], a[148:151], v[8:11], 0\n"
    "s_waitcnt lgkmcnt(10)\n"
    "ds_read_b64_tr_b16 a[148:149], v90 offset:19464\n"
    "ds_read_b64_tr_b16 a[150:151], v90 offset:27656\n"
    "v_mfma_f32_16x16x32_bf16 v[120:123], a[152:155], v[0:3], 0\n"
    "buffer_load_dwordx4 v92, s[40:43], s56 offen offset:256 lds\n"
    "v_mfma_f32_16x16x32_bf16 v[124:127], a[156:159], v[0:3], 0\n"
    "s_add_u32 m0, m0, 896\n"
    "v_fma_f32 v33, v33, s52, v84\n"
    "v_fma_f32 v35, v35, s52, v84\n"
    "v_fma_f32 v37, v37, s52, v85\n"
    "v_fma_f32 v39, v39, s52, v85\n"
    "v_fma_f32 v41, v41, s52, v86\n"
    "v_fma_f32 v43, v43, s52, v86\n"
    "v_mfma_f32_16x16x32_bf16 v[184:187], a[152:155], v[4:7], 0\n"
    "v_exp_f32 v33, v33\n"
    "v_add_f32 v80, v80, v32\n"
    "v_exp_f32 v37, v37\n"
    "v_add_f32 v81, v81, v36\n"
    "v_exp_f32 v41, v41\n"
    "v_add_f32 v82, v82, v40\n"
    "v_exp_f32 v42, v42\n"
    "v_mfma_f32_16x16x32_bf16 v[188:191], a[156:159], v[4:7], 0\n"
    "v_add_f32 v80, v80, v33\n"
    "v_exp_f32 v35, v35\n"
    "v_add_f32 v81, v81, v37\n"
    "v_exp_f32 v39, v39\n"
    "v_add_f32 v82, v82, v41\n"
    "v_exp_f32 v43, v43\n"
    "v_mfma_f32_16x16x32_bf16 v[248:251], a[152:155], v[8:11], 0\n"
    "ds_read_b64_tr_b16 a[152:153], v91 offset:19456\n"
    "ds_read_b64_tr_b16 a[154:155], v91 offset:27648\n"
    "v_add_f32 v80, v80, v34\n"
    "v_add_f32 v81, v81, v38\n"
    "v_add_f32_e64 v82, v82, v42\n"
    "v_mfma_f32_16x16x32_bf16 v[252:255], a[156:159], v[8:11], 0\n"
    "s_waitcnt lgkmcnt(10)\n"
    "ds_read_b64_tr_b16 a[156:157], v91 offset:19464\n"
    "ds_read_b64_tr_b16 a[158:159], v91 offset:27656\n"
    "v_add_f32 v80, v80, v35\n"
    "v_add_f32 v81, v81, v39\n"
    "v_add_f32_e64 v82, v82, v43\n"
    "v_mfma_f32_16x16x32_bf16 v[128:131], a[128:131], v[0:3], 0\n"
    "buffer_load_dwordx4 v92, s[40:43], s56 offen offset:384 lds\n"
    "v_mfma_f32_16x16x32_bf16 v[132:135], a[132:135], v[0:3], 0\n"
    "s_add_u32 s55, s55, s96\n"
    "v_add_u32 v88, s62, v88\n"
    "v_add_u32_e64 v89, s62, v89\n"
    "v_mfma_f32_16x16x32_bf16 v[192:195], a[128:131], v[4:7], 0\n"
    "s_add_u32 s56, s56, 0x8000\n"
    "v_fma_f32 v48, v48, s52, v84\n"
    "v_fma_f32 v50, v50, s52, v84\n"
    "v_mfma_f32_16x16x32_bf16 v[196:199], a[132:135], v[4:7], 0\n"
    "s_mov_b32 m0, s59\n"
    "v_exp_f32_e64 v48, v48\n"
    "v_mfma_f32_16x16x32_bf16 a[224:227], a[128:131], v[8:11], 0\n"
    "s_waitcnt vmcnt(8) lgkmcnt(0)\n"
    "buffer_load_dwordx4 v92, s[44:47], s57 offen offset:0 nt lds\n"
    "v_mfma_f32_16x16x32_bf16 a[228:231], a[132:135], v[8:11], 0\n"
    "s_barrier\n"
    "v_mfma_f32_16x16x32_bf16 v[136:139], a[136:139], v[0:3], 0\n"
    "s_add_u32 m0, m0, s97\n"
    "ds_read_b128 a[128:131], v88 offset:0\n"
    "ds_read_b128 a[132:135], v89 offset:0\n"
    "v_mfma_f32_16x16x32_bf16 v[140:143], a[140:143], v[0:3], 0\n"
    "buffer_load_dwordx4 v92, s[44:47], s57 offen offset:128 nt lds\n"
    "v_mfma_f32_16x16x32_bf16 v[200:203], a[136:139], v[4:7], 0\n"
    "s_add_u32 s57, s57, s58\n"
    "v_exp_f32 v50, v50\n"
    "v_mfma_f32_16x16x32_bf16 v[204:207], a[140:143], v[4:7], 0\n"
    "s_xor_b32 s58, s58, 0xe00\n"
    "v_fma_f32 v52, v52, s52, v85\n"
    "v_fma_f32 v54, v54, s52, v85\n"
    "v_mfma_f32_16x16x32_bf16 a[232:235], a[136:139], v[8:11], 0\n"
    "s_add_u32 s59, s59, 0x800\n"
    "ds_read_b128 a[136:139], v88 offset:1024\n"
    "v_fma_f32 v56, v56, s52, v86\n"
    "v_mfma_f32_16x16x32_bf16 a[236:239], a[140:143], v[8:11], 0\n"
    "ds_read_b128 a[140:143], v89 offset:1024\n"
    "v_fma_f32 v58, v58, s52, v86\n"
    "v_mfma_f32_16x16x32_bf16 v[144:147], a[144:147], v[0:3], 0\n"
    "v_exp_f32 v52, v52\n"
    "v_mfma_f32_16x16x32_bf16 v[148:151], a[148:151], v[0:3], 0\n"
    "v_exp_f32 v56, v56\n"
    "v_mfma_f32_16x16x32_bf16 v[208:211], a[144:147], v[4:7], 0\n"
    "v_exp_f32 v54, v54\n"
    "v_mfma_f32_16x16x32_bf16 v[212:215], a[148:151], v[4:7], 0\n"
    "v_exp_f32 v58, v58\n"
    "v_mfma_f32_16x16x32_bf16 a[240:243], a[144:147], v[8:11], 0\n"
    "ds_read_b128 a[144:147], v88 offset:2048\n"
    "v_fma_f32 v49, v49, s52, v84\n"
    "v_mfma_f32_16x16x32_bf16 a[244:247], a[148:151], v[8:11], 0\n"
    "ds_read_b128 a[148:151], v89 offset:2048\n"
    "v_fma_f32 v51, v51, s52, v84\n"
    "v_mfma_f32_16x16x32_bf16 v[152:155], a[152:155], v[0:3], 0\n"
    "v_fma_f32 v53, v53, s52, v85\n"
    "v_fma_f32 v55, v55, s52, v85\n"
    "v_fma_f32 v57, v57, s52, v86\n"
    "v_fma_f32 v59, v59, s52, v86\n"
    "v_mfma_f32_16x16x32_bf16 v[156:159], a[156:159], v[0:3], 0\n"
    "v_exp_f32 v49, v49\n"
    "v_add_f32 v80, v80, v48\n"
    "v_exp_f32 v53, v53\n"
    "v_add_f32 v81, v81, v52\n"
    "v_exp_f32 v57, v57\n"
    "v_add_f32 v82, v82, v56\n"
    "v_mfma_f32_16x16x32_bf16 v[216:219], a[152:155], v[4:7], 0\n"
    "v_add_f32 v80, v80, v49\n"
    "v_exp_f32 v51, v51\n"
    "v_add_f32 v81, v81, v53\n"
    "v_exp_f32 v55, v55\n"
    "v_add_f32 v82, v82, v57\n"
    "v_exp_f32 v59, v59\n"
    "v_mfma_f32_16x16x32_bf16 v[220:223], a[156:159], v[4:7], 0\n"
    "v_add_f32 v80, v80, v50\n"
    "v_add_f32 v81, v81, v54\n"
    "v_add_f32_e64 v82, v82, v58\n"
    "v_mfma_f32_16x16x32_bf16 a[248:251], a[152:155], v[8:11], 0\n"
    "ds_read_b128 a[152:155], v88 offset:3072\n"
    "v_add_f32_e64 v80, v80, v51\n"
    "v_add_f32 v81, v81, v55\n"
    "v_add_f32_e64 v82, v82, v59\n"
    "v_mfma_f32_16x16x32_bf16 a[252:255], a[156:159], v[8:11], 0\n"
    "s_waitcnt lgkmcnt(5)\n"
    "ds_read_b128 a[156:159], v89 offset:3072\n"
    "v_add_u32 v93, 4096, v93\n"
    "LQ3_%=:\n"
    "v_mfma_f32_16x16x32_bf16 v[0:3], a[128:131], a[0:3], 0\n"
    "s_mov_b32 m0, s61\n"
    "v_cvt_pk_bf16_f32 v64, v32, v33\n"
    "s_nop 0\n"
    "v_cvt_pk_bf16_f32 v65, v34, v35\n"
    "v_mfma_f32_16x16x32_bf16 v[0:3], a[132:135], a[4:7], v[0:3]\n"
    "buffer_load_dwordx4 v92, s[36:39], s53 offen offset:0 lds\n"
    "v_mfma_f32_16x16x32_bf16 v[4:7], a[128:131], a[32:35], 0\n"
    "s_add_u32 m0, m0, 896\n"
    "v_cvt_pk_bf16_f32 v66, v48, v49\n"
    "v_cvt_pk_bf16_f32 v67, v50, v51\n"
    "v_mfma_f32_16x16x32_bf16 v[4:7], a[132:135], a[36:39], v[4:7]\n"
    "s_sub_u32 s64, s60, s61\n"
    "v_cvt_pk_bf16_f32 v68, v36, v37\n"
    "v_cvt_pk_bf16_f32 v69, v38, v39\n"
    "v_mfma_f32_16x16x32_bf16 v[8:11], a[128:131], a[64:67], 0\n"
    "s_mov_b32 s71, s61\n"
    "ds_read_b128 a[128:131], v88 offset:8192\n"
    "v_add_u32 v90, s64, v90\n"
    "v_mfma_f32_16x16x32_bf16 v[8:11], a[132:135], a[68:71], v[8:11]\n"
    "s_waitcnt lgkmcnt(5)\n"
    "ds_read_b128 a[132:135], v89 offset:8192\n"
    "v_add_u32_e64 v91, s64, v91\n"
    "v_mfma_f32_16x16x32_bf16 v[0:3], a[136:139], a[8:11], v[0:3]\n"
    "buffer_load_dwordx4 v92, s[36:39], s53 offen offset:128 lds\n"
    "v_mfma_f32_16x16x32_bf16 v[0:3], a[140:143], a[12:15], v[0:3]\n"
    "s_add_u32 m0, m0, 896\n"
    "v_cvt_pk_bf16_f32 v70, v52, v53\n"
    "v_cvt_pk_bf16_f32 v71, v54, v55\n"
    "v_mfma_f32_16x16x32_bf16 v[4:7], a[136:139], a[40:43], v[4:7]\n"
    "s_mov_b32 s61, s60\n"
    "v_cvt_pk_bf16_f32 v72, v40, v41\n"
    "v_cvt_pk_bf16_f32 v73, v42, v43\n"
    "v_mfma_f32_16x16x32_bf16 v[4:7], a[140:143], a[44:47], v[4:7]\n"
    "s_nop 0\n"
    "buffer_load_dwordx4 v92, s[36:39], s53 offen offset:256 lds\n"
    "v_mfma_f32_16x16x32_bf16 v[8:11], a[136:139], a[72:75], v[8:11]\n"
    "s_add_u32 m0, m0, 896\n"
    "ds_read_b128 a[136:139], v88 offset:9216\n"
    "v_cvt_pk_bf16_f32 v74, v56, v57\n"
    "v_mfma_f32_16x16x32_bf16 v[8:11], a[140:143], a[76:79], v[8:11]\n"
    "s_waitcnt lgkmcnt(5)\n"
    "ds_read_b128 a[140:143], v89 offset:9216\n"
    "s_nop 0\n"
    "v_cvt_pk_bf16_f32 v75, v58, v59\n"
    "v_mfma_f32_16x16x32_bf16 v[0:3], a[144:147], a[16:19], v[0:3]\n"
    "buffer_load_dwordx4 v92, s[36:39], s53 offen offset:384 lds\n"
    "v_mfma_f32_16x16x32_bf16 v[0:3], a[148:151], a[20:23], v[0:3]\n"
    "s_add_u32 m0, m0, 1408\n"
    "v_mfma_f32_16x16x32_bf16 v[4:7], a[144:147], a[48:51], v[4:7]\n"
    "s_mov_b32 s60, s63\n"
    "v_mfma_f32_16x16x32_bf16 v[4:7], a[148:151], a[52:55], v[4:7]\n"
    "s_nop 0\n"
    "buffer_load_dwordx4 v92, s[36:39], s54 offen offset:0 lds\n"
    "v_mfma_f32_16x16x32_bf16 v[8:11], a[144:147], a[80:83], v[8:11]\n"
    "s_add_u32 m0, m0, 896\n"
    "ds_read_b128 a[144:147], v88 offset:10240\n"
    "v_mfma_f32_16x16x32_bf16 v[8:11], a[148:151], a[84:87], v[8:11]\n"
    "s_waitcnt lgkmcnt(5)\n"
    "ds_read_b128 a[148:151], v89 offset:10240\n"
    "v_mfma_f32_16x16x32_bf16 v[0:3], a[152:155], a[24:27], v[0:3]\n"
    "s_nop 0\n"
    "buffer_load_dwordx4 v92, s[36:39], s54 offen offset:128 lds\n"
    "v_mfma_f32_16x16x32_bf16 v[0:3], a[156:159], a[28:31], v[0:3]\n"
    "s_add_u32 m0, m0, 896\n"
    "v_mfma_f32_16x16x32_bf16 v[4:7], a[152:155], a[56:59], v[4:7]\n"
    "s_mov_b32 s63, s71\n"
    "v_mfma_f32_16x16x32_bf16 v[4:7], a[156:159], a[60:63], v[4:7]\n"
    "s_nop 0\n"
    "buffer_load_dwordx4 v92, s[36:39], s54 offen offset:256 lds\n"
    "v_mfma_f32_16x16x32_bf16 v[8:11], a[152:155], a[88:91], v[8:11]\n"
    "s_add_u32 m0, m0, s97\n"
    "ds_read_b128 a[152:155], v88 offset:11264\n"
    "v_fma_f32 v0, v0, s52, v84\n"
    "v_mfma_f32_16x16x32_bf16 v[8:11], a[156:159], a[92:95], v[8:11]\n"
    "s_waitcnt lgkmcnt(5)\n"
    "ds_read_b128 a[156:159], v89 offset:11264\n"
    "v_fma_f32 v2, v2, s52, v84\n"
    "v_mfma_f32_16x16x32_bf16 v[16:19], a[128:131], a[0:3], 0\n"
    "buffer_load_dwordx4 v92, s[36:39], s54 offen offset:384 lds\n"
    "v_mfma_f32_16x16x32_bf16 v[16:19], a[132:135], a[4:7], v[16:19]\n"
    "s_add_u32 s53, s53, s96\n"
    "v_exp_f32 v0, v0\n"
    "v_mfma_f32_16x16x32_bf16 v[20:23], a[128:131], a[32:35], 0\n"
    "s_add_u32 s54, s54, 0x8000\n"
    "v_exp_f32 v2, v2\n"
    "v_mfma_f32_16x16x32_bf16 v[20:23], a[132:135], a[36:39], v[20:23]\n"
    "s_sub_u32 s62, s63, s60\n"
    "v_fma_f32 v4, v4, s52, v85\n"
    "v_fma_f32 v6, v6, s52, v85\n"
    "v_mfma_f32_16x16x32_bf16 v[24:27], a[128:131], a[64:67], 0\n"
    "s_mov_b32 m0, s59\n"
    "ds_read_b128 a[128:131], v88 offset:16384\n"
    "v_fma_f32 v8, v8, s52, v86\n"
    "v_mfma_f32_16x16x32_bf16 v[24:27], a[132:135], a[68:71], v[24:27]\n"
    "s_waitcnt lgkmcnt(5)\n"
    "ds_read_b128 a[132:135], v89 offset:16384\n"
    "v_fma_f32 v10, v10, s52, v86\n"
    "v_mfma_f32_16x16x32_bf16 v[16:19], a[136:139], a[8:11], v[16:19]\n"
    "buffer_load_dwordx4 v92, s[44:47], s57 offen offset:0 nt lds\n"
    "v_mfma_f32_16x16x32_bf16 v[16:19], a[140:143], a[12:15], v[16:19]\n"
    "s_add_u32 m0, m0, s97\n"
    "v_exp_f32 v4, v4\n"
    "v_mfma_f32_16x16x32_bf16 v[20:23], a[136:139], a[40:43], v[20:23]\n"
    "v_exp_f32_e64 v8, v8\n"
    "v_mfma_f32_16x16x32_bf16 v[20:23], a[140:143], a[44:47], v[20:23]\n"
    "buffer_load_dwordx4 v92, s[44:47], s57 offen offset:128 nt lds\n"
    "v_mfma_f32_16x16x32_bf16 v[24:27], a[136:139], a[72:75], v[24:27]\n"
    "s_add_u32 s57, s57, s58\n"
    "ds_read_b128 a[136:139], v88 offset:17408\n"
    "v_fma_f32 v1, v1, s52, v84\n"
    "v_mfma_f32_16x16x32_bf16 v[24:27], a[140:143], a[76:79], v[24:27]\n"
    "s_waitcnt lgkmcnt(5)\n"
    "ds_read_b128 a[140:143], v89 offset:17408\n"
    "v_fma_f32 v3, v3, s52, v84\n"
    "v_mfma_f32_16x16x32_bf16 v[16:19], a[144:147], a[16:19], v[16:19]\n"
    "s_xor_b32 s58, s58, 0xe00\n"
    "v_exp_f32_e64 v1, v1\n"
    "v_mfma_f32_16x16x32_bf16 v[16:19], a[148:151], a[20:23], v[16:19]\n"
    "s_add_u32 s59, s59, 0x800\n"
    "v_exp_f32_e64 v6, v6\n"
    "v_mfma_f32_16x16x32_bf16 v[20:23], a[144:147], a[48:51], v[20:23]\n"
    "v_exp_f32_e64 v10, v10\n"
    "v_mfma_f32_16x16x32_bf16 v[20:23], a[148:151], a[52:55], v[20:23]\n"
    "v_exp_f32_e64 v3, v3\n"
    "v_mfma_f32_16x16x32_bf16 v[24:27], a[144:147], a[80:83], v[24:27]\n"
    "ds_read_b128 a[144:147], v88 offset:18432\n"
    "v_fma_f32 v5, v5, s52, v85\n"
    "v_mfma_f32_16x16x32_bf16 v[24:27], a[148:151], a[84:87], v[24:27]\n"
    "s_waitcnt lgkmcnt(5)\n"
    "ds_read_b128 a[148:151], v89 offset:18432\n"
    "v_fma_f32 v7, v7, s52, v85\n"
    "v_mfma_f32_16x16x32_bf16 v[16:19], a[152:155], a[24:27], v[16:19]\n"
    "v_exp_f32 v5, v5\n"
    "v_mfma_f32_16x16x32_bf16 v[16:19], a[156:159], a[28:31], v[16:19]\n"
    "v_exp_f32_e64 v7, v7\n"
    "v_mfma_f32_16x16x32_bf16 v[20:23], a[152:155], a[56:59], v[20:23]\n"
    "v_fma_f32 v9, v9, s52, v86\n"
    "v_fma_f32 v11, v11, s52, v86\n"
    "v_mfma_f32_16x16x32_bf16 v[20:23], a[156:159], a[60:63], v[20:23]\n"
    "v_exp_f32_e64 v9, v9\n"
    "v_mfma_f32_16x16x32_bf16 v[24:27], a[152:155], a[88:91], v[24:27]\n"
    "ds_read_b128 a[152:155], v88 offset:19456\n"
    "v_add_f32 v80, v80, v0\n"
    "v_mfma_f32_16x16x32_bf16 v[24:27], a[156:159], a[92:95], v[24:27]\n"
    "s_waitcnt lgkmcnt(5)\n"
    "ds_read_b128 a[156:159], v89 offset:19456\n"
    "v_add_f32 v81, v81, v4\n"
    "v_mfma_f32_16x16x32_bf16 v[32:35], a[128:131], a[0:3], 0\n"
    "v_exp_f32 v11, v11\n"
    "v_mfma_f32_16x16x32_bf16 v[32:35], a[132:135], a[4:7], v[32:35]\n"
    "v_add_f32 v82, v82, v8\n"
    "v_add_f32 v80, v80, v1\n"
    "v_mfma_f32_16x16x32_bf16 v[36:39], a[128:131], a[32:35], 0\n"
    "v_add_f32_e64 v81, v81, v5\n"
    "v_add_f32_e64 v82, v82, v9\n"
    "v_mfma_f32_16x16x32_bf16 v[36:39], a[132:135], a[36:39], v[36:39]\n"
    "v_add_f32_e64 v80, v80, v2\n"
    "v_add_f32_e64 v81, v81, v6\n"
    "v_mfma_f32_16x16x32_bf16 v[40:43], a[128:131], a[64:67], 0\n"
    "ds_read_b128 a[128:131], v88 offset:24576\n"
    "v_add_f32 v82, v82, v10\n"
    "v_mfma_f32_16x16x32_bf16 v[40:43], a[132:135], a[68:71], v[40:43]\n"
    "s_waitcnt lgkmcnt(5)\n"
    "ds_read_b128 a[132:135], v89 offset:24576\n"
    "v_add_f32_e64 v80, v80, v3\n"
    "v_mfma_f32_16x16x32_bf16 v[32:35], a[136:139], a[8:11], v[32:35]\n"
    "v_add_f32_e64 v81, v81, v7\n"
    "v_add_f32_e64 v82, v82, v11\n"
    "v_mfma_f32_16x16x32_bf16 v[32:35], a[140:143], a[12:15], v[32:35]\n"
    "v_cvt_pk_bf16_f32 v0, v0, v1\n"
    "v_cvt_pk_bf16_f32 v1, v2, v3\n"
    "v_mfma_f32_16x16x32_bf16 v[36:39], a[136:139], a[40:43], v[36:39]\n"
    "v_cvt_pk_bf16_f32 v4, v4, v5\n"
    "v_cvt_pk_bf16_f32 v5, v6, v7\n"
    "v_mfma_f32_16x16x32_bf16 v[36:39], a[140:143], a[44:47], v[36:39]\n"
    "v_cvt_pk_bf16_f32 v8, v8, v9\n"
    "v_cvt_pk_bf16_f32 v9, v10, v11\n"
    "v_mfma_f32_16x16x32_bf16 v[40:43], a[136:139], a[72:75], v[40:43]\n"
    "ds_read_b128 a[136:139], v88 offset:25600\n"
    "v_fma_f32 v16, v16, s52, v84\n"
    "v_mfma_f32_16x16x32_bf16 v[40:43], a[140:143], a[76:79], v[40:43]\n"
    "s_waitcnt lgkmcnt(5)\n"
    "ds_read_b128 a[140:143], v89 offset:25600\n"
    "v_fma_f32 v18, v18, s52, v84\n"
    "v_mfma_f32_16x16x32_bf16 v[32:35], a[144:147], a[16:19], v[32:35]\n"
    "v_exp_f32 v16, v16\n"
    "v_mfma_f32_16x16x32_bf16 v[32:35], a[148:151], a[20:23], v[32:35]\n"
    "v_exp_f32_e64 v18, v18\n"
    "v_mfma_f32_16x16x32_bf16 v[36:39], a[144:147], a[48:51], v[36:39]\n"
    "v_fma_f32 v20, v20, s52, v85\n"
    "v_fma_f32 v22, v22, s52, v85\n"
    "v_mfma_f32_16x16x32_bf16 v[36:39], a[148:151], a[52:55], v[36:39]\n"
    "v_exp_f32_e64 v20, v20\n"
    "v_mfma_f32_16x16x32_bf16 v[40:43], a[144:147], a[80:83], v[40:43]\n"
    "s_nop 0\n"
    "ds_read_b128 a[144:147], v88 offset:26624\n"
    "v_fma_f32 v24, v24, s52, v86\n"
    "v_mfma_f32_16x16x32_bf16 v[40:43], a[148:151], a[84:87], v[40:43]\n"
    "s_waitcnt lgkmcnt(5)\n"
    "ds_read_b128 a[148:151], v89 offset:26624\n"
    "v_fma_f32 v26, v26, s52, v86\n"
    "v_mfma_f32_16x16x32_bf16 v[32:35], a[152:155], a[24:27], v[32:35]\n"
    "v_exp_f32_e64 v24, v24\n"
    "v_mfma_f32_16x16x32_bf16 v[32:35], a[156:159], a[28:31], v[32:35]\n"
    "v_exp_f32_e64 v22, v22\n"
    "v_mfma_f32_16x16x32_bf16 v[36:39], a[152:155], a[56:59], v[36:39]\n"
    "v_exp_f32_e64 v26, v26\n"
    "v_mfma_f32_16x16x32_bf16 v[36:39], a[156:159], a[60:63], v[36:39]\n"
    "v_fma_f32 v17, v17, s52, v84\n"
    "v_fma_f32 v19, v19, s52, v84\n"
    "v_mfma_f32_16x16x32_bf16 v[40:43], a[152:155], a[88:91], v[40:43]\n"
    "s_nop 0\n"
    "ds_read_b128 a[152:155], v88 offset:27648\n"
    "v_fma_f32 v21, v21, s52, v85\n"
    "v_mfma_f32_16x16x32_bf16 v[40:43], a[156:159], a[92:95], v[40:43]\n"
    "s_waitcnt lgkmcnt(5)\n"
    "ds_read_b128 a[156:159], v89 offset:27648\n"
    "v_fma_f32 v23, v23, s52, v85\n"
    "v_mfma_f32_16x16x32_bf16 v[48:51], a[128:131], a[0:3], 0\n"
    "v_exp_f32_e64 v17, v17\n"
    "v_mfma_f32_16x16x32_bf16 v[48:51], a[132:135], a[4:7], v[48:51]\n"
    "v_exp_f32 v21, v21\n"
    "v_mfma_f32_16x16x32_bf16 v[52:55], a[128:131], a[32:35], 0\n"
    "v_exp_f32 v19, v19\n"
    "v_mfma_f32_16x16x32_bf16 v[52:55], a[132:135], a[36:39], v[52:55]\n"
    "v_exp_f32 v23, v23\n"
    "v_mfma_f32_16x16x32_bf16 v[56:59], a[128:131], a[64:67], 0\n"
    "s_waitcnt vmcnt(12) lgkmcnt(0)\n"
    "v_fma_f32 v25, v25, s52, v86\n"
    "v_fma_f32 v27, v27, s52, v86\n"
    "v_mfma_f32_16x16x32_bf16 v[56:59], a[132:135], a[68:71], v[56:59]\n"
    "s_barrier\n"
    "v_mfma_f32_16x16x32_bf16 v[48:51], a[136:139], a[8:11], v[48:51]\n"
    "s_nop 0\n"
    "ds_read_b64_tr_b16 a[128:129], v90 offset:0\n"
    "ds_read_b64_tr_b16 a[130:131], v90 offset:8192\n"
    "v_mfma_f32_16x16x32_bf16 v[48:51], a[140:143], a[12:15], v[48:51]\n"
    "ds_read_b64_tr_b16 a[132:133], v90 offset:16384\n"
    "ds_read_b64_tr_b16 a[134:135], v90 offset:24576\n"
    "v_mfma_f32_16x16x32_bf16 v[52:55], a[136:139], a[40:43], v[52:55]\n"
    "v_exp_f32 v25, v25\n"
    "v_mfma_f32_16x16x32_bf16 v[52:55], a[140:143], a[44:47], v[52:55]\n"
    "v_exp_f32 v27, v27\n"
    "v_mfma_f32_16x16x32_bf16 v[56:59], a[136:139], a[72:75], v[56:59]\n"
    "ds_read_b64_tr_b16 a[136:137], v90 offset:8\n"
    "ds_read_b64_tr_b16 a[138:139], v90 offset:8200\n"
    "v_mfma_f32_16x16x32_bf16 v[56:59], a[140:143], a[76:79], v[56:59]\n"
    "ds_read_b64_tr_b16 a[140:141], v90 offset:16392\n"
    "ds_read_b64_tr_b16 a[142:143], v90 offset:24584\n"
    "v_mfma_f32_16x16x32_bf16 v[48:51], a[144:147], a[16:19], v[48:51]\n"
    "v_add_f32 v80, v80, v16\n"
    "v_add_f32 v81, v81, v20\n"
    "v_mfma_f32_16x16x32_bf16 v[48:51], a[148:151], a[20:23], v[48:51]\n"
    "v_add_f32 v82, v82, v24\n"
    "v_add_f32 v80, v80, v17\n"
    "v_mfma_f32_16x16x32_bf16 v[52:55], a[144:147], a[48:51], v[52:55]\n"
    "v_add_f32 v81, v81, v21\n"
    "v_add_f32 v82, v82, v25\n"
    "v_mfma_f32_16x16x32_bf16 v[52:55], a[148:151], a[52:55], v[52:55]\n"
    "v_add_f32 v80, v80, v18\n"
    "v_add_f32 v81, v81, v22\n"
    "v_mfma_f32_16x16x32_bf16 v[56:59], a[144:147], a[80:83], v[56:59]\n"
    "ds_read_b64_tr_b16 a[144:145], v91 offset:0\n"
    "ds_read_b64_tr_b16 a[146:147], v91 offset:8192\n"
    "v_mfma_f32_16x16x32_bf16 v[56:59], a[148:151], a[84:87], v[56:59]\n"
    "ds_read_b64_tr_b16 a[148:149], v91 offset:16384\n"
    "ds_read_b64_tr_b16 a[150:151], v91 offset:24576\n"
    "v_mfma_f32_16x16x32_bf16 v[48:51], a[152:155], a[24:27], v[48:51]\n"
    "v_add_f32 v82, v82, v26\n"
    "v_add_f32 v80, v80, v19\n"
    "v_mfma_f32_16x16x32_bf16 v[48:51], a[156:159], a[28:31], v[48:51]\n"
    "v_add_f32 v81, v81, v23\n"
    "v_add_f32 v82, v82, v27\n"
    "v_mfma_f32_16x16x32_bf16 v[52:55], a[152:155], a[56:59], v[52:55]\n"
    "v_cvt_pk_bf16_f32 v2, v16, v17\n"
    "v_cvt_pk_bf16_f32 v3, v18, v19\n"
    "v_mfma_f32_16x16x32_bf16 v[52:55], a[156:159], a[60:63], v[52:55]\n"
    "v_cvt_pk_bf16_f32 v6, v20, v21\n"
    "v_cvt_pk_bf16_f32 v7, v22, v23\n"
    "v_mfma_f32_16x16x32_bf16 v[56:59], a[152:155], a[88:91], v[56:59]\n"
    "ds_read_b64_tr_b16 a[152:153], v91 offset:8\n"
    "ds_read_b64_tr_b16 a[154:155], v91 offset:8200\n"
    "v_mfma_f32_16x16x32_bf16 v[56:59], a[156:159], a[92:95], v[56:59]\n"
    "s_waitcnt lgkmcnt(10)\n"
    "ds_read_b64_tr_b16 a[156:157], v91 offset:16392\n"
    "ds_read_b64_tr_b16 a[158:159], v91 offset:24584\n"
    "v_mfma_f32_16x16x32_bf16 v[96:99], a[128:131], v[64:67], v[96:99]\n"
    "v_cvt_pk_bf16_f32 v10, v24, v25\n"
    "v_cvt_pk_bf16_f32 v11, v26, v27\n"
    "v_mfma_f32_16x16x32_bf16 v[96:99], a[132:135], v[0:3], v[96:99]\n"
    "s_mov_b32 m0, s60\n"
    "v_fma_f32 v32, v32, s52, v84\n"
    "v_fma_f32 v34, v34, s52, v84\n"
    "v_mfma_f32_16x16x32_bf16 v[160:163], a[128:131], v[68:71], v[160:163]\n"
    "buffer_load_dwordx4 v92, s[40:43], s55 offen offset:0 lds\n"
    "v_mfma_f32_16x16x32_bf16 v[160:163], a[132:135], v[4:7], v[160:163]\n"
    "s_add_u32 m0, m0, 896\n"
    "v_exp_f32_e64 v32, v32\n"
    "v_mfma_f32_16x16x32_bf16 v[224:227], a[128:131], v[72:75], v[224:227]\n"
    "s_nop 0\n"
    "ds_read_b64_tr_b16 a[128:129], v90 offset:1024\n"
    "ds_read_b64_tr_b16 a[130:131], v90 offset:9216\n"
    "v_mfma_f32_16x16x32_bf16 v[224:227], a[132:135], v[8:11], v[224:227]\n"
    "s_waitcnt lgkmcnt(10)\n"
    "ds_read_b64_tr_b16 a[132:133], v90 offset:17408\n"
    "ds_read_b64_tr_b16 a[134:135], v90 offset:25600\n"
    "v_mfma_f32_16x16x32_bf16 v[100:103], a[136:139], v[64:67], v[100:103]\n"
    "buffer_load_dwordx4 v92, s[40:43], s55 offen offset:128 lds\n"
    "v_mfma_f32_16x16x32_bf16 v[100:103], a[140:143], v[0:3], v[100:103]\n"
    "s_add_u32 m0, m0, s97\n"
    "v_exp_f32 v34, v34\n"
    "v_mfma_f32_16x16x32_bf16 v[164:167], a[136:139], v[68:71], v[164:167]\n"
    "v_fma_f32 v36, v36, s52, v85\n"
    "v_fma_f32 v38, v38, s52, v85\n"
    "v_mfma_f32_16x16x32_bf16 v[164:167], a[140:143], v[4:7], v[164:167]\n"
    "v_exp_f32_e64 v36, v36\n"
    "v_mfma_f32_16x16x32_bf16 v[228:231], a[136:139], v[72:75], v[228:231]\n"
    "s_nop 0\n"
    "ds_read_b64_tr_b16 a[136:137], v90 offset:1032\n"
    "ds_read_b64_tr_b16 a[138:139], v90 offset:9224\n"
    "v_mfma_f32_16x16x32_bf16 v[228:231], a[140:143], v[8:11], v[228:231]\n"
    "s_waitcnt lgkmcnt(10)\n"
    "ds_read_b64_tr_b16 a[140:141], v90 offset:17416\n"
    "ds_read_b64_tr_b16 a[142:143], v90 offset:25608\n"
    "v_mfma_f32_16x16x32_bf16 v[104:107], a[144:147], v[64:67], v[104:107]\n"
    "buffer_load_dwordx4 v92, s[40:43], s55 offen offset:256 lds\n"
    "v_mfma_f32_16x16x32_bf16 v[104:107], a[148:151], v[0:3], v[104:107]\n"
    "s_add_u32 m0, m0, s97\n"
    "v_exp_f32 v38, v38\n"
    "v_mfma_f32_16x16x32_bf16 v[168:171], a[144:147], v[68:71], v[168:171]\n"
    "v_fma_f32 v40, v40, s52, v86\n"
    "v_fma_f32 v42, v42, s52, v86\n"
    "v_mfma_f32_16x16x32_bf16 v[168:171], a[148:151], v[4:7], v[168:171]\n"
    "v_exp_f32_e64 v40, v40\n"
    "v_mfma_f32_16x16x32_bf16 v[232:235], a[144:147], v[72:75], v[232:235]\n"
    "s_nop 0\n"
    "ds_read_b64_tr_b16 a[144:145], v91 offset:1024\n"
    "ds_read_b64_tr_b16 a[146:147], v91 offset:9216\n"
    "v_mfma_f32_16x16x32_bf16 v[232:235], a[148:151], v[8:11], v[232:235]\n"
    "s_waitcnt lgkmcnt(10)\n"
    "ds_read_b64_tr_b16 a[148:149], v91 offset:17408\n"
    "ds_read_b64_tr_b16 a[150:151], v91 offset:25600\n"
    "v_mfma_f32_16x16x32_bf16 v[108:111], a[152:155], v[64:67], v[108:111]\n"
    "buffer_load_dwordx4 v92, s[40:43], s55 offen offset:384 lds\n"
    "v_mfma_f32_16x16x32_bf16 v[108:111], a[156:159], v[0:3], v[108:111]\n"
    "s_add_u32 m0, m0, 1408\n"
    "v_exp_f32_e64 v42, v42\n"
    "v_mfma_f32_16x16x32_bf16 v[172:175], a[152:155], v[68:71], v[172:175]\n"
    "v_fma_f32 v33, v33, s52, v84\n"
    "v_fma_f32 v35, v35, s52, v84\n"
    "v_mfma_f32_16x16x32_bf16 v[172:175], a[156:159], v[4:7], v[172:175]\n"
    "v_exp_f32_e64 v33, v33\n"
    "v_mfma_f32_16x16x32_bf16 v[236:239], a[152:155], v[72:75], v[236:239]\n"
    "ds_read_b64_tr_b16 a[152:153], v91 offset:1032\n"
    "ds_read_b64_tr_b16 a[154:155], v91 offset:9224\n"
    "v_mfma_f32_16x16x32_bf16 v[236:239], a[156:159], v[8:11], v[236:239]\n"
    "s_waitcnt lgkmcnt(10)\n"
    "ds_read_b64_tr_b16 a[156:157], v91 offset:17416\n"
    "ds_read_b64_tr_b16 a[158:159], v91 offset:25608\n"
    "v_mfma_f32_16x16x32_bf16 v[112:115], a[128:131], v[64:67], v[112:115]\n"
    "s_nop 0\n"
    "buffer_load_dwordx4 v92, s[40:43], s56 offen offset:0 lds\n"
    "v_mfma_f32_16x16x32_bf16 v[112:115], a[132:135], v[0:3], v[112:115]\n"
    "s_add_u32 m0, m0, s97\n"
    "v_exp_f32 v35, v35\n"
    "v_mfma_f32_16x16x32_bf16 v[176:179], a[128:131], v[68:71], v[176:179]\n"
    "v_fma_f32 v37, v37, s52, v85\n"
    "v_fma_f32 v39, v39, s52, v85\n"
    "v_mfma_f32_16x16x32_bf16 v[176:179], a[132:135], v[4:7], v[176:179]\n"
    "v_exp_f32_e64 v37, v37\n"
    "v_mfma_f32_16x16x32_bf16 v[240:243], a[128:131], v[72:75], v[240:243]\n"
    "ds_read_b64_tr_b16 a[128:129], v90 offset:2048\n"
    "ds_read_b64_tr_b16 a[130:131], v90 offset:10240\n"
    "v_mfma_f32_16x16x32_bf16 v[240:243], a[132:135], v[8:11], v[240:243]\n"
    "s_waitcnt lgkmcnt(10)\n"
    "ds_read_b64_tr_b16 a[132:133], v90 offset:18432\n"
    "ds_read_b64_tr_b16 a[134:135], v90 offset:26624\n"
    "v_mfma_f32_16x16x32_bf16 v[116:119], a[136:139], v[64:67], v[116:119]\n"
    "s_nop 0\n"
    "buffer_load_dwordx4 v92, s[40:43], s56 offen offset:128 lds\n"
    "v_mfma_f32_16x16x32_bf16 v[116:119], a[140:143], v[0:3], v[116:119]\n"
    "s_add_u32 m0, m0, s97\n"
    "v_exp_f32 v39, v39\n"
    "v_mfma_f32_16x16x32_bf16 v[180:183], a[136:139], v[68:71], v[180:183]\n"
    "v_fma_f32 v41, v41, s52, v86\n"
    "v_fma_f32 v43, v43, s52, v86\n"
    "v_mfma_f32_16x16x32_bf16 v[180:183], a[140:143], v[4:7], v[180:183]\n"
    "v_exp_f32_e64 v41, v41\n"
    "v_mfma_f32_16x16x32_bf16 v[244:247], a[136:139], v[72:75], v[244:247]\n"
    "ds_read_b64_tr_b16 a[136:137], v90 offset:2056\n"
    "ds_read_b64_tr_b16 a[138:139], v90 offset:10248\n"
    "v_mfma_f32_16x16x32_bf16 v[244:247], a[140:143], v[8:11], v[244:247]\n"
    "s_waitcnt lgkmcnt(10)\n"
    "ds_read_b64_tr_b16 a[140:141], v90 offset:18440\n"
    "ds_read_b64_tr_b16 a[142:143], v90 offset:26632\n"
    "v_mfma_f32_16x16x32_bf16 v[120:123], a[144:147], v[64:67], v[120:123]\n"
    "s_nop 0\n"
    "buffer_load_dwordx4 v92, s[40:43], s56 offen offset:256 lds\n"
    "v_mfma_f32_16x16x32_bf16 v[120:123], a[148:151], v[0:3], v[120:123]\n"
    "s_add_u32 m0, m0, s97\n"
    "v_exp_f32 v43, v43\n"
    "v_mfma_f32_16x16x32_bf16 v[184:187], a[144:147], v[68:71], v[184:187]\n"
    "v_add_u32 v88, s62, v88\n"
    "v_add_u32 v89, s62, v89\n"
    "v_mfma_f32_16x16x32_bf16 v[184:187], a[148:151], v[4:7], v[184:187]\n"
    "v_add_f32_e64 v80, v80, v32\n"
    "v_add_f32_e64 v81, v81, v36\n"
    "v_mfma_f32_16x16x32_bf16 v[248:251], a[144:147], v[72:75], v[248:251]\n"
    "ds_read_b64_tr_b16 a[144:145], v91 offset:2048\n"
    "ds_read_b64_tr_b16 a[146:147], v91 offset:10240\n"
    "v_mfma_f32_16x16x32_bf16 v[248:251], a[148:151], v[8:11], v[248:251]\n"
    "s_waitcnt lgkmcnt(10)\n"
    "ds_read_b64_tr_b16 a[148:149], v91 offset:18432\n"
    "ds_read_b64_tr_b16 a[150:151], v91 offset:26624\n"
    "v_mfma_f32_16x16x32_bf16 v[124:127], a[152:155], v[64:67], v[124:127]\n"
    "s_nop 0\n"
    "buffer_load_dwordx4 v92, s[40:43], s56 offen offset:384 lds\n"
    "v_mfma_f32_16x16x32_bf16 v[124:127], a[156:159], v[0:3], v[124:127]\n"
    "s_add_u32 s55, s55, 0x8000\n"
    "ds_read_b128 v[16:19], v93 offset:0\n"
    "ds_read_b128 v[20:23], v93 offset:1024\n"
    "v_mfma_f32_16x16x32_bf16 v[188:191], a[152:155], v[68:71], v[188:191]\n"
    "s_add_u32 s56, s56, 0x8000\n"
    "v_add_f32_e64 v82, v82, v40\n"
    "v_add_f32_e64 v80, v80, v33\n"
    "v_mfma_f32_16x16x32_bf16 v[188:191], a[156:159], v[4:7], v[188:191]\n"
    "v_add_f32_e64 v81, v81, v37\n"
    "v_add_f32_e64 v82, v82, v41\n"
    "v_mfma_f32_16x16x32_bf16 v[252:255], a[152:155], v[72:75], v[252:255]\n"
    "ds_read_b64_tr_b16 a[152:153], v91 offset:2056\n"
    "ds_read_b64_tr_b16 a[154:155], v91 offset:10248\n"
    "v_mfma_f32_16x16x32_bf16 v[252:255], a[156:159], v[8:11], v[252:255]\n"
    "s_waitcnt lgkmcnt(12)\n"
    "ds_read_b64_tr_b16 a[156:157], v91 offset:18440\n"
    "ds_read_b64_tr_b16 a[158:159], v91 offset:26632\n"
    "v_mfma_f32_16x16x32_bf16 v[128:131], a[128:131], v[64:67], v[128:131]\n"
    "v_add_f32 v80, v80, v34\n"
    "v_add_f32 v81, v81, v38\n"
    "v_mfma_f32_16x16x32_bf16 v[128:131], a[132:135], v[0:3], v[128:131]\n"
    "v_add_f32 v82, v82, v42\n"
    "v_add_f32 v80, v80, v35\n"
    "v_mfma_f32_16x16x32_bf16 v[192:195], a[128:131], v[68:71], v[192:195]\n"
    "v_add_f32 v81, v81, v39\n"
    "v_add_f32_e64 v82, v82, v43\n"
    "v_mfma_f32_16x16x32_bf16 v[192:195], a[132:135], v[4:7], v[192:195]\n"
    "v_fma_f32 v48, v48, s52, v84\n"
    "v_fma_f32 v50, v50, s52, v84\n"
    "v_mfma_f32_16x16x32_bf16 a[224:227], a[128:131], v[72:75], a[224:227]\n"
    "ds_read_b64_tr_b16 a[128:129], v90 offset:3072\n"
    "ds_read_b64_tr_b16 a[130:131], v90 offset:11264\n"
    "v_mfma_f32_16x16x32_bf16 a[224:227], a[132:135], v[8:11], a[224:227]\n"
    "s_waitcnt lgkmcnt(12)\n"
    "ds_read_b64_tr_b16 a[132:133], v90 offset:19456\n"
    "ds_read_b64_tr_b16 a[134:135], v90 offset:27648\n"
    "v_mfma_f32_16x16x32_bf16 v[132:135], a[136:139], v[64:67], v[132:135]\n"
    "s_waitcnt lgkmcnt(8)\n"
    "buffer_store_dwordx4 v[16:19], v92, s[48:51], s98 offen offset:0 nt\n"
    "v_mfma_f32_16x16x32_bf16 v[132:135], a[140:143], v[0:3], v[132:135]\n"
    "v_exp_f32 v48, v48\n"
    "v_mfma_f32_16x16x32_bf16 v[196:199], a[136:139], v[68:71], v[196:199]\n"
    "v_exp_f32 v50, v50\n"
    "v_mfma_f32_16x16x32_bf16 v[196:199], a[140:143], v[4:7], v[196:199]\n"
    "v_fma_f32 v52, v52, s52, v85\n"
    "v_fma_f32 v54, v54, s52, v85\n"
    "v_mfma_f32_16x16x32_bf16 a[228:231], a[136:139], v[72:75], a[228:231]\n"
    "s_nop 0\n"
    "ds_read_b64_tr_b16 a[136:137], v90 offset:3080\n"
    "ds_read_b64_tr_b16 a[138:139], v90 offset:11272\n"
    "v_mfma_f32_16x16x32_bf16 a[228:231], a[140:143], v[8:11], a[228:231]\n"
    "s_waitcnt lgkmcnt(12)\n"
    "ds_read_b64_tr_b16 a[140:141], v90 offset:19464\n"
    "ds_read_b64_tr_b16 a[142:143], v90 offset:27656\n"
    "v_mfma_f32_16x16x32_bf16 v[136:139], a[144:147], v[64:67], v[136:139]\n"
    "buffer_store_dwordx4 v[20:23], v92, s[48:51], s98 offen offset:128 nt\n"
    "v_mfma_f32_16x16x32_bf16 v[136:139], a[148:151], v[0:3], v[136:139]\n"
    "s_add_u32 s98, s98, s99\n"
    "v_exp_f32 v52, v52\n"
    "v_mfma_f32_16x16x32_bf16 v[200:203], a[144:147], v[68:71], v[200:203]\n"
    "s_xor_b32 s99, s99, 0xe00\n"
    "v_exp_f32_e64 v54, v54\n"
    "v_mfma_f32_16x16x32_bf16 v[200:203], a[148:151], v[4:7], v[200:203]\n"
    "v_fma_f32 v56, v56, s52, v86\n"
    "v_fma_f32 v58, v58, s52, v86\n"
    "v_mfma_f32_16x16x32_bf16 a[232:235], a[144:147], v[72:75], a[232:235]\n"
    "ds_read_b64_tr_b16 a[144:145], v91 offset:3072\n"
    "ds_read_b64_tr_b16 a[146:147], v91 offset:11264\n"
    "v_mfma_f32_16x16x32_bf16 a[232:235], a[148:151], v[8:11], a[232:235]\n"
    "s_waitcnt lgkmcnt(10)\n"
    "ds_read_b64_tr_b16 a[148:149], v91 offset:19456\n"
    "ds_read_b64_tr_b16 a[150:151], v91 offset:27648\n"
    "v_mfma_f32_16x16x32_bf16 v[140:143], a[152:155], v[64:67], v[140:143]\n"
    "v_exp_f32 v56, v56\n"
    "v_mfma_f32_16x16x32_bf16 v[140:143], a[156:159], v[0:3], v[140:143]\n"
    "v_exp_f32_e64 v58, v58\n"
    "v_mfma_f32_16x16x32_bf16 v[204:207], a[152:155], v[68:71], v[204:207]\n"
    "v_fma_f32 v49, v49, s52, v84\n"
    "v_fma_f32 v51, v51, s52, v84\n"
    "v_mfma_f32_16x16x32_bf16 v[204:207], a[156:159], v[4:7], v[204:207]\n"
    "v_exp_f32_e64 v49, v49\n"
    "v_mfma_f32_16x16x32_bf16 a[236:239], a[152:155], v[72:75], a[236:239]\n"
    "s_nop 0\n"
    "ds_read_b64_tr_b16 a[152:153], v91 offset:3080\n"
    "ds_read_b64_tr_b16 a[154:155], v91 offset:11272\n"
    "v_mfma_f32_16x16x32_bf16 a[236:239], a[156:159], v[8:11], a[236:239]\n"
    "s_waitcnt lgkmcnt(10)\n"
    "ds_read_b64_tr_b16 a[156:157], v91 offset:19464\n"
    "ds_read_b64_tr_b16 a[158:159], v91 offset:27656\n"
    "v_mfma_f32_16x16x32_bf16 v[144:147], a[128:131], v[64:67], v[144:147]\n"
    "v_exp_f32_e64 v51, v51\n"
    "v_mfma_f32_16x16x32_bf16 v[144:147], a[132:135], v[0:3], v[144:147]\n"
    "v_fma_f32 v53, v53, s52, v85\n"
    "v_fma_f32 v55, v55, s52, v85\n"
    "v_mfma_f32_16x16x32_bf16 v[208:211], a[128:131], v[68:71], v[208:211]\n"
    "v_exp_f32_e64 v53, v53\n"
    "v_mfma_f32_16x16x32_bf16 v[208:211], a[132:135], v[4:7], v[208:211]\n"
    "v_exp_f32_e64 v55, v55\n"
    "v_mfma_f32_16x16x32_bf16 a[240:243], a[128:131], v[72:75], a[240:243]\n"
    "s_waitcnt vmcnt(12) lgkmcnt(0)\n"
    "v_fma_f32 v57, v57, s52, v86\n"
    "v_fma_f32 v59, v59, s52, v86\n"
    "v_mfma_f32_16x16x32_bf16 a[240:243], a[132:135], v[8:11], a[240:243]\n"
    "s_barrier\n"
    "v_mfma_f32_16x16x32_bf16 v[148:151], a[136:139], v[64:67], v[148:151]\n"
    "ds_read_b128 a[128:131], v88 offset:0\n"
    "ds_read_b128 a[132:135], v89 offset:0\n"
    "v_mfma_f32_16x16x32_bf16 v[148:151], a[140:143], v[0:3], v[148:151]\n"
    "v_exp_f32 v57, v57\n"
    "v_mfma_f32_16x16x32_bf16 v[212:215], a[136:139], v[68:71], v[212:215]\n"
    "v_exp_f32 v59, v59\n"
    "v_mfma_f32_16x16x32_bf16 v[212:215], a[140:143], v[4:7], v[212:215]\n"
    "v_add_f32 v80, v80, v48\n"
    "v_add_f32 v81, v81, v52\n"
    "v_mfma_f32_16x16x32_bf16 a[244:247], a[136:139], v[72:75], a[244:247]\n"
    "ds_read_b128 a[136:139], v88 offset:1024\n"
    "v_add_f32_e64 v82, v82, v56\n"
    "v_mfma_f32_16x16x32_bf16 a[244:247], a[140:143], v[8:11], a[244:247]\n"
    "ds_read_b128 a[140:143], v89 offset:1024\n"
    "v_add_f32 v80, v80, v49\n"
    "v_mfma_f32_16x16x32_bf16 v[152:155], a[144:147], v[64:67], v[152:155]\n"
    "v_add_f32 v81, v81, v53\n"
    "v_add_f32_e64 v82, v82, v57\n"
    "v_mfma_f32_16x16x32_bf16 v[152:155], a[148:151], v[0:3], v[152:155]\n"
    "v_add_u32 v93, 2048, v93\n"
    "v_add_f32 v80, v80, v50\n"
    "v_mfma_f32_16x16x32_bf16 v[216:219], a[144:147], v[68:71], v[216:219]\n"
    "v_add_f32 v81, v81, v54\n"
    "v_add_f32 v82, v82, v58\n"
    "v_mfma_f32_16x16x32_bf16 v[216:219], a[148:151], v[4:7], v[216:219]\n"
    "v_add_f32 v80, v80, v51\n"
    "v_add_f32_e64 v81, v81, v55\n"
    "v_mfma_f32_16x16x32_bf16 a[248:251], a[144:147], v[72:75], a[248:251]\n"
    "ds_read_b128 a[144:147], v88 offset:2048\n"
    "v_add_f32_e64 v82, v82, v59\n"
    "v_mfma_f32_16x16x32_bf16 a[248:251], a[148:151], v[8:11], a[248:251]\n"
    "ds_read_b128 a[148:151], v89 offset:2048\n"
    "v_mfma_f32_16x16x32_bf16 v[156:159], a[152:155], v[64:67], v[156:159]\n"
    "v_mfma_f32_16x16x32_bf16 v[156:159], a[156:159], v[0:3], v[156:159]\n"
    "v_mfma_f32_16x16x32_bf16 v[220:223], a[152:155], v[68:71], v[220:223]\n"
    "v_mfma_f32_16x16x32_bf16 v[220:223], a[156:159], v[4:7], v[220:223]\n"
    "v_mfma_f32_16x16x32_bf16 a[252:255], a[152:155], v[72:75], a[252:255]\n"
    "s_sub_u32 s65, s65, 1\n"
    "ds_read_b128 a[152:155], v88 offset:3072\n"
    "v_mfma_f32_16x16x32_bf16 a[252:255], a[156:159], v[8:11], a[252:255]\n"
    "s_waitcnt lgkmcnt(5)\n"
    "ds_read_b128 a[156:159], v89 offset:3072\n"
    "s_cbranch_scc0 LQ3_%=\n"
    "v_mfma_f32_16x16x32_bf16 v[0:3], a[128:131], a[0:3], 0\n"
    "s_mov_b32 m0, s61\n"
    "v_cvt_pk_bf16_f32 v64, v32, v33\n"
    "v_cvt_pk_bf16_f32 v65, v34, v35\n"
    "v_mfma_f32_16x16x32_bf16 v[0:3], a[132:135], a[4:7], v[0:3]\n"
    "buffer_load_dwordx4 v92, s[40:43], s55 offen offset:0 lds\n"
    "v_mfma_f32_16x16x32_bf16 v[4:7], a[128:131], a[32:35], 0\n"
    "s_add_u32 m0, m0, 896\n"
    "v_cvt_pk_bf16_f32 v66, v48, v49\n"
    "v_cvt_pk_bf16_f32 v67, v50, v51\n"
    "v_mfma_f32_16x16x32_bf16 v[4:7], a[132:135], a[36:39], v[4:7]\n"
    "s_sub_u32 s64, s60, s61\n"
    "v_cvt_pk_bf16_f32 v68, v36, v37\n"
    "v_cvt_pk_bf16_f32 v69, v38, v39\n"
    "v_mfma_f32_16x16x32_bf16 v[8:11], a[128:131], a[64:67], 0\n"
    "s_mov_b32 s71, s61\n"
    "ds_read_b128 a[128:131], v88 offset:8192\n"
    "v_add_u32 v90, s64, v90\n"
    "v_mfma_f32_16x16x32_bf16 v[8:11], a[132:135], a[68:71], v[8:11]\n"
    "s_waitcnt lgkmcnt(5)\n"
    "ds_read_b128 a[132:135], v89 offset:8192\n"
    "v_add_u32_e64 v91, s64, v91\n"
    "v_mfma_f32_16x16x32_bf16 v[0:3], a[136:139], a[8:11], v[0:3]\n"
    "buffer_load_dwordx4 v92, s[40:43], s55 offen offset:128 lds\n"
    "v_mfma_f32_16x16x32_bf16 v[0:3], a[140:143], a[12:15], v[0:3]\n"
    "s_add_u32 m0, m0, s97\n"
    "v_cvt_pk_bf16_f32 v70, v52, v53\n"
    "v_cvt_pk_bf16_f32 v71, v54, v55\n"
    "v_mfma_f32_16x16x32_bf16 v[4:7], a[136:139], a[40:43], v[4:7]\n"
    "s_mov_b32 s61, s60\n"
    "v_cvt_pk_bf16_f32 v72, v40, v41\n"
    "v_cvt_pk_bf16_f32 v73, v42, v43\n"
    "v_mfma_f32_16x16x32_bf16 v[4:7], a[140:143], a[44:47], v[4:7]\n"
    "buffer_load_dwordx4 v92, s[40:43], s55 offen offset:256 lds\n"
    "v_mfma_f32_16x16x32_bf16 v[8:11], a[136:139], a[72:75], v[8:11]\n"
    "s_add_u32 m0, m0, 896\n"
    "ds_read_b128 a[136:139], v88 offset:9216\n"
    "v_cvt_pk_bf16_f32 v74, v56, v57\n"
    "v_mfma_f32_16x16x32_bf16 v[8:11], a[140:143], a[76:79], v[8:11]\n"
    "s_waitcnt lgkmcnt(5)\n"
    "ds_read_b128 a[140:143], v89 offset:9216\n"
    "v_cvt_pk_bf16_f32 v75, v58, v59\n"
    "s_nop 0\n"
    "v_mfma_f32_16x16x32_bf16 v[0:3], a[144:147], a[16:19], v[0:3]\n"
    "buffer_load_dwordx4 v92, s[40:43], s55 offen offset:384 lds\n"
    "v_mfma_f32_16x16x32_bf16 v[0:3], a[148:151], a[20:23], v[0:3]\n"
    "s_add_u32 m0, m0, 1408\n"
    "v_add_u32 v93, 0xffffc000, v93\n"
    "v_mfma_f32_16x16x32_bf16 v[4:7], a[144:147], a[48:51], v[4:7]\n"
    "s_mov_b32 s60, s63\n"
    "v_mfma_f32_16x16x32_bf16 v[4:7], a[148:151], a[52:55], v[4:7]\n"
    "s_nop 0\n"
    "buffer_load_dwordx4 v92, s[40:43], s56 offen offset:0 lds\n"
    "v_mfma_f32_16x16x32_bf16 v[8:11], a[144:147], a[80:83], v[8:11]\n"
    "s_add_u32 m0, m0, 896\n"
    "ds_read_b128 a[144:147], v88 offset:10240\n"
    "v_mfma_f32_16x16x32_bf16 v[8:11], a[148:151], a[84:87], v[8:11]\n"
    "s_waitcnt lgkmcnt(5)\n"
    "ds_read_b128 a[148:151], v89 offset:10240\n"
    "v_mfma_f32_16x16x32_bf16 v[0:3], a[152:155], a[24:27], v[0:3]\n"
    "s_nop 0\n"
    "buffer_load_dwordx4 v92, s[40:43], s56 offen offset:128 lds\n"
    "v_mfma_f32_16x16x32_bf16 v[0:3], a[156:159], a[28:31], v[0:3]\n"
    "s_add_u32 m0, m0, 896\n"
    "v_mfma_f32_16x16x32_bf16 v[4:7], a[152:155], a[56:59], v[4:7]\n"
    "s_mov_b32 s63, s71\n"
    "v_mfma_f32_16x16x32_bf16 v[4:7], a[156:159], a[60:63], v[4:7]\n"
    "s_nop 0\n"
    "buffer_load_dwordx4 v92, s[40:43], s56 offen offset:256 lds\n"
    "v_mfma_f32_16x16x32_bf16 v[8:11], a[152:155], a[88:91], v[8:11]\n"
    "s_add_u32 m0, m0, s97\n"
    "ds_read_b128 a[152:155], v88 offset:11264\n"
    "v_fma_f32 v0, v0, s52, v84\n"
    "v_mfma_f32_16x16x32_bf16 v[8:11], a[156:159], a[92:95], v[8:11]\n"
    "s_waitcnt lgkmcnt(5)\n"
    "ds_read_b128 a[156:159], v89 offset:11264\n"
    "v_fma_f32 v2, v2, s52, v84\n"
    "v_mfma_f32_16x16x32_bf16 v[16:19], a[128:131], a[0:3], 0\n"
    "buffer_load_dwordx4 v92, s[40:43], s56 offen offset:384 lds\n"
    "v_mfma_f32_16x16x32_bf16 v[16:19], a[132:135], a[4:7], v[16:19]\n"
    "s_add_u32 s55, s55, s96\n"
    "v_exp_f32 v0, v0\n"
    "v_mfma_f32_16x16x32_bf16 v[20:23], a[128:131], a[32:35], 0\n"
    "s_add_u32 s56, s56, 0x8000\n"
    "v_exp_f32 v2, v2\n"
    "v_mfma_f32_16x16x32_bf16 v[20:23], a[132:135], a[36:39], v[20:23]\n"
    "s_sub_u32 s62, s63, s60\n"
    "v_fma_f32 v4, v4, s52, v85\n"
    "v_fma_f32 v6, v6, s52, v85\n"
    "v_mfma_f32_16x16x32_bf16 v[24:27], a[128:131], a[64:67], 0\n"
    "s_mov_b32 m0, s59\n"
    "ds_read_b128 a[128:131], v88 offset:16384\n"
    "v_fma_f32 v8, v8, s52, v86\n"
    "v_mfma_f32_16x16x32_bf16 v[24:27], a[132:135], a[68:71], v[24:27]\n"
    "s_waitcnt lgkmcnt(5)\n"
    "ds_read_b128 a[132:135], v89 offset:16384\n"
    "v_fma_f32 v10, v10, s52, v86\n"
    "v_mfma_f32_16x16x32_bf16 v[16:19], a[136:139], a[8:11], v[16:19]\n"
    "buffer_load_dwordx4 v92, s[44:47], s57 offen offset:0 nt lds\n"
    "v_mfma_f32_16x16x32_bf16 v[16:19], a[140:143], a[12:15], v[16:19]\n"
    "s_add_u32 m0, m0, 896\n"
    "v_exp_f32_e64 v4, v4\n"
    "v_mfma_f32_16x16x32_bf16 v[20:23], a[136:139], a[40:43], v[20:23]\n"
    "s_mov_b32 s50, 434176\n"
    "v_exp_f32_e64 v8, v8\n"
    "v_mfma_f32_16x16x32_bf16 v[20:23], a[140:143], a[44:47], v[20:23]\n"
    "buffer_load_dwordx4 v92, s[44:47], s57 offen offset:128 nt lds\n"
    "v_mfma_f32_16x16x32_bf16 v[24:27], a[136:139], a[72:75], v[24:27]\n"
    "s_add_u32 s57, s57, s58\n"
    "ds_read_b128 a[136:139], v88 offset:17408\n"
    "v_fma_f32 v1, v1, s52, v84\n"
    "v_mfma_f32_16x16x32_bf16 v[24:27], a[140:143], a[76:79], v[24:27]\n"
    "s_waitcnt lgkmcnt(5)\n"
    "ds_read_b128 a[140:143], v89 offset:17408\n"
    "v_fma_f32 v3, v3, s52, v84\n"
    "v_mfma_f32_16x16x32_bf16 v[16:19], a[144:147], a[16:19], v[16:19]\n"
    "s_xor_b32 s58, s58, 0xe00\n"
    "v_exp_f32_e64 v1, v1\n"
    "v_mfma_f32_16x16x32_bf16 v[16:19], a[148:151], a[20:23], v[16:19]\n"
    "s_add_u32 s59, s59, 0x800\n"
    "v_exp_f32_e64 v6, v6\n"
    "v_mfma_f32_16x16x32_bf16 v[20:23], a[144:147], a[48:51], v[20:23]\n"
    "s_add_u32 s48, s48, 434176\n"
    "v_exp_f32 v10, v10\n"
    "v_mfma_f32_16x16x32_bf16 v[20:23], a[148:151], a[52:55], v[20:23]\n"
    "s_addc_u32 s49, s49, 0\n"
    "v_exp_f32_e64 v3, v3\n"
    "v_mfma_f32_16x16x32_bf16 v[24:27], a[144:147], a[80:83], v[24:27]\n"
    "s_add_u32 s66, s66, 1\n"
    "ds_read_b128 a[144:147], v88 offset:18432\n"
    "v_fma_f32 v5, v5, s52, v85\n"
    "v_mfma_f32_16x16x32_bf16 v[24:27], a[148:151], a[84:87], v[24:27]\n"
    "s_waitcnt lgkmcnt(5)\n"
    "ds_read_b128 a[148:151], v89 offset:18432\n"
    "v_fma_f32 v7, v7, s52, v85\n"
    "v_mfma_f32_16x16x32_bf16 v[16:19], a[152:155], a[24:27], v[16:19]\n"
    "v_exp_f32_e64 v5, v5\n"
    "v_mfma_f32_16x16x32_bf16 v[16:19], a[156:159], a[28:31], v[16:19]\n"
    "v_exp_f32_e64 v7, v7\n"
    "v_mfma_f32_16x16x32_bf16 v[20:23], a[152:155], a[56:59], v[20:23]\n"
    "v_fma_f32 v9, v9, s52, v86\n"
    "v_fma_f32 v11, v11, s52, v86\n"
    "v_mfma_f32_16x16x32_bf16 v[20:23], a[156:159], a[60:63], v[20:23]\n"
    "v_exp_f32_e64 v9, v9\n"
    "v_mfma_f32_16x16x32_bf16 v[24:27], a[152:155], a[88:91], v[24:27]\n"
    "ds_read_b128 a[152:155], v88 offset:19456\n"
    "v_add_f32 v80, v80, v0\n"
    "v_mfma_f32_16x16x32_bf16 v[24:27], a[156:159], a[92:95], v[24:27]\n"
    "s_waitcnt lgkmcnt(5)\n"
    "ds_read_b128 a[156:159], v89 offset:19456\n"
    "v_add_f32 v81, v81, v4\n"
    "v_mfma_f32_16x16x32_bf16 v[32:35], a[128:131], a[0:3], 0\n"
    "v_exp_f32 v11, v11\n"
    "v_mfma_f32_16x16x32_bf16 v[32:35], a[132:135], a[4:7], v[32:35]\n"
    "v_add_f32 v82, v82, v8\n"
    "v_add_f32 v80, v80, v1\n"
    "v_mfma_f32_16x16x32_bf16 v[36:39], a[128:131], a[32:35], 0\n"
    "v_add_f32_e64 v81, v81, v5\n"
    "v_add_f32_e64 v82, v82, v9\n"
    "v_mfma_f32_16x16x32_bf16 v[36:39], a[132:135], a[36:39], v[36:39]\n"
    "v_add_f32_e64 v80, v80, v2\n"
    "v_add_f32_e64 v81, v81, v6\n"
    "v_mfma_f32_16x16x32_bf16 v[40:43], a[128:131], a[64:67], 0\n"
    "ds_read_b128 a[128:131], v88 offset:24576\n"
    "v_add_f32 v82, v82, v10\n"
    "v_mfma_f32_16x16x32_bf16 v[40:43], a[132:135], a[68:71], v[40:43]\n"
    "s_waitcnt lgkmcnt(5)\n"
    "ds_read_b128 a[132:135], v89 offset:24576\n"
    "v_add_f32_e64 v80, v80, v3\n"
    "v_mfma_f32_16x16x32_bf16 v[32:35], a[136:139], a[8:11], v[32:35]\n"
    "v_add_f32_e64 v81, v81, v7\n"
    "v_add_f32_e64 v82, v82, v11\n"
    "v_mfma_f32_16x16x32_bf16 v[32:35], a[140:143], a[12:15], v[32:35]\n"
    "v_cvt_pk_bf16_f32 v0, v0, v1\n"
    "v_cvt_pk_bf16_f32 v1, v2, v3\n"
    "v_mfma_f32_16x16x32_bf16 v[36:39], a[136:139], a[40:43], v[36:39]\n"
    "v_cvt_pk_bf16_f32 v4, v4, v5\n"
    "v_cvt_pk_bf16_f32 v5, v6, v7\n"
    "v_mfma_f32_16x16x32_bf16 v[36:39], a[140:143], a[44:47], v[36:39]\n"
    "v_cvt_pk_bf16_f32 v8, v8, v9\n"
    "v_cvt_pk_bf16_f32 v9, v10, v11\n"
    "v_mfma_f32_16x16x32_bf16 v[40:43], a[136:139], a[72:75], v[40:43]\n"
    "ds_read_b128 a[136:139], v88 offset:25600\n"
    "v_fma_f32 v16, v16, s52, v84\n"
    "v_mfma_f32_16x16x32_bf16 v[40:43], a[140:143], a[76:79], v[40:43]\n"
    "s_waitcnt lgkmcnt(5)\n"
    "ds_read_b128 a[140:143], v89 offset:25600\n"
    "v_fma_f32 v18, v18, s52, v84\n"
    "v_mfma_f32_16x16x32_bf16 v[32:35], a[144:147], a[16:19], v[32:35]\n"
    "v_exp_f32 v16, v16\n"
    "v_mfma_f32_16x16x32_bf16 v[32:35], a[148:151], a[20:23], v[32:35]\n"
    "v_exp_f32_e64 v18, v18\n"
    "v_mfma_f32_16x16x32_bf16 v[36:39], a[144:147], a[48:51], v[36:39]\n"
    "v_fma_f32 v20, v20, s52, v85\n"
    "v_fma_f32 v22, v22, s52, v85\n"
    "v_mfma_f32_16x16x32_bf16 v[36:39], a[148:151], a[52:55], v[36:39]\n"
    "v_exp_f32_e64 v20, v20\n"
    "v_mfma_f32_16x16x32_bf16 v[40:43], a[144:147], a[80:83], v[40:43]\n"
    "s_nop 0\n"
    "ds_read_b128 a[144:147], v88 offset:26624\n"
    "v_fma_f32 v24, v24, s52, v86\n"
    "v_mfma_f32_16x16x32_bf16 v[40:43], a[148:151], a[84:87], v[40:43]\n"
    "s_waitcnt lgkmcnt(5)\n"
    "ds_read_b128 a[148:151], v89 offset:26624\n"
    "v_fma_f32 v26, v26, s52, v86\n"
    "v_mfma_f32_16x16x32_bf16 v[32:35], a[152:155], a[24:27], v[32:35]\n"
    "v_exp_f32_e64 v24, v24\n"
    "v_mfma_f32_16x16x32_bf16 v[32:35], a[156:159], a[28:31], v[32:35]\n"
    "v_exp_f32_e64 v22, v22\n"
    "v_mfma_f32_16x16x32_bf16 v[36:39], a[152:155], a[56:59], v[36:39]\n"
    "v_exp_f32_e64 v26, v26\n"
    "v_mfma_f32_16x16x32_bf16 v[36:39], a[156:159], a[60:63], v[36:39]\n"
    "v_fma_f32 v17, v17, s52, v84\n"
    "v_fma_f32 v19, v19, s52, v84\n"
    "v_mfma_f32_16x16x32_bf16 v[40:43], a[152:155], a[88:91], v[40:43]\n"
    "s_nop 0\n"
    "ds_read_b128 a[152:155], v88 offset:27648\n"
    "v_fma_f32 v21, v21, s52, v85\n"
    "v_mfma_f32_16x16x32_bf16 v[40:43], a[156:159], a[92:95], v[40:43]\n"
    "s_waitcnt lgkmcnt(5)\n"
    "ds_read_b128 a[156:159], v89 offset:27648\n"
    "v_fma_f32 v23, v23, s52, v85\n"
    "v_mfma_f32_16x16x32_bf16 v[48:51], a[128:131], a[0:3], 0\n"
    "v_exp_f32_e64 v17, v17\n"
    "v_mfma_f32_16x16x32_bf16 v[48:51], a[132:135], a[4:7], v[48:51]\n"
    "v_exp_f32 v21, v21\n"
    "v_mfma_f32_16x16x32_bf16 v[52:55], a[128:131], a[32:35], 0\n"
    "v_exp_f32 v19, v19\n"
    "v_mfma_f32_16x16x32_bf16 v[52:55], a[132:135], a[36:39], v[52:55]\n"
    "v_exp_f32 v23, v23\n"
    "v_mfma_f32_16x16x32_bf16 v[56:59], a[128:131], a[64:67], 0\n"
    "s_waitcnt vmcnt(12) lgkmcnt(0)\n"
    "v_fma_f32 v25, v25, s52, v86\n"
    "v_fma_f32 v27, v27, s52, v86\n"
    "v_mfma_f32_16x16x32_bf16 v[56:59], a[132:135], a[68:71], v[56:59]\n"
    "s_barrier\n"
    "v_mfma_f32_16x16x32_bf16 v[48:51], a[136:139], a[8:11], v[48:51]\n"
    "s_mov_b32 m0, s60\n"
    "ds_read_b64_tr_b16 a[128:129], v90 offset:0\n"
    "ds_read_b64_tr_b16 a[130:131], v90 offset:8192\n"
    "v_mfma_f32_16x16x32_bf16 v[48:51], a[140:143], a[12:15], v[48:51]\n"
    "s_sub_u32 s64, s63, s61\n"
    "ds_read_b64_tr_b16 a[132:133], v90 offset:16384\n"
    "s_nop 0\n"
    "ds_read_b64_tr_b16 a[134:135], v90 offset:24576\n"
    "v_mfma_f32_16x16x32_bf16 v[52:55], a[136:139], a[40:43], v[52:55]\n"
    "buffer_load_dwordx4 v92, s[36:39], s53 offen offset:0 lds\n"
    "v_mfma_f32_16x16x32_bf16 v[52:55], a[140:143], a[44:47], v[52:55]\n"
    "s_add_u32 m0, m0, s97\n"
    "v_exp_f32_e64 v25, v25\n"
    "v_mfma_f32_16x16x32_bf16 v[56:59], a[136:139], a[72:75], v[56:59]\n"
    "s_lshl_b32 s72, s69, 12\n"
    "ds_read_b64_tr_b16 a[136:137], v90 offset:8\n"
    "ds_read_b64_tr_b16 a[138:139], v90 offset:8200\n"
    "v_mfma_f32_16x16x32_bf16 v[56:59], a[140:143], a[76:79], v[56:59]\n"
    "ds_read_b64_tr_b16 a[140:141], v90 offset:16392\n"
    "ds_read_b64_tr_b16 a[142:143], v90 offset:24584\n"
    "v_mfma_f32_16x16x32_bf16 v[48:51], a[144:147], a[16:19], v[48:51]\n"
    "buffer_load_dwordx4 v92, s[36:39], s53 offen offset:128 lds\n"
    "v_mfma_f32_16x16x32_bf16 v[48:51], a[148:151], a[20:23], v[48:51]\n"
    "s_add_u32 m0, m0, s97\n"
    "v_exp_f32 v27, v27\n"
    "v_mfma_f32_16x16x32_bf16 v[52:55], a[144:147], a[48:51], v[52:55]\n"
    "s_sub_u32 s72, s63, s72\n"
    "v_add_f32 v80, v80, v16\n"
    "v_add_f32 v81, v81, v20\n"
    "v_mfma_f32_16x16x32_bf16 v[52:55], a[148:151], a[52:55], v[52:55]\n"
    "s_mov_b32 s71, s61\n"
    "v_add_f32 v82, v82, v24\n"
    "v_add_f32_e64 v80, v80, v17\n"
    "v_mfma_f32_16x16x32_bf16 v[56:59], a[144:147], a[80:83], v[56:59]\n"
    "s_mov_b32 s61, s63\n"
    "ds_read_b64_tr_b16 a[144:145], v91 offset:0\n"
    "ds_read_b64_tr_b16 a[146:147], v91 offset:8192\n"
    "v_mfma_f32_16x16x32_bf16 v[56:59], a[148:151], a[84:87], v[56:59]\n"
    "ds_read_b64_tr_b16 a[148:149], v91 offset:16384\n"
    "ds_read_b64_tr_b16 a[150:151], v91 offset:24576\n"
    "v_mfma_f32_16x16x32_bf16 v[48:51], a[152:155], a[24:27], v[48:51]\n"
    "buffer_load_dwordx4 v92, s[36:39], s53 offen offset:256 lds\n"
    "v_mfma_f32_16x16x32_bf16 v[48:51], a[156:159], a[28:31], v[48:51]\n"
    "s_add_u32 m0, m0, s97\n"
    "v_add_f32 v81, v81, v21\n"
    "v_add_f32 v82, v82, v25\n"
    "v_mfma_f32_16x16x32_bf16 v[52:55], a[152:155], a[56:59], v[52:55]\n"
    "s_mov_b32 s63, s71\n"
    "v_add_f32 v80, v80, v18\n"
    "v_add_f32 v81, v81, v22\n"
    "v_add_f32_e64 v82, v82, v26\n"
    "v_mfma_f32_16x16x32_bf16 v[52:55], a[156:159], a[60:63], v[52:55]\n"
    "s_sub_u32 s62, s63, s60\n"
    "v_add_f32 v80, v80, v19\n"
    "v_add_f32_e64 v81, v81, v23\n"
    "v_add_f32_e64 v82, v82, v27\n"
    "v_mfma_f32_16x16x32_bf16 v[56:59], a[152:155], a[88:91], v[56:59]\n"
    "s_mov_b32 s57, s79\n"
    "ds_read_b64_tr_b16 a[152:153], v91 offset:8\n"
    "ds_read_b64_tr_b16 a[154:155], v91 offset:8200\n"
    "v_cvt_pk_bf16_f32 v2, v16, v17\n"
    "v_cvt_pk_bf16_f32 v3, v18, v19\n"
    "v_mfma_f32_16x16x32_bf16 v[56:59], a[156:159], a[92:95], v[56:59]\n"
    "s_waitcnt lgkmcnt(10)\n"
    "ds_read_b64_tr_b16 a[156:157], v91 offset:16392\n"
    "ds_read_b64_tr_b16 a[158:159], v91 offset:24584\n"
    "v_mfma_f32_16x16x32_bf16 v[96:99], a[128:131], v[64:67], v[96:99]\n"
    "buffer_load_dwordx4 v92, s[36:39], s53 offen offset:384 lds\n"
    "v_cvt_pk_bf16_f32 v6, v20, v21\n"
    "v_cvt_pk_bf16_f32 v7, v22, v23\n"
    "v_mfma_f32_16x16x32_bf16 v[96:99], a[132:135], v[0:3], v[96:99]\n"
    "s_add_u32 m0, m0, 1408\n"
    "v_cvt_pk_bf16_f32 v10, v24, v25\n"
    "v_cvt_pk_bf16_f32 v11, v26, v27\n"
    "v_mfma_f32_16x16x32_bf16 v[160:163], a[128:131], v[68:71], v[160:163]\n"
    "s_mov_b32 s58, 0x100\n"
    "v_fma_f32 v32, v32, s52, v84\n"
    "v_fma_f32 v34, v34, s52, v84\n"
    "v_mfma_f32_16x16x32_bf16 v[160:163], a[132:135], v[4:7], v[160:163]\n"
    "s_mov_b32 s59, s70\n"
    "v_exp_f32 v32, v32\n"
    "v_mfma_f32_16x16x32_bf16 v[224:227], a[128:131], v[72:75], v[224:227]\n"
    "s_mov_b32 s98, s79\n"
    "ds_read_b64_tr_b16 a[128:129], v90 offset:1024\n"
    "ds_read_b64_tr_b16 a[130:131], v90 offset:9216\n"
    "v_mfma_f32_16x16x32_bf16 v[224:227], a[132:135], v[8:11], v[224:227]\n"
    "s_waitcnt lgkmcnt(10)\n"
    "ds_read_b64_tr_b16 a[132:133], v90 offset:17408\n"
    "ds_read_b64_tr_b16 a[134:135], v90 offset:25600\n"
    "v_mfma_f32_16x16x32_bf16 v[100:103], a[136:139], v[64:67], v[100:103]\n"
    "buffer_load_dwordx4 v92, s[36:39], s54 offen offset:0 lds\n"
    "v_mfma_f32_16x16x32_bf16 v[100:103], a[140:143], v[0:3], v[100:103]\n"
    "s_add_u32 m0, m0, s97\n"
    "v_exp_f32 v34, v34\n"
    "v_mfma_f32_16x16x32_bf16 v[164:167], a[136:139], v[68:71], v[164:167]\n"
    "s_mov_b32 s99, 0x100\n"
    "v_fma_f32 v36, v36, s52, v85\n"
    "v_fma_f32 v38, v38, s52, v85\n"
    "v_mfma_f32_16x16x32_bf16 v[164:167], a[140:143], v[4:7], v[164:167]\n"
    "s_mov_b32 s65, 5\n"
    "v_exp_f32 v36, v36\n"
    "v_mfma_f32_16x16x32_bf16 v[228:231], a[136:139], v[72:75], v[228:231]\n"
    "ds_read_b64_tr_b16 a[136:137], v90 offset:1032\n"
    "ds_read_b64_tr_b16 a[138:139], v90 offset:9224\n"
    "v_mfma_f32_16x16x32_bf16 v[228:231], a[140:143], v[8:11], v[228:231]\n"
    "s_waitcnt lgkmcnt(10)\n"
    "ds_read_b64_tr_b16 a[140:141], v90 offset:17416\n"
    "ds_read_b64_tr_b16 a[142:143], v90 offset:25608\n"
    "v_mfma_f32_16x16x32_bf16 v[104:107], a[144:147], v[64:67], v[104:107]\n"
    "s_nop 0\n"
    "buffer_load_dwordx4 v92, s[36:39], s54 offen offset:128 lds\n"
    "v_mfma_f32_16x16x32_bf16 v[104:107], a[148:151], v[0:3], v[104:107]\n"
    "s_add_u32 m0, m0, s97\n"
    "v_exp_f32 v38, v38\n"
    "v_mfma_f32_16x16x32_bf16 v[168:171], a[144:147], v[68:71], v[168:171]\n"
    "v_fma_f32 v40, v40, s52, v86\n"
    "v_fma_f32 v42, v42, s52, v86\n"
    "v_mfma_f32_16x16x32_bf16 v[168:171], a[148:151], v[4:7], v[168:171]\n"
    "v_exp_f32_e64 v40, v40\n"
    "v_mfma_f32_16x16x32_bf16 v[232:235], a[144:147], v[72:75], v[232:235]\n"
    "ds_read_b64_tr_b16 a[144:145], v91 offset:1024\n"
    "ds_read_b64_tr_b16 a[146:147], v91 offset:9216\n"
    "v_mfma_f32_16x16x32_bf16 v[232:235], a[148:151], v[8:11], v[232:235]\n"
    "s_waitcnt lgkmcnt(10)\n"
    "ds_read_b64_tr_b16 a[148:149], v91 offset:17408\n"
    "ds_read_b64_tr_b16 a[150:151], v91 offset:25600\n"
    "v_mfma_f32_16x16x32_bf16 v[108:111], a[152:155], v[64:67], v[108:111]\n"
    "s_nop 0\n"
    "buffer_load_dwordx4 v92, s[36:39], s54 offen offset:256 lds\n"
    "v_mfma_f32_16x16x32_bf16 v[108:111], a[156:159], v[0:3], v[108:111]\n"
    "s_add_u32 m0, m0, s97\n"
    "v_exp_f32 v42, v42\n"
    "v_mfma_f32_16x16x32_bf16 v[172:175], a[152:155], v[68:71], v[172:175]\n"
    "v_fma_f32 v33, v33, s52, v84\n"
    "v_fma_f32 v35, v35, s52, v84\n"
    "v_mfma_f32_16x16x32_bf16 v[172:175], a[156:159], v[4:7], v[172:175]\n"
    "v_exp_f32_e64 v33, v33\n"
    "v_mfma_f32_16x16x32_bf16 v[236:239], a[152:155], v[72:75], v[236:239]\n"
    "ds_read_b64_tr_b16 a[152:153], v91 offset:1032\n"
    "ds_read_b64_tr_b16 a[154:155], v91 offset:9224\n"
    "v_mfma_f32_16x16x32_bf16 v[236:239], a[156:159], v[8:11], v[236:239]\n"
    "s_waitcnt lgkmcnt(10)\n"
    "ds_read_b64_tr_b16 a[156:157], v91 offset:17416\n"
    "ds_read_b64_tr_b16 a[158:159], v91 offset:25608\n"
    "v_mfma_f32_16x16x32_bf16 v[112:115], a[128:131], v[64:67], v[112:115]\n"
    "s_nop 0\n"
    "buffer_load_dwordx4 v92, s[36:39], s54 offen offset:384 lds\n"
    "v_mfma_f32_16x16x32_bf16 v[112:115], a[132:135], v[0:3], v[112:115]\n"
    "s_add_u32 s53, s53, 0x8000\n"
    "v_exp_f32_e64 v35, v35\n"
    "v_mfma_f32_16x16x32_bf16 v[176:179], a[128:131], v[68:71], v[176:179]\n"
    "s_add_u32 s54, s54, 0x8000\n"
    "v_fma_f32 v37, v37, s52, v85\n"
    "v_fma_f32 v39, v39, s52, v85\n"
    "v_mfma_f32_16x16x32_bf16 v[176:179], a[132:135], v[4:7], v[176:179]\n"
    "s_waitcnt vmcnt(8)\n"
    "ds_read_b128 a[0:3], v94 offset:0\n"
    "ds_read_b128 a[4:7], v95 offset:0\n"
    "v_mfma_f32_16x16x32_bf16 v[240:243], a[128:131], v[72:75], v[240:243]\n"
    "ds_read_b64_tr_b16 a[128:129], v90 offset:2048\n"
    "ds_read_b64_tr_b16 a[130:131], v90 offset:10240\n"
    "v_mfma_f32_16x16x32_bf16 v[240:243], a[132:135], v[8:11], v[240:243]\n"
    "s_waitcnt lgkmcnt(12)\n"
    "ds_read_b64_tr_b16 a[132:133], v90 offset:18432\n"
    "ds_read_b64_tr_b16 a[134:135], v90 offset:26624\n"
    "v_mfma_f32_16x16x32_bf16 v[116:119], a[136:139], v[64:67], v[116:119]\n"
    "ds_read_b128 a[8:11], v94 offset:1024\n"
    "ds_read_b128 a[12:15], v95 offset:1024\n"
    "v_mfma_f32_16x16x32_bf16 v[116:119], a[140:143], v[0:3], v[116:119]\n"
    "ds_read_b128 a[16:19], v94 offset:2048\n"
    "ds_read_b128 a[20:23], v95 offset:2048\n"
    "v_mfma_f32_16x16x32_bf16 v[180:183], a[136:139], v[68:71], v[180:183]\n"
    "ds_read_b128 a[24:27], v94 offset:3072\n"
    "ds_read_b128 a[28:31], v95 offset:3072\n"
    "v_mfma_f32_16x16x32_bf16 v[180:183], a[140:143], v[4:7], v[180:183]\n"
    "ds_read_b128 a[32:35], v94 offset:8192\n"
    "ds_read_b128 a[36:39], v95 offset:8192\n"
    "v_mfma_f32_16x16x32_bf16 v[244:247], a[136:139], v[72:75], v[244:247]\n"
    "ds_read_b64_tr_b16 a[136:137], v90 offset:2056\n"
    "ds_read_b64_tr_b16 a[138:139], v90 offset:10248\n"
    "v_mfma_f32_16x16x32_bf16 v[244:247], a[140:143], v[8:11], v[244:247]\n"
    "s_waitcnt lgkmcnt(15)\n"
    "ds_read_b64_tr_b16 a[140:141], v90 offset:18440\n"
    "s_nop 0\n"
    "ds_read_b64_tr_b16 a[142:143], v90 offset:26632\n"
    "v_mfma_f32_16x16x32_bf16 v[120:123], a[144:147], v[64:67], v[120:123]\n"
    "ds_read_b128 a[40:43], v94 offset:9216\n"
    "ds_read_b128 a[44:47], v95 offset:9216\n"
    "v_mfma_f32_16x16x32_bf16 v[120:123], a[148:151], v[0:3], v[120:123]\n"
    "ds_read_b128 a[48:51], v94 offset:10240\n"
    "ds_read_b128 a[52:55], v95 offset:10240\n"
    "v_mfma_f32_16x16x32_bf16 v[184:187], a[144:147], v[68:71], v[184:187]\n"
    "ds_read_b128 a[56:59], v94 offset:11264\n"
    "ds_read_b128 a[60:63], v95 offset:11264\n"
    "v_mfma_f32_16x16x32_bf16 v[184:187], a[148:151], v[4:7], v[184:187]\n"
    "v_exp_f32_e64 v37, v37\n"
    "v_mfma_f32_16x16x32_bf16 v[248:251], a[144:147], v[72:75], v[248:251]\n"
    "ds_read_b64_tr_b16 a[144:145], v91 offset:2048\n"
    "ds_read_b64_tr_b16 a[146:147], v91 offset:10240\n"
    "v_mfma_f32_16x16x32_bf16 v[248:251], a[148:151], v[8:11], v[248:251]\n"
    "s_waitcnt lgkmcnt(15)\n"
    "ds_read_b64_tr_b16 a[148:149], v91 offset:18432\n"
    "ds_read_b64_tr_b16 a[150:151], v91 offset:26624\n"
    "v_mfma_f32_16x16x32_bf16 v[124:127], a[152:155], v[64:67], v[124:127]\n"
    "v_exp_f32 v39, v39\n"
    "v_mfma_f32_16x16x32_bf16 v[124:127], a[156:159], v[0:3], v[124:127]\n"
    "v_fma_f32 v41, v41, s52, v86\n"
    "v_fma_f32 v43, v43, s52, v86\n"
    "v_mfma_f32_16x16x32_bf16 v[188:191], a[152:155], v[68:71], v[188:191]\n"
    "v_exp_f32_e64 v41, v41\n"
    "v_mfma_f32_16x16x32_bf16 v[188:191], a[156:159], v[4:7], v[188:191]\n"
    "v_exp_f32_e64 v43, v43\n"
    "v_mfma_f32_16x16x32_bf16 v[252:255], a[152:155], v[72:75], v[252:255]\n"
    "s_waitcnt lgkmcnt(4)\n"
    "ds_read_b64_tr_b16 a[152:153], v91 offset:2056\n"
    "ds_read_b64_tr_b16 a[154:155], v91 offset:10248\n"
    "v_mfma_f32_16x16x32_bf16 v[252:255], a[156:159], v[8:11], v[252:255]\n"
    "s_waitcnt lgkmcnt(15)\n"
    "ds_read_b64_tr_b16 a[156:157], v91 offset:18440\n"
    "ds_read_b64_tr_b16 a[158:159], v91 offset:26632\n"
    "v_mfma_f32_16x16x32_bf16 v[128:131], a[128:131], v[64:67], v[128:131]\n"
    "s_add_u32 s73, s78, 16384\n"
    "v_add_f32_e64 v80, v80, v32\n"
    "v_add_f32_e64 v81, v81, v36\n"
    "v_mfma_f32_16x16x32_bf16 v[128:131], a[132:135], v[0:3], v[128:131]\n"
    "s_add_u32 s74, s78, 20480\n"
    "v_add_f32 v82, v82, v40\n"
    "v_add_f32_e64 v80, v80, v33\n"
    "v_mfma_f32_16x16x32_bf16 v[192:195], a[128:131], v[68:71], v[192:195]\n"
    "s_add_u32 m0, s70, 0\n"
    "v_add_f32_e64 v81, v81, v37\n"
    "v_add_f32_e64 v82, v82, v41\n"
    "v_mfma_f32_16x16x32_bf16 v[192:195], a[132:135], v[4:7], v[192:195]\n"
    "buffer_load_dwordx4 v92, s[44:47], s73 offen offset:0 nt lds\n"
    "v_mfma_f32_16x16x32_bf16 a[224:227], a[128:131], v[72:75], a[224:227]\n"
    "s_add_u32 m0, m0, s97\n"
    "ds_read_b64_tr_b16 a[128:129], v90 offset:3072\n"
    "ds_read_b64_tr_b16 a[130:131], v90 offset:11264\n"
    "v_mfma_f32_16x16x32_bf16 a[224:227], a[132:135], v[8:11], a[224:227]\n"
    "s_waitcnt lgkmcnt(15)\n"
    "ds_read_b64_tr_b16 a[132:133], v90 offset:19456\n"
    "ds_read_b64_tr_b16 a[134:135], v90 offset:27648\n"
    "v_mfma_f32_16x16x32_bf16 v[132:135], a[136:139], v[64:67], v[132:135]\n"
    "v_add_f32 v80, v80, v34\n"
    "v_add_f32 v81, v81, v38\n"
    "v_mfma_f32_16x16x32_bf16 v[132:135], a[140:143], v[0:3], v[132:135]\n"
    "buffer_load_dwordx4 v92, s[44:47], s73 offen offset:128 nt lds\n"
    "v_mfma_f32_16x16x32_bf16 v[196:199], a[136:139], v[68:71], v[196:199]\n"
    "s_add_u32 m0, m0, s97\n"
    "v_add_f32 v82, v82, v42\n"
    "v_add_f32_e64 v80, v80, v35\n"
    "v_mfma_f32_16x16x32_bf16 v[196:199], a[140:143], v[4:7], v[196:199]\n"
    "v_add_f32_e64 v81, v81, v39\n"
    "v_add_f32_e64 v82, v82, v43\n"
    "v_mfma_f32_16x16x32_bf16 a[228:231], a[136:139], v[72:75], a[228:231]\n"
    "ds_read_b64_tr_b16 a[136:137], v90 offset:3080\n"
    "ds_read_b64_tr_b16 a[138:139], v90 offset:11272\n"
    "v_mfma_f32_16x16x32_bf16 a[228:231], a[140:143], v[8:11], a[228:231]\n"
    "s_waitcnt lgkmcnt(10)\n"
    "ds_read_b64_tr_b16 a[140:141], v90 offset:19464\n"
    "ds_read_b64_tr_b16 a[142:143], v90 offset:27656\n"
    "v_mfma_f32_16x16x32_bf16 v[136:139], a[144:147], v[64:67], v[136:139]\n"
    "s_nop 0\n"
    "buffer_load_dwordx4 v92, s[44:47], s73 offen offset:256 nt lds\n"
    "v_mfma_f32_16x16x32_bf16 v[136:139], a[148:151], v[0:3], v[136:139]\n"
    "s_add_u32 m0, m0, 896\n"
    "v_cvt_pk_bf16_f32 v32, v32, v33\n"
    "v_cvt_pk_bf16_f32 v33, v34, v35\n"
    "v_mfma_f32_16x16x32_bf16 v[200:203], a[144:147], v[68:71], v[200:203]\n"
    "v_cvt_pk_bf16_f32 v36, v36, v37\n"
    "v_cvt_pk_bf16_f32 v37, v38, v39\n"
    "v_mfma_f32_16x16x32_bf16 v[200:203], a[148:151], v[4:7], v[200:203]\n"
    "v_cvt_pk_bf16_f32 v40, v40, v41\n"
    "v_cvt_pk_bf16_f32 v41, v42, v43\n"
    "v_mfma_f32_16x16x32_bf16 a[232:235], a[144:147], v[72:75], a[232:235]\n"
    "s_nop 0\n"
    "ds_read_b64_tr_b16 a[144:145], v91 offset:3072\n"
    "ds_read_b64_tr_b16 a[146:147], v91 offset:11264\n"
    "v_mfma_f32_16x16x32_bf16 a[232:235], a[148:151], v[8:11], a[232:235]\n"
    "s_waitcnt lgkmcnt(10)\n"
    "ds_read_b64_tr_b16 a[148:149], v91 offset:19456\n"
    "ds_read_b64_tr_b16 a[150:151], v91 offset:27648\n"
    "v_mfma_f32_16x16x32_bf16 v[140:143], a[152:155], v[64:67], v[140:143]\n"
    "buffer_load_dwordx4 v92, s[44:47], s73 offen offset:384 nt lds\n"
    "v_mfma_f32_16x16x32_bf16 v[140:143], a[156:159], v[0:3], v[140:143]\n"
    "s_add_u32 m0, m0, 1408\n"
    "v_fma_f32 v48, v48, s52, v84\n"
    "v_fma_f32 v50, v50, s52, v84\n"
    "v_mfma_f32_16x16x32_bf16 v[204:207], a[152:155], v[68:71], v[204:207]\n"
    "v_exp_f32 v48, v48\n"
    "v_mfma_f32_16x16x32_bf16 v[204:207], a[156:159], v[4:7], v[204:207]\n"
    "v_exp_f32 v50, v50\n"
    "v_mfma_f32_16x16x32_bf16 a[236:239], a[152:155], v[72:75], a[236:239]\n"
    "s_nop 0\n"
    "ds_read_b64_tr_b16 a[152:153], v91 offset:3080\n"
    "ds_read_b64_tr_b16 a[154:155], v91 offset:11272\n"
    "v_mfma_f32_16x16x32_bf16 a[236:239], a[156:159], v[8:11], a[236:239]\n"
    "s_waitcnt lgkmcnt(10)\n"
    "ds_read_b64_tr_b16 a[156:157], v91 offset:19464\n"
    "ds_read_b64_tr_b16 a[158:159], v91 offset:27656\n"
    "v_mfma_f32_16x16x32_bf16 v[144:147], a[128:131], v[64:67], v[144:147]\n"
    "buffer_load_dwordx4 v92, s[44:47], s74 offen offset:0 nt lds\n"
    "v_mfma_f32_16x16x32_bf16 v[144:147], a[132:135], v[0:3], v[144:147]\n"
    "s_add_u32 m0, m0, s97\n"
    "v_add_u32 v90, s64, v90\n"
    "v_add_u32_e64 v91, s64, v91\n"
    "v_mfma_f32_16x16x32_bf16 v[208:211], a[128:131], v[68:71], v[208:211]\n"
    "v_fma_f32 v52, v52, s52, v85\n"
    "v_fma_f32 v54, v54, s52, v85\n"
    "v_mfma_f32_16x16x32_bf16 v[208:211], a[132:135], v[4:7], v[208:211]\n"
    "v_exp_f32_e64 v52, v52\n"
    "v_mfma_f32_16x16x32_bf16 a[240:243], a[128:131], v[72:75], a[240:243]\n"
    "s_waitcnt vmcnt(15) lgkmcnt(0)\n"
    "buffer_load_dwordx4 v92, s[44:47], s74 offen offset:128 nt lds\n"
    "v_mfma_f32_16x16x32_bf16 a[240:243], a[132:135], v[8:11], a[240:243]\n"
    "s_barrier\n"
    "v_mfma_f32_16x16x32_bf16 v[148:151], a[136:139], v[64:67], v[148:151]\n"
    "s_add_u32 m0, m0, 896\n"
    "ds_read_b64_tr_b16 a[128:129], v90 offset:0\n"
    "ds_read_b64_tr_b16 a[130:131], v90 offset:8192\n"
    "v_mfma_f32_16x16x32_bf16 v[148:151], a[140:143], v[0:3], v[148:151]\n"
    "ds_read_b64_tr_b16 a[132:133], v90 offset:8\n"
    "ds_read_b64_tr_b16 a[134:135], v90 offset:8200\n"
    "v_mfma_f32_16x16x32_bf16 v[212:215], a[136:139], v[68:71], v[212:215]\n"
    "buffer_load_dwordx4 v92, s[44:47], s74 offen offset:256 nt lds\n"
    "v_mfma_f32_16x16x32_bf16 v[212:215], a[140:143], v[4:7], v[212:215]\n"
    "s_add_u32 m0, m0, s97\n"
    "v_exp_f32 v54, v54\n"
    "v_mfma_f32_16x16x32_bf16 a[244:247], a[136:139], v[72:75], a[244:247]\n"
    "ds_read_b64_tr_b16 a[136:137], v91 offset:0\n"
    "ds_read_b64_tr_b16 a[138:139], v91 offset:8192\n"
    "v_mfma_f32_16x16x32_bf16 a[244:247], a[140:143], v[8:11], a[244:247]\n"
    "ds_read_b64_tr_b16 a[140:141], v91 offset:8\n"
    "ds_read_b64_tr_b16 a[142:143], v91 offset:8200\n"
    "v_mfma_f32_16x16x32_bf16 v[152:155], a[144:147], v[64:67], v[152:155]\n"
    "buffer_load_dwordx4 v92, s[44:47], s74 offen offset:384 nt lds\n"
    "v_mfma_f32_16x16x32_bf16 v[152:155], a[148:151], v[0:3], v[152:155]\n"
    "s_add_u32 s73, s78, 24576\n"
    "v_fma_f32 v56, v56, s52, v86\n"
    "v_fma_f32 v58, v58, s52, v86\n"
    "v_mfma_f32_16x16x32_bf16 v[216:219], a[144:147], v[68:71], v[216:219]\n"
    "s_add_u32 s74, s78, 28672\n"
    "v_exp_f32 v56, v56\n"
    "v_mfma_f32_16x16x32_bf16 v[216:219], a[148:151], v[4:7], v[216:219]\n"
    "s_add_u32 m0, s70, 8192\n"
    "v_exp_f32 v58, v58\n"
    "v_mfma_f32_16x16x32_bf16 a[248:251], a[144:147], v[72:75], a[248:251]\n"
    "ds_read_b64_tr_b16 a[144:145], v90 offset:1024\n"
    "ds_read_b64_tr_b16 a[146:147], v90 offset:9216\n"
    "v_mfma_f32_16x16x32_bf16 a[248:251], a[148:151], v[8:11], a[248:251]\n"
    "ds_read_b64_tr_b16 a[148:149], v90 offset:1032\n"
    "ds_read_b64_tr_b16 a[150:151], v90 offset:9224\n"
    "v_fma_f32 v49, v49, s52, v84\n"
    "v_fma_f32 v51, v51, s52, v84\n"
    "v_fma_f32 v53, v53, s52, v85\n"
    "v_fma_f32 v55, v55, s52, v85\n"
    "v_fma_f32 v57, v57, s52, v86\n"
    "v_fma_f32 v59, v59, s52, v86\n"
    "v_mfma_f32_16x16x32_bf16 v[156:159], a[152:155], v[64:67], v[156:159]\n"
    "buffer_load_dwordx4 v92, s[44:47], s73 offen offset:0 nt lds\n"
    "v_exp_f32 v49, v49\n"
    "v_add_f32 v80, v80, v48\n"
    "v_exp_f32 v53, v53\n"
    "v_add_f32 v81, v81, v52\n"
    "v_exp_f32 v57, v57\n"
    "v_add_f32 v82, v82, v56\n"
    "v_mfma_f32_16x16x32_bf16 v[156:159], a[156:159], v[0:3], v[156:159]\n"
    "s_add_u32 m0, m0, s97\n"
    "v_add_f32 v80, v80, v49\n"
    "v_exp_f32 v51, v51\n"
    "v_add_f32 v81, v81, v53\n"
    "v_exp_f32 v55, v55\n"
    "v_add_f32 v82, v82, v57\n"
    "v_exp_f32 v59, v59\n"
    "v_mfma_f32_16x16x32_bf16 v[220:223], a[152:155], v[68:71], v[220:223]\n"
    "v_add_f32 v80, v80, v50\n"
    "v_add_f32_e64 v81, v81, v54\n"
    "v_add_f32_e64 v82, v82, v58\n"
    "v_mfma_f32_16x16x32_bf16 v[220:223], a[156:159], v[4:7], v[220:223]\n"
    "v_cvt_pk_bf16_f32 v34, v48, v49\n"
    "v_add_f32_e64 v80, v80, v51\n"
    "v_cvt_pk_bf16_f32 v35, v50, v51\n"
    "v_add_f32_e64 v81, v81, v55\n"
    "v_add_f32_e64 v82, v82, v59\n"
    "v_mfma_f32_16x16x32_bf16 a[252:255], a[152:155], v[72:75], a[252:255]\n"
    "s_nop 0\n"
    "ds_read_b64_tr_b16 a[152:153], v91 offset:1024\n"
    "ds_read_b64_tr_b16 a[154:155], v91 offset:9216\n"
    "v_mfma_f32_16x16x32_bf16 a[252:255], a[156:159], v[8:11], a[252:255]\n"
    "s_waitcnt lgkmcnt(10)\n"
    "ds_read_b64_tr_b16 a[156:157], v91 offset:1032\n"
    "ds_read_b64_tr_b16 a[158:159], v91 offset:9224\n"
    "v_cvt_pk_bf16_f32 v38, v52, v53\n"
    "v_cvt_pk_bf16_f32 v39, v54, v55\n"
    "v_mfma_f32_16x16x32_bf16 v[96:99], a[128:131], v[32:35], v[96:99]\n"
    "buffer_load_dwordx4 v92, s[44:47], s73 offen offset:128 nt lds\n"
    "v_mfma_f32_16x16x32_bf16 v[100:103], a[132:135], v[32:35], v[100:103]\n"
    "s_add_u32 m0, m0, 896\n"
    "v_cvt_pk_bf16_f32 v42, v56, v57\n"
    "v_cvt_pk_bf16_f32 v43, v58, v59\n"
    "v_mfma_f32_16x16x32_bf16 v[160:163], a[128:131], v[36:39], v[160:163]\n"
    "v_mov_b32_e64 v20, v80\n"
    "v_mov_b32_e64 v21, v81\n"
    "v_mfma_f32_16x16x32_bf16 v[164:167], a[132:135], v[36:39], v[164:167]\n"
    "v_mov_b32_e64 v22, v82\n"
    "v_mbcnt_lo_u32_b32 v25, -1, 0\n"
    "v_mfma_f32_16x16x32_bf16 v[224:227], a[128:131], v[40:43], v[224:227]\n"
    "ds_read_b64_tr_b16 a[128:129], v90 offset:2048\n"
    "ds_read_b64_tr_b16 a[130:131], v90 offset:10240\n"
    "v_mfma_f32_16x16x32_bf16 v[228:231], a[132:135], v[40:43], v[228:231]\n"
    "s_waitcnt lgkmcnt(10)\n"
    "ds_read_b64_tr_b16 a[132:133], v90 offset:2056\n"
    "ds_read_b64_tr_b16 a[134:135], v90 offset:10248\n"
    "v_mfma_f32_16x16x32_bf16 v[104:107], a[136:139], v[32:35], v[104:107]\n"
    "s_nop 0\n"
    "buffer_load_dwordx4 v92, s[44:47], s73 offen offset:256 nt lds\n"
    "v_mfma_f32_16x16x32_bf16 v[108:111], a[140:143], v[32:35], v[108:111]\n"
    "s_add_u32 m0, m0, s97\n"
    "v_permlane32_swap_b32 v80, v20\n"
    "v_mfma_f32_16x16x32_bf16 v[168:171], a[136:139], v[36:39], v[168:171]\n"
    "s_nop 0\n"
    "v_permlane32_swap_b32 v81, v21\n"
    "v_mfma_f32_16x16x32_bf16 v[172:175], a[140:143], v[36:39], v[172:175]\n"
    "s_nop 0\n"
    "v_permlane32_swap_b32 v82, v22\n"
    "v_mfma_f32_16x16x32_bf16 v[232:235], a[136:139], v[40:43], v[232:235]\n"
    "s_nop 0\n"
    "ds_read_b64_tr_b16 a[136:137], v91 offset:2048\n"
    "ds_read_b64_tr_b16 a[138:139], v91 offset:10240\n"
    "v_mfma_f32_16x16x32_bf16 v[236:239], a[140:143], v[40:43], v[236:239]\n"
    "s_waitcnt lgkmcnt(10)\n"
    "ds_read_b64_tr_b16 a[140:141], v91 offset:2056\n"
    "ds_read_b64_tr_b16 a[142:143], v91 offset:10248\n"
    "v_mfma_f32_16x16x32_bf16 v[112:115], a[144:147], v[32:35], v[112:115]\n"
    "buffer_load_dwordx4 v92, s[44:47], s73 offen offset:384 nt lds\n"
    "v_mfma_f32_16x16x32_bf16 v[116:119], a[148:151], v[32:35], v[116:119]\n"
    "s_add_u32 m0, m0, 1408\n"
    "v_add_f32 v80, v80, v20\n"
    "v_add_f32 v81, v81, v21\n"
    "v_mfma_f32_16x16x32_bf16 v[176:179], a[144:147], v[36:39], v[176:179]\n"
    "v_add_f32 v82, v82, v22\n"
    "v_mov_b32 v20, v80\n"
    "v_mfma_f32_16x16x32_bf16 v[180:183], a[148:151], v[36:39], v[180:183]\n"
    "v_mov_b32_e64 v21, v81\n"
    "v_mov_b32_e64 v22, v82\n"
    "v_mfma_f32_16x16x32_bf16 v[240:243], a[144:147], v[40:43], v[240:243]\n"
    "ds_read_b64_tr_b16 a[144:145], v90 offset:3072\n"
    "ds_read_b64_tr_b16 a[146:147], v90 offset:11264\n"
    "v_mfma_f32_16x16x32_bf16 v[244:247], a[148:151], v[40:43], v[244:247]\n"
    "s_waitcnt lgkmcnt(10)\n"
    "ds_read_b64_tr_b16 a[148:149], v90 offset:3080\n"
    "ds_read_b64_tr_b16 a[150:151], v90 offset:11272\n"
    "v_mfma_f32_16x16x32_bf16 v[120:123], a[152:155], v[32:35], v[120:123]\n"
    "s_nop 0\n"
    "buffer_load_dwordx4 v92, s[44:47], s74 offen offset:0 nt lds\n"
    "v_mfma_f32_16x16x32_bf16 v[124:127], a[156:159], v[32:35], v[124:127]\n"
    "s_add_u32 m0, m0, s97\n"
    "v_permlane16_swap_b32 v80, v20\n"
    "v_mfma_f32_16x16x32_bf16 v[184:187], a[152:155], v[36:39], v[184:187]\n"
    "s_nop 0\n"
    "v_permlane16_swap_b32 v81, v21\n"
    "v_mfma_f32_16x16x32_bf16 v[188:191], a[156:159], v[36:39], v[188:191]\n"
    "s_nop 0\n"
    "v_permlane16_swap_b32 v82, v22\n"
    "v_mfma_f32_16x16x32_bf16 v[248:251], a[152:155], v[40:43], v[248:251]\n"
    "s_nop 0\n"
    "ds_read_b64_tr_b16 a[152:153], v91 offset:3072\n"
    "ds_read_b64_tr_b16 a[154:155], v91 offset:11264\n"
    "v_mfma_f32_16x16x32_bf16 v[252:255], a[156:159], v[40:43], v[252:255]\n"
    "s_waitcnt lgkmcnt(10)\n"
    "ds_read_b64_tr_b16 a[156:157], v91 offset:3080\n"
    "ds_read_b64_tr_b16 a[158:159], v91 offset:11272\n"
    "v_mfma_f32_16x16x32_bf16 v[128:131], a[128:131], v[32:35], v[128:131]\n"
    "buffer_load_dwordx4 v92, s[44:47], s74 offen offset:128 nt lds\n"
    "v_mfma_f32_16x16x32_bf16 v[132:135], a[132:135], v[32:35], v[132:135]\n"
    "s_add_u32 m0, m0, s97\n"
    "v_add_f32 v80, v80, v20\n"
    "v_add_f32 v81, v81, v21\n"
    "v_mfma_f32_16x16x32_bf16 v[192:195], a[128:131], v[36:39], v[192:195]\n"
    "v_add_f32 v82, v82, v22\n"
    "v_add_f32 v24, v80, v81\n"
    "v_mfma_f32_16x16x32_bf16 v[196:199], a[132:135], v[36:39], v[196:199]\n"
    "v_add_f32 v24, v24, v82\n"
    "v_mbcnt_hi_u32_b32 v25, -1, v25\n"
    "v_mfma_f32_16x16x32_bf16 a[224:227], a[128:131], v[40:43], a[224:227]\n"
    "s_waitcnt vmcnt(14) lgkmcnt(0)\n"
    "buffer_load_dwordx4 v92, s[44:47], s74 offen offset:256 nt lds\n"
    "v_mfma_f32_16x16x32_bf16 a[228:231], a[132:135], v[40:43], a[228:231]\n"
    "s_barrier\n"
    "v_mfma_f32_16x16x32_bf16 v[136:139], a[136:139], v[32:35], v[136:139]\n"
    "s_add_u32 m0, m0, s97\n"
    "v_rcp_f32 v80, v80\n"
    "v_mfma_f32_16x16x32_bf16 v[140:143], a[140:143], v[32:35], v[140:143]\n"
    "v_rcp_f32_e64 v81, v81\n"
    "v_mfma_f32_16x16x32_bf16 v[200:203], a[136:139], v[36:39], v[200:203]\n"
    "buffer_load_dwordx4 v92, s[44:47], s74 offen offset:384 nt lds\n"
    "v_mfma_f32_16x16x32_bf16 v[204:207], a[140:143], v[36:39], v[204:207]\n"
    "s_add_u32 s44, s44, 434176\n"
    "v_rcp_f32_e64 v82, v82\n"
    "v_mfma_f32_16x16x32_bf16 a[232:235], a[136:139], v[40:43], a[232:235]\n"
    "s_addc_u32 s45, s45, 0\n"
    "v_lshrrev_b32 v26, 3, v25\n"
    "v_and_b32 v27, 7, v25\n"
    "v_mfma_f32_16x16x32_bf16 a[236:239], a[140:143], v[40:43], a[236:239]\n"
    "v_xor_b32 v19, v26, v27\n"
    "v_lshlrev_b32 v19, 4, v19\n"
    "v_mfma_f32_16x16x32_bf16 v[144:147], a[144:147], v[32:35], v[144:147]\n"
    "s_cmp_ge_u32 s66, 3\n"
    "v_lshl_add_u32 v19, v26, 9, v19\n"
    "v_and_b32_e64 v26, 1, v26\n"
    "v_mfma_f32_16x16x32_bf16 v[148:151], a[148:151], v[32:35], v[148:151]\n"
    "s_cselect_b32 s46, 0, s46\n"
    "v_lshlrev_b32 v26, 10, v26\n"
    "v_lshl_add_u32 v26, v27, 7, v26\n"
    "v_mfma_f32_16x16x32_bf16 v[208:211], a[144:147], v[36:39], v[208:211]\n"
    "v_lshrrev_b32 v16, 4, v25\n"
    "v_xor_b32 v16, v16, v27\n"
    "v_mfma_f32_16x16x32_bf16 v[212:215], a[148:151], v[36:39], v[212:215]\n"
    "v_lshl_add_u32 v16, v16, 4, v26\n"
    "v_lshlrev_b32_e64 v18, 4, v25\n"
    "v_mfma_f32_16x16x32_bf16 a[240:243], a[144:147], v[40:43], a[240:243]\n"
    "v_cmp_nge_f32 vcc, s75, v24\n"
    "v_add_u32 v16, s72, v16\n"
    "v_mfma_f32_16x16x32_bf16 a[244:247], a[148:151], v[40:43], a[244:247]\n"
    "v_xor_b32 v17, 64, v16\n"
    "v_add_u32 v18, s72, v18\n"
    "v_mfma_f32_16x16x32_bf16 v[152:155], a[152:155], v[32:35], v[152:155]\n"
    "s_cmp_lg_u64 vcc, 0\n"
    "v_mul_f32 v96, v96, v80\n"
    "v_mul_f32 v97, v97, v80\n"
    "v_mfma_f32_16x16x32_bf16 v[156:159], a[156:159], v[32:35], v[156:159]\n"
    "s_cselect_b32 s71, s68, 0\n"
    "v_mul_f32 v98, v98, v80\n"
    "v_mul_f32_e64 v99, v99, v80\n"
    "v_mfma_f32_16x16x32_bf16 v[216:219], a[152:155], v[36:39], v[216:219]\n"
    "s_or_b32 s67, s67, s71\n"
    "v_mul_f32_e64 v100, v100, v80\n"
    "v_mul_f32 v101, v101, v80\n"
    "v_mfma_f32_16x16x32_bf16 v[220:223], a[156:159], v[36:39], v[220:223]\n"
    "s_lshl_b32 s68, s68, 1\n"
    "v_mul_f32_e64 v102, v102, v80\n"
    "v_mul_f32_e64 v103, v103, v80\n"
    "v_mfma_f32_16x16x32_bf16 a[248:251], a[152:155], v[40:43], a[248:251]\n"
    "v_cvt_pk_bf16_f32 v96, v96, v97\n"
    "v_cvt_pk_bf16_f32 v97, v98, v99\n"
    "v_mfma_f32_16x16x32_bf16 a[252:255], a[156:159], v[40:43], a[252:255]\n"
    "s_cmp_eq_u32 s66, 4\n"
    "v_cvt_pk_bf16_f32 v98, v100, v101\n"
    "v_cvt_pk_bf16_f32 v99, v102, v103\n"
    "s_cbranch_scc1 LEND3_%=\n"
    "ds_read_b128 a[128:131], v88 offset:0\n"
    "ds_read_b128 a[132:135], v89 offset:0\n"
    "ds_read_b128 a[136:139], v88 offset:1024\n"
    "ds_read_b128 a[140:143], v89 offset:1024\n"
    "ds_read_b128 a[144:147], v88 offset:2048\n"
    "ds_read_b128 a[148:151], v89 offset:2048\n"
    "ds_read_b128 a[152:155], v88 offset:3072\n"
    "ds_read_b128 a[156:159], v89 offset:3072\n"
    "s_cmp_lt_u32 s69, 1\n"
    "s_cbranch_scc1 LXS3_%=\n"
    "s_nop 3\n"
    "s_cmp_lt_u32 s69, 2\n"
    "s_cbranch_scc1 LXS3_%=\n"
    "s_nop 3\n"
    "s_cmp_lt_u32 s69, 3\n"
    "s_cbranch_scc1 LXS3_%=\n"
    "s_nop 3\n"
    "s_nop 0\n"
    "LXS3_%=:\n"
    "v_pk_mul_f32 v[160:161], v[160:161], v[80:81] op_sel:[0,1] op_sel_hi:[1,1]\n"
    "v_pk_mul_f32 v[162:163], v[162:163], v[80:81] op_sel:[0,1] op_sel_hi:[1,1]\n"
    "v_pk_mul_f32 v[164:165], v[164:165], v[80:81] op_sel:[0,1] op_sel_hi:[1,1]\n"
    "v_pk_mul_f32 v[166:167], v[166:167], v[80:81] op_sel:[0,1] op_sel_hi:[1,1]\n"
    "v_cvt_pk_bf16_f32 v160, v160, v161\n"
    "v_cvt_pk_bf16_f32 v161, v162, v163\n"
    "v_cvt_pk_bf16_f32 v162, v164, v165\n"
    "v_cvt_pk_bf16_f32 v163, v166, v167\n"
    "v_pk_mul_f32 v[104:105], v[104:105], v[80:81] op_sel_hi:[1,0]\n"
    "v_pk_mul_f32 v[106:107], v[106:107], v[80:81] op_sel_hi:[1,0]\n"
    "v_pk_mul_f32 v[108:109], v[108:109], v[80:81] op_sel_hi:[1,0]\n"
    "v_pk_mul_f32 v[110:111], v[110:111], v[80:81] op_sel_hi:[1,0]\n"
    "v_cvt_pk_bf16_f32 v104, v104, v105\n"
    "v_cvt_pk_bf16_f32 v105, v106, v107\n"
    "v_cvt_pk_bf16_f32 v106, v108, v109\n"
    "v_cvt_pk_bf16_f32 v107, v110, v111\n"
    "v_pk_mul_f32 v[168:169], v[168:169], v[80:81] op_sel:[0,1] op_sel_hi:[1,1]\n"
    "v_pk_mul_f32 v[170:171], v[170:171], v[80:81] op_sel:[0,1] op_sel_hi:[1,1]\n"
    "v_pk_mul_f32 v[172:173], v[172:173], v[80:81] op_sel:[0,1] op_sel_hi:[1,1]\n"
    "v_pk_mul_f32 v[174:175], v[174:175], v[80:81] op_sel:[0,1] op_sel_hi:[1,1]\n"
    "v_cvt_pk_bf16_f32 v168, v168, v169\n"
    "v_pk_mul_f32 v[224:225], v[224:225], v[82:83] op_sel_hi:[1,0]\n"
    "v_pk_mul_f32 v[226:227], v[226:227], v[82:83] op_sel_hi:[1,0]\n"
    "v_pk_mul_f32 v[228:229], v[228:229], v[82:83] op_sel_hi:[1,0]\n"
    "v_pk_mul_f32 v[230:231], v[230:231], v[82:83] op_sel_hi:[1,0]\n"
    "v_pk_mul_f32 v[232:233], v[232:233], v[82:83] op_sel_hi:[1,0]\n"
    "v_pk_mul_f32 v[234:235], v[234:235], v[82:83] op_sel_hi:[1,0]\n"
    "v_pk_mul_f32 v[236:237], v[236:237], v[82:83] op_sel_hi:[1,0]\n"
    "v_pk_mul_f32 v[238:239], v[238:239], v[82:83] op_sel_hi:[1,0]\n"
    "v_cvt_pk_bf16_f32 v224, v224, v225\n"
    "v_cvt_pk_bf16_f32 v225, v226, v227\n"
    "v_cvt_pk_bf16_f32 v226, v228, v229\n"
    "v_cvt_pk_bf16_f32 v227, v230, v231\n"
    "v_cvt_pk_bf16_f32 v232, v232, v233\n"
    "v_cvt_pk_bf16_f32 v233, v234, v235\n"
    "v_cvt_pk_bf16_f32 v234, v236, v237\n"
    "v_cvt_pk_bf16_f32 v235, v238, v239\n"
    "ds_write_b128 v16, v[224:227] offset:0\n"
    "ds_write_b128 v17, v[232:235] offset:0\n"
    "v_cvt_pk_bf16_f32 v169, v170, v171\n"
    "v_cvt_pk_bf16_f32 v170, v172, v173\n"
    "v_cvt_pk_bf16_f32 v171, v174, v175\n"
    "v_pk_mul_f32 v[112:113], v[112:113], v[80:81] op_sel_hi:[1,0]\n"
    "v_pk_mul_f32 v[114:115], v[114:115], v[80:81] op_sel_hi:[1,0]\n"
    "v_pk_mul_f32 v[116:117], v[116:117], v[80:81] op_sel_hi:[1,0]\n"
    "v_pk_mul_f32 v[118:119], v[118:119], v[80:81] op_sel_hi:[1,0]\n"
    "v_cvt_pk_bf16_f32 v112, v112, v113\n"
    "v_cvt_pk_bf16_f32 v113, v114, v115\n"
    "v_cvt_pk_bf16_f32 v114, v116, v117\n"
    "v_cvt_pk_bf16_f32 v115, v118, v119\n"
    "v_pk_mul_f32 v[176:177], v[176:177], v[80:81] op_sel:[0,1] op_sel_hi:[1,1]\n"
    "v_pk_mul_f32 v[178:179], v[178:179], v[80:81] op_sel:[0,1] op_sel_hi:[1,1]\n"
    "v_pk_mul_f32 v[180:181], v[180:181], v[80:81] op_sel:[0,1] op_sel_hi:[1,1]\n"
    "v_pk_mul_f32 v[182:183], v[182:183], v[80:81] op_sel:[0,1] op_sel_hi:[1,1]\n"
    "v_cvt_pk_bf16_f32 v176, v176, v177\n"
    "v_cvt_pk_bf16_f32 v177, v178, v179\n"
    "v_cvt_pk_bf16_f32 v178, v180, v181\n"
    "ds_read_b128 v[64:67], v18 offset:0\n"
    "ds_read_b128 v[68:71], v18 offset:1024\n"
    "v_cvt_pk_bf16_f32 v179, v182, v183\n"
    "v_pk_mul_f32 v[120:121], v[120:121], v[80:81] op_sel_hi:[1,0]\n"
    "v_pk_mul_f32 v[122:123], v[122:123], v[80:81] op_sel_hi:[1,0]\n"
    "v_pk_mul_f32 v[240:241], v[240:241], v[82:83] op_sel_hi:[1,0]\n"
    "v_pk_mul_f32 v[242:243], v[242:243], v[82:83] op_sel_hi:[1,0]\n"
    "v_pk_mul_f32 v[244:245], v[244:245], v[82:83] op_sel_hi:[1,0]\n"
    "v_pk_mul_f32 v[246:247], v[246:247], v[82:83] op_sel_hi:[1,0]\n"
    "v_pk_mul_f32 v[248:249], v[248:249], v[82:83] op_sel_hi:[1,0]\n"
    "v_pk_mul_f32 v[250:251], v[250:251], v[82:83] op_sel_hi:[1,0]\n"
    "v_pk_mul_f32 v[252:253], v[252:253], v[82:83] op_sel_hi:[1,0]\n"
    "v_pk_mul_f32 v[254:255], v[254:255], v[82:83] op_sel_hi:[1,0]\n"
    "v_cvt_pk_bf16_f32 v240, v240, v241\n"
    "v_cvt_pk_bf16_f32 v241, v242, v243\n"
    "v_cvt_pk_bf16_f32 v242, v244, v245\n"
    "v_cvt_pk_bf16_f32 v243, v246, v247\n"
    "v_cvt_pk_bf16_f32 v248, v248, v249\n"
    "v_cvt_pk_bf16_f32 v249, v250, v251\n"
    "v_cvt_pk_bf16_f32 v250, v252, v253\n"
    "v_cvt_pk_bf16_f32 v251, v254, v255\n"
    "ds_write_b128 v16, v[240:243] offset:2048\n"
    "ds_write_b128 v17, v[248:251] offset:2048\n"
    "v_pk_mul_f32 v[124:125], v[124:125], v[80:81] op_sel_hi:[1,0]\n"
    "v_pk_mul_f32 v[126:127], v[126:127], v[80:81] op_sel_hi:[1,0]\n"
    "v_cvt_pk_bf16_f32 v120, v120, v121\n"
    "v_cvt_pk_bf16_f32 v121, v122, v123\n"
    "v_cvt_pk_bf16_f32 v122, v124, v125\n"
    "v_accvgpr_read_b32 v32, a224\n"
    "v_accvgpr_read_b32 v33, a225\n"
    "v_accvgpr_read_b32 v34, a226\n"
    "v_accvgpr_read_b32 v35, a227\n"
    "v_accvgpr_read_b32 v36, a228\n"
    "v_accvgpr_read_b32 v37, a229\n"
    "v_accvgpr_read_b32 v38, a230\n"
    "v_accvgpr_read_b32 v39, a231\n"
    "v_accvgpr_read_b32 v40, a232\n"
    "v_accvgpr_read_b32 v41, a233\n"
    "v_accvgpr_read_b32 v42, a234\n"
    "v_accvgpr_read_b32 v43, a235\n"
    "v_accvgpr_read_b32 v44, a236\n"
    "ds_read_b128 v[72:75], v18 offset:2048\n"
    "ds_read_b128 v[76:79], v18 offset:3072\n"
    "v_accvgpr_read_b32 v45, a237\n"
    "v_accvgpr_read_b32 v46, a238\n"
    "v_accvgpr_read_b32 v47, a239\n"
    "v_pk_mul_f32 v[32:33], v[32:33], v[82:83] op_sel_hi:[1,0]\n"
    "v_pk_mul_f32 v[34:35], v[34:35], v[82:83] op_sel_hi:[1,0]\n"
    "v_pk_mul_f32 v[36:37], v[36:37], v[82:83] op_sel_hi:[1,0]\n"
    "v_pk_mul_f32 v[38:39], v[38:39], v[82:83] op_sel_hi:[1,0]\n"
    "v_pk_mul_f32 v[40:41], v[40:41], v[82:83] op_sel_hi:[1,0]\n"
    "v_pk_mul_f32 v[42:43], v[42:43], v[82:83] op_sel_hi:[1,0]\n"
    "v_pk_mul_f32 v[44:45], v[44:45], v[82:83] op_sel_hi:[1,0]\n"
    "v_pk_mul_f32 v[46:47], v[46:47], v[82:83] op_sel_hi:[1,0]\n"
    "v_cvt_pk_bf16_f32 v32, v32, v33\n"
    "v_cvt_pk_bf16_f32 v33, v34, v35\n"
    "v_cvt_pk_bf16_f32 v34, v36, v37\n"
    "v_cvt_pk_bf16_f32 v35, v38, v39\n"
    "v_cvt_pk_bf16_f32 v40, v40, v41\n"
    "v_cvt_pk_bf16_f32 v41, v42, v43\n"
    "v_cvt_pk_bf16_f32 v42, v44, v45\n"
    "v_cvt_pk_bf16_f32 v43, v46, v47\n"
    "s_waitcnt lgkmcnt(4)\n"
    "s_nop 0\n"
    "s_add_u32 s76, s79, 16384\n"
    "s_add_u32 s77, s79, 20480\n"
    "buffer_store_dwordx4 v[64:67], v19, s[48:51], s76 offen offset:0\n"
    "buffer_store_dwordx4 v[68:71], v19, s[48:51], s77 offen offset:0\n"
    "ds_write_b128 v16, v[32:35] offset:0\n"
    "ds_write_b128 v17, v[40:43] offset:0\n"
    "v_cvt_pk_bf16_f32 v123, v126, v127\n"
    "v_pk_mul_f32 v[184:185], v[184:185], v[80:81] op_sel:[0,1] op_sel_hi:[1,1]\n"
    "v_pk_mul_f32 v[186:187], v[186:187], v[80:81] op_sel:[0,1] op_sel_hi:[1,1]\n"
    "v_pk_mul_f32 v[188:189], v[188:189], v[80:81] op_sel:[0,1] op_sel_hi:[1,1]\n"
    "v_pk_mul_f32 v[190:191], v[190:191], v[80:81] op_sel:[0,1] op_sel_hi:[1,1]\n"
    "v_accvgpr_read_b32 v48, a240\n"
    "v_accvgpr_read_b32 v49, a241\n"
    "v_accvgpr_read_b32 v50, a242\n"
    "v_accvgpr_read_b32 v51, a243\n"
    "v_accvgpr_read_b32 v52, a244\n"
    "v_accvgpr_read_b32 v53, a245\n"
    "v_accvgpr_read_b32 v54, a246\n"
    "v_accvgpr_read_b32 v55, a247\n"
    "v_accvgpr_read_b32 v56, a248\n"
    "v_accvgpr_read_b32 v57, a249\n"
    "v_accvgpr_read_b32 v58, a250\n"
    "v_accvgpr_read_b32 v59, a251\n"
    "v_accvgpr_read_b32 v60, a252\n"
    "ds_read_b128 v[64:67], v18 offset:0\n"
    "ds_read_b128 v[68:71], v18 offset:1024\n"
    "v_accvgpr_read_b32 v61, a253\n"
    "v_accvgpr_read_b32 v62, a254\n"
    "v_accvgpr_read_b32 v63, a255\n"
    "v_pk_mul_f32 v[48:49], v[48:49], v[82:83] op_sel_hi:[1,0]\n"
    "v_pk_mul_f32 v[50:51], v[50:51], v[82:83] op_sel_hi:[1,0]\n"
    "v_pk_mul_f32 v[52:53], v[52:53], v[82:83] op_sel_hi:[1,0]\n"
    "v_pk_mul_f32 v[54:55], v[54:55], v[82:83] op_sel_hi:[1,0]\n"
    "v_pk_mul_f32 v[56:57], v[56:57], v[82:83] op_sel_hi:[1,0]\n"
    "v_pk_mul_f32 v[58:59], v[58:59], v[82:83] op_sel_hi:[1,0]\n"
    "v_pk_mul_f32 v[60:61], v[60:61], v[82:83] op_sel_hi:[1,0]\n"
    "v_pk_mul_f32 v[62:63], v[62:63], v[82:83] op_sel_hi:[1,0]\n"
    "v_cvt_pk_bf16_f32 v48, v48, v49\n"
    "v_cvt_pk_bf16_f32 v49, v50, v51\n"
    "v_cvt_pk_bf16_f32 v50, v52, v53\n"
    "v_cvt_pk_bf16_f32 v51, v54, v55\n"
    "v_cvt_pk_bf16_f32 v56, v56, v57\n"
    "v_cvt_pk_bf16_f32 v57, v58, v59\n"
    "v_cvt_pk_bf16_f32 v58, v60, v61\n"
    "v_cvt_pk_bf16_f32 v59, v62, v63\n"
    "s_waitcnt lgkmcnt(4)\n"
    "buffer_store_dwordx4 v[72:75], v19, s[48:51], s76 offen offset:128\n"
    "buffer_store_dwordx4 v[76:79], v19, s[48:51], s77 offen offset:128\n"
    "s_nop 0\n"
    "ds_write_b128 v16, v[48:51] offset:2048\n"
    "ds_write_b128 v17, v[56:59] offset:2048\n"
    "v_cvt_pk_bf16_f32 v184, v184, v185\n"
    "v_cvt_pk_bf16_f32 v185, v186, v187\n"
    "v_cvt_pk_bf16_f32 v186, v188, v189\n"
    "v_cvt_pk_bf16_f32 v187, v190, v191\n"
    "v_pk_mul_f32 v[128:129], v[128:129], v[80:81] op_sel_hi:[1,0]\n"
    "v_pk_mul_f32 v[130:131], v[130:131], v[80:81] op_sel_hi:[1,0]\n"
    "v_pk_mul_f32 v[132:133], v[132:133], v[80:81] op_sel_hi:[1,0]\n"
    "v_pk_mul_f32 v[134:135], v[134:135], v[80:81] op_sel_hi:[1,0]\n"
    "v_cvt_pk_bf16_f32 v128, v128, v129\n"
    "v_cvt_pk_bf16_f32 v129, v130, v131\n"
    "v_cvt_pk_bf16_f32 v130, v132, v133\n"
    "v_cvt_pk_bf16_f32 v131, v134, v135\n"
    "v_pk_mul_f32 v[192:193], v[192:193], v[80:81] op_sel:[0,1] op_sel_hi:[1,1]\n"
    "v_pk_mul_f32 v[194:195], v[194:195], v[80:81] op_sel:[0,1] op_sel_hi:[1,1]\n"
    "v_pk_mul_f32 v[196:197], v[196:197], v[80:81] op_sel:[0,1] op_sel_hi:[1,1]\n"
    "v_pk_mul_f32 v[198:199], v[198:199], v[80:81] op_sel:[0,1] op_sel_hi:[1,1]\n"
    "v_cvt_pk_bf16_f32 v192, v192, v193\n"
    "v_cvt_pk_bf16_f32 v193, v194, v195\n"
    "ds_read_b128 v[72:75], v18 offset:2048\n"
    "ds_read_b128 v[76:79], v18 offset:3072\n"
    "v_cvt_pk_bf16_f32 v194, v196, v197\n"
    "v_cvt_pk_bf16_f32 v195, v198, v199\n"
    "v_pk_mul_f32 v[136:137], v[136:137], v[80:81] op_sel_hi:[1,0]\n"
    "v_pk_mul_f32 v[138:139], v[138:139], v[80:81] op_sel_hi:[1,0]\n"
    "v_pk_mul_f32 v[140:141], v[140:141], v[80:81] op_sel_hi:[1,0]\n"
    "v_pk_mul_f32 v[142:143], v[142:143], v[80:81] op_sel_hi:[1,0]\n"
    "v_cvt_pk_bf16_f32 v136, v136, v137\n"
    "v_cvt_pk_bf16_f32 v137, v138, v139\n"
    "v_cvt_pk_bf16_f32 v138, v140, v141\n"
    "v_cvt_pk_bf16_f32 v139, v142, v143\n"
    "v_pk_mul_f32 v[200:201], v[200:201], v[80:81] op_sel:[0,1] op_sel_hi:[1,1]\n"
    "v_pk_mul_f32 v[202:203], v[202:203], v[80:81] op_sel:[0,1] op_sel_hi:[1,1]\n"
    "v_pk_mul_f32 v[204:205], v[204:205], v[80:81] op_sel:[0,1] op_sel_hi:[1,1]\n"
    "v_pk_mul_f32 v[206:207], v[206:207], v[80:81] op_sel:[0,1] op_sel_hi:[1,1]\n"
    "v_cvt_pk_bf16_f32 v200, v200, v201\n"
    "v_cvt_pk_bf16_f32 v201, v202, v203\n"
    "v_cvt_pk_bf16_f32 v202, v204, v205\n"
    "v_cvt_pk_bf16_f32 v203, v206, v207\n"
    "v_pk_mul_f32 v[144:145], v[144:145], v[80:81] op_sel_hi:[1,0]\n"
    "s_waitcnt lgkmcnt(4)\n"
    "buffer_store_dwordx4 v[64:67], v19, s[48:51], s76 offen offset:256\n"
    "buffer_store_dwordx4 v[68:71], v19, s[48:51], s77 offen offset:256\n"
    "s_nop 0\n"
    "v_pk_mul_f32 v[146:147], v[146:147], v[80:81] op_sel_hi:[1,0]\n"
    "v_pk_mul_f32 v[148:149], v[148:149], v[80:81] op_sel_hi:[1,0]\n"
    "v_pk_mul_f32 v[150:151], v[150:151], v[80:81] op_sel_hi:[1,0]\n"
    "v_cvt_pk_bf16_f32 v144, v144, v145\n"
    "v_cvt_pk_bf16_f32 v145, v146, v147\n"
    "v_cvt_pk_bf16_f32 v146, v148, v149\n"
    "v_cvt_pk_bf16_f32 v147, v150, v151\n"
    "v_pk_mul_f32 v[208:209], v[208:209], v[80:81] op_sel:[0,1] op_sel_hi:[1,1]\n"
    "v_pk_mul_f32 v[210:211], v[210:211], v[80:81] op_sel:[0,1] op_sel_hi:[1,1]\n"
    "v_pk_mul_f32 v[212:213], v[212:213], v[80:81] op_sel:[0,1] op_sel_hi:[1,1]\n"
    "v_pk_mul_f32 v[214:215], v[214:215], v[80:81] op_sel:[0,1] op_sel_hi:[1,1]\n"
    "v_cvt_pk_bf16_f32 v208, v208, v209\n"
    "v_cvt_pk_bf16_f32 v209, v210, v211\n"
    "v_cvt_pk_bf16_f32 v210, v212, v213\n"
    "v_cvt_pk_bf16_f32 v211, v214, v215\n"
    "s_waitcnt vmcnt(6)\n"
    "s_nop 0\n"
    "ds_read_b128 a[64:67], v94 offset:0\n"
    "ds_read_b128 a[68:71], v95 offset:0\n"
    "ds_read_b128 a[96:99], v94 offset:8192\n"
    "ds_read_b128 a[100:103], v95 offset:8192\n"
    "ds_read_b128 a[72:75], v94 offset:1024\n"
    "ds_read_b128 a[76:79], v95 offset:1024\n"
    "ds_read_b128 a[104:107], v94 offset:9216\n"
    "ds_read_b128 a[108:111], v95 offset:9216\n"
    "ds_read_b128 a[80:83], v94 offset:2048\n"
    "ds_read_b128 a[84:87], v95 offset:2048\n"
    "ds_read_b128 a[112:115], v94 offset:10240\n"
    "ds_read_b128 a[116:119], v95 offset:10240\n"
    "ds_read_b128 a[88:91], v94 offset:3072\n"
    "ds_read_b128 a[92:95], v95 offset:3072\n"
    "ds_read_b128 a[120:123], v94 offset:11264\n"
    "ds_read_b128 a[124:127], v95 offset:11264\n"
    "v_pk_mul_f32 v[152:153], v[152:153], v[80:81] op_sel_hi:[1,0]\n"
    "v_pk_mul_f32 v[154:155], v[154:155], v[80:81] op_sel_hi:[1,0]\n"
    "v_pk_mul_f32 v[156:157], v[156:157], v[80:81] op_sel_hi:[1,0]\n"
    "v_pk_mul_f32 v[158:159], v[158:159], v[80:81] op_sel_hi:[1,0]\n"
    "v_cvt_pk_bf16_f32 v152, v152, v153\n"
    "v_cvt_pk_bf16_f32 v153, v154, v155\n"
    "v_cvt_pk_bf16_f32 v154, v156, v157\n"
    "v_cvt_pk_bf16_f32 v155, v158, v159\n"
    "v_pk_mul_f32 v[216:217], v[216:217], v[80:81] op_sel:[0,1] op_sel_hi:[1,1]\n"
    "v_pk_mul_f32 v[218:219], v[218:219], v[80:81] op_sel:[0,1] op_sel_hi:[1,1]\n"
    "v_pk_mul_f32 v[220:221], v[220:221], v[80:81] op_sel:[0,1] op_sel_hi:[1,1]\n"
    "v_pk_mul_f32 v[222:223], v[222:223], v[80:81] op_sel:[0,1] op_sel_hi:[1,1]\n"
    "v_cvt_pk_bf16_f32 v216, v216, v217\n"
    "v_cvt_pk_bf16_f32 v217, v218, v219\n"
    "v_cvt_pk_bf16_f32 v218, v220, v221\n"
    "v_cvt_pk_bf16_f32 v219, v222, v223\n"
    "s_waitcnt lgkmcnt(15)\n"
    "buffer_store_dwordx4 v[72:75], v19, s[48:51], s76 offen offset:384\n"
    "buffer_store_dwordx4 v[76:79], v19, s[48:51], s77 offen offset:384\n"
    "s_branch LSP4_%=\n"
    ".p2align 5\n"
    "GLSP3f_%=:\n"
    "s_mov_b32 s57, s78\n"
    "s_mov_b32 s58, 0x100\n"
    "s_mov_b32 s59, s70\n"
    "s_mov_b32 s98, s78\n"
    "s_mov_b32 s99, 0x100\n"
    "s_mov_b32 s65, 5\n"
    "s_mov_b32 m0, s61\n"
    "v_add_u32 v93, 4096, v93\n"
    "buffer_load_dwordx4 v92, s[40:43], s55 offen offset:0 lds\n"
    "s_sleep 1\n"
    "s_add_u32 m0, m0, s97\n"
    "s_nop 0\n"
    "buffer_load_dwordx4 v92, s[40:43], s55 offen offset:128 lds\n"
    "s_sleep 1\n"
    "s_add_u32 m0, m0, s97\n"
    "s_nop 0\n"
    "buffer_load_dwordx4 v92, s[40:43], s55 offen offset:256 lds\n"
    "s_sleep 1\n"
    "s_add_u32 m0, m0, s97\n"
    "s_nop 0\n"
    "buffer_load_dwordx4 v92, s[40:43], s55 offen offset:384 lds\n"
    "s_sleep 1\n"
    "s_add_u32 m0, m0, 1408\n"
    "s_nop 0\n"
    "buffer_load_dwordx4 v92, s[40:43], s56 offen offset:0 lds\n"
    "s_sleep 1\n"
    "s_add_u32 m0, m0, s97\n"
    "s_nop 0\n"
    "buffer_load_dwordx4 v92, s[40:43], s56 offen offset:128 lds\n"
    "s_sleep 1\n"
    "s_add_u32 m0, m0, s97\n"
    "s_nop 0\n"
    "buffer_load_dwordx4 v92, s[40:43], s56 offen offset:256 lds\n"
    "s_sleep 1\n"
    "s_add_u32 m0, m0, 896\n"
    "s_nop 0\n"
    "buffer_load_dwordx4 v92, s[40:43], s56 offen offset:384 lds\n"
    "s_sleep 1\n"
    "s_add_u32 s55, s60, s96\n"
    "s_add_u32 s56, s60, 0x9000\n"
    "s_add_u32 m0, s60, s96\n"
    "s_nop 0\n"
    "buffer_load_dwordx4 v92, s[36:39], s53 offen offset:0 lds\n"
    "s_sleep 1\n"
    "s_add_u32 m0, m0, s97\n"
    "s_nop 0\n"
    "buffer_load_dwordx4 v92, s[36:39], s53 offen offset:128 lds\n"
    "s_sleep 1\n"
    "s_add_u32 m0, m0, s97\n"
    "s_nop 0\n"
    "buffer_load_dwordx4 v92, s[36:39], s53 offen offset:256 lds\n"
    "s_sleep 1\n"
    "s_add_u32 m0, m0, 896\n"
    "s_nop 0\n"
    "buffer_load_dwordx4 v92, s[36:39], s53 offen offset:384 lds\n"
    "s_sleep 1\n"
    "s_add_u32 m0, m0, 1408\n"
    "s_nop 0\n"
    "buffer_load_dwordx4 v92, s[36:39], s54 offen offset:0 lds\n"
    "s_sleep 1\n"
    "s_add_u32 m0, m0, s97\n"
    "s_nop 0\n"
    "buffer_load_dwordx4 v92, s[36:39], s54 offen offset:128 lds\n"
    "s_sleep 1\n"
    "s_add_u32 m0, m0, s97\n"
    "s_nop 0\n"
    "buffer_load_dwordx4 v92, s[36:39], s54 offen offset:256 lds\n"
    "s_sleep 1\n"
    "s_add_u32 m0, m0, s97\n"
    "s_nop 0\n"
    "buffer_load_dwordx4 v92, s[36:39], s54 offen offset:384 lds\n"
    "s_sleep 1\n"
    "s_add_u32 s53, s53, s96\n"
    "s_add_u32 s54, s54, s96\n"
    "s_waitcnt vmcnt(8) lgkmcnt(0)\n"
    "s_barrier\n"
    "s_mov_b32 m0, s60\n"
    "s_nop 0\n"
    "buffer_load_dwordx4 v92, s[40:43], s55 offen offset:0 lds\n"
    "s_sleep 1\n"
    "s_add_u32 m0, m0, s97\n"
    "s_nop 0\n"
    "buffer_load_dwordx4 v92, s[40:43], s55 offen offset:128 lds\n"
    "s_sleep 1\n"
    "s_add_u32 m0, m0, s97\n"
    "s_nop 0\n"
    "buffer_load_dwordx4 v92, s[40:43], s55 offen offset:256 lds\n"
    "s_sleep 1\n"
    "s_add_u32 m0, m0, s97\n"
    "s_nop 0\n"
    "buffer_load_dwordx4 v92, s[40:43], s55 offen offset:384 lds\n"
    "s_sleep 1\n"
    "s_add_u32 m0, m0, 1408\n"
    "s_nop 0\n"
    "buffer_load_dwordx4 v92, s[40:43], s56 offen offset:0 lds\n"
    "s_sleep 1\n"
    "s_add_u32 m0, m0, s97\n"
    "s_nop 0\n"
    "buffer_load_dwordx4 v92, s[40:43], s56 offen offset:128 lds\n"
    "s_sleep 1\n"
    "s_add_u32 m0, m0, s97\n"
    "s_nop 0\n"
    "buffer_load_dwordx4 v92, s[40:43], s56 offen offset:256 lds\n"
    "s_sleep 1\n"
    "s_add_u32 m0, m0, s97\n"
    "s_nop 0\n"
    "buffer_load_dwordx4 v92, s[40:43], s56 offen offset:384 lds\n"
    "s_sleep 1\n"
    "s_add_u32 s55, s55, s96\n"
    "v_add_u32 v88, s62, v88\n"
    "v_add_u32 v89, s62, v89\n"
    "s_add_u32 s56, s56, 0x8000\n"
    "s_mov_b32 m0, s59\n"
    "s_waitcnt vmcnt(8) lgkmcnt(0)\n"
    "buffer_load_dwordx4 v92, s[44:47], s57 offen offset:0 nt lds\n"
    "s_sleep 1\n"
    "s_barrier\n"
    "s_add_u32 m0, m0, s97\n"
    "s_nop 0\n"
    "buffer_load_dwordx4 v92, s[44:47], s57 offen offset:128 nt lds\n"
    "s_sleep 1\n"
    "s_add_u32 s57, s57, s58\n"
    "s_xor_b32 s58, s58, 0xe00\n"
    "s_add_u32 s59, s59, 0x800\n"
    "s_branch GLQ3_%=\n"
    ".p2align 5\n"
    "GLSP3_%=:\n"
    "ds_write_b128 v94, v[96:99] offset:0\n"
    "ds_write_b128 v95, v[104:107] offset:0\n"
    "ds_write_b128 v94, v[112:115] offset:1024\n"
    "ds_write_b128 v95, v[120:123] offset:1024\n"
    "ds_write_b128 v94, v[128:131] offset:2048\n"
    "ds_write_b128 v95, v[136:139] offset:2048\n"
    "ds_write_b128 v94, v[144:147] offset:3072\n"
    "ds_write_b128 v95, v[152:155] offset:3072\n"
    "ds_read_b128 v[48:51], v93 offset:0\n"
    "ds_read_b128 v[52:55], v93 offset:1024\n"
    "ds_write_b128 v94, v[160:163] offset:8192\n"
    "ds_read_b128 v[56:59], v93 offset:2048\n"
    "ds_read_b128 v[60:63], v93 offset:3072\n"
    "ds_write_b128 v95, v[168:171] offset:8192\n"
    "ds_write_b128 v94, v[176:179] offset:9216\n"
    "s_waitcnt lgkmcnt(2)\n"
    "s_nop 0\n"
    "buffer_store_dwordx4 v[48:51], v92, s[48:51], s98 offen offset:0 nt\n"
    "ds_write_b128 v95, v[184:187] offset:9216\n"
    "buffer_store_dwordx4 v[52:55], v92, s[48:51], s98 offen offset:128 nt\n"
    "ds_write_b128 v94, v[192:195] offset:10240\n"
    "buffer_store_dwordx4 v[56:59], v92, s[48:51], s98 offen offset:256 nt\n"
    "ds_write_b128 v95, v[200:203] offset:10240\n"
    "buffer_store_dwordx4 v[60:63], v92, s[48:51], s98 offen offset:384 nt\n"
    "s_add_u32 s98, s98, 0x1000\n"
    "s_mov_b32 m0, s63\n"
    "ds_write_b128 v94, v[208:211] offset:11264\n"
    "buffer_load_dwordx4 v92, s[36:39], s53 offen offset:0 lds\n"
    "s_sleep 1\n"
    "s_add_u32 m0, m0, 896\n"
    "ds_write_b128 v95, v[216:219] offset:11264\n"
    "buffer_load_dwordx4 v92, s[36:39], s53 offen offset:128 lds\n"
    "s_sleep 1\n"
    "s_add_u32 m0, m0, 896\n"
    "s_nop 0\n"
    "buffer_load_dwordx4 v92, s[36:39], s53 offen offset:256 lds\n"
    "s_sleep 1\n"
    "s_add_u32 m0, m0, s97\n"
    "s_nop 0\n"
    "buffer_load_dwordx4 v92, s[36:39], s53 offen offset:384 lds\n"
    "s_sleep 1\n"
    "s_add_u32 m0, m0, 1408\n"
    "s_nop 0\n"
    "buffer_load_dwordx4 v92, s[36:39], s54 offen offset:0 lds\n"
    "s_sleep 1\n"
    "s_add_u32 m0, m0, s97\n"
    "s_nop 0\n"
    "buffer_load_dwordx4 v92, s[36:39], s54 offen offset:128 lds\n"
    "s_sleep 1\n"
    "s_add_u32 m0, m0, s97\n"
    "s_nop 0\n"
    "buffer_load_dwordx4 v92, s[36:39], s54 offen offset:256 lds\n"
    "s_sleep 1\n"
    "s_add_u32 m0, m0, s97\n"
    "s_nop 0\n"
    "buffer_load_dwordx4 v92, s[36:39], s54 offen offset:384 lds\n"
    "s_sleep 1\n"
    "s_add_u32 s53, s53, s96\n"
    "s_add_u32 s54, s54, s96\n"
    "s_waitcnt lgkmcnt(0)\n"
    "s_barrier\n"
    "s_mov_b32 m0, s60\n"
    "s_nop 0\n"
    "buffer_load_dwordx4 v92, s[40:43], s55 offen offset:0 lds\n"
    "s_sleep 1\n"
    "s_add_u32 m0, m0, s97\n"
    "s_nop 0\n"
    "buffer_load_dwordx4 v92, s[40:43], s55 offen offset:128 lds\n"
    "s_sleep 1\n"
    "s_add_u32 m0, m0, s97\n"
    "s_nop 0\n"
    "buffer_load_dwordx4 v92, s[40:43], s55 offen offset:256 lds\n"
    "s_sleep 1\n"
    "s_add_u32 m0, m0, s97\n"
    "s_nop 0\n"
    "buffer_load_dwordx4 v92, s[40:43], s55 offen offset:384 lds\n"
    "s_sleep 1\n"
    "s_add_u32 m0, m0, 1408\n"
    "s_nop 0\n"
    "buffer_load_dwordx4 v92, s[40:43], s56 offen offset:0 lds\n"
    "s_sleep 1\n"
    "s_add_u32 m0, m0, s97\n"
    "s_nop 0\n"
    "buffer_load_dwordx4 v92, s[40:43], s56 offen offset:128 lds\n"
    "s_sleep 1\n"
    "s_add_u32 m0, m0, s97\n"
    "s_nop 0\n"
    "buffer_load_dwordx4 v92, s[40:43], s56 offen offset:256 lds\n"
    "s_sleep 1\n"
    "s_add_u32 m0, m0, 896\n"
    "s_nop 0\n"
    "buffer_load_dwordx4 v92, s[40:43], s56 offen offset:384 lds\n"
    "s_sleep 1\n"
    "s_add_u32 s55, s55, s96\n"
    "v_add_u32 v88, s62, v88\n"
    "v_add_u32 v89, s62, v89\n"
    "s_add_u32 s56, s56, s96\n"
    "s_mov_b32 m0, s59\n"
    "s_waitcnt vmcnt(8) lgkmcnt(0)\n"
    "buffer_load_dwordx4 v92, s[44:47], s57 offen offset:0 nt lds\n"
    "s_sleep 1\n"
    "s_barrier\n"
    "s_add_u32 m0, m0, 896\n"
    "s_nop 0\n"
    "buffer_load_dwordx4 v92, s[44:47], s57 offen offset:128 nt lds\n"
    "s_sleep 1\n"
    "s_add_u32 s57, s57, s58\n"
    "s_xor_b32 s58, s58, 0xe00\n"
    "s_add_u32 s59, s59, 0x800\n"
    "v_add_u32 v93, 4096, v93\n"
    "GLQ3_%=:\n"
    "s_mov_b32 m0, s61\n"
    "s_nop 0\n"
    "buffer_load_dwordx4 v92, s[36:39], s53 offen offset:0 lds\n"
    "s_sleep 1\n"
    "s_add_u32 m0, m0, s97\n"
    "s_sub_u32 s64, s60, s61\n"
    "s_mov_b32 s71, s61\n"
    "v_add_u32 v90, s64, v90\n"
    "v_add_u32 v91, s64, v91\n"
    "buffer_load_dwordx4 v92, s[36:39], s53 offen offset:128 lds\n"
    "s_sleep 1\n"
    "s_add_u32 m0, m0, s97\n"
    "s_mov_b32 s61, s60\n"
    "buffer_load_dwordx4 v92, s[36:39], s53 offen offset:256 lds\n"
    "s_sleep 1\n"
    "s_add_u32 m0, m0, s97\n"
    "s_nop 0\n"
    "buffer_load_dwordx4 v92, s[36:39], s53 offen offset:384 lds\n"
    "s_sleep 1\n"
    "s_add_u32 m0, m0, 1408\n"
    "s_mov_b32 s60, s63\n"
    "buffer_load_dwordx4 v92, s[36:39], s54 offen offset:0 lds\n"
    "s_sleep 1\n"
    "s_add_u32 m0, m0, s97\n"
    "s_nop 0\n"
    "buffer_load_dwordx4 v92, s[36:39], s54 offen offset:128 lds\n"
    "s_sleep 1\n"
    "s_add_u32 m0, m0, s97\n"
    "s_mov_b32 s63, s71\n"
    "buffer_load_dwordx4 v92, s[36:39], s54 offen offset:256 lds\n"
    "s_sleep 1\n"
    "s_add_u32 m0, m0, s97\n"
    "s_nop 0\n"
    "buffer_load_dwordx4 v92, s[36:39], s54 offen offset:384 lds\n"
    "s_sleep 1\n"
    "s_add_u32 s53, s53, s96\n"
    "s_add_u32 s54, s54, s96\n"
    "s_sub_u32 s62, s63, s60\n"
    "s_mov_b32 m0, s59\n"
    "s_nop 0\n"
    "buffer_load_dwordx4 v92, s[44:47], s57 offen offset:0 nt lds\n"
    "s_sleep 1\n"
    "s_add_u32 m0, m0, s97\n"
    "s_nop 0\n"
    "buffer_load_dwordx4 v92, s[44:47], s57 offen offset:128 nt lds\n"
    "s_sleep 1\n"
    "s_add_u32 s57, s57, s58\n"
    "s_xor_b32 s58, s58, 0xe00\n"
    "s_add_u32 s59, s59, 0x800\n"
    "s_waitcnt vmcnt(12) lgkmcnt(0)\n"
    "s_barrier\n"
    "s_mov_b32 m0, s60\n"
    "s_nop 0\n"
    "buffer_load_dwordx4 v92, s[40:43], s55 offen offset:0 lds\n"
    "s_sleep 1\n"
    "s_add_u32 m0, m0, s97\n"
    "s_nop 0\n"
    "buffer_load_dwordx4 v92, s[40:43], s55 offen offset:128 lds\n"
    "s_sleep 1\n"
    "s_add_u32 m0, m0, s97\n"
    "s_nop 0\n"
    "buffer_load_dwordx4 v92, s[40:43], s55 offen offset:256 lds\n"
    "s_sleep 1\n"
    "s_add_u32 m0, m0, s97\n"
    "s_nop 0\n"
    "buffer_load_dwordx4 v92, s[40:43], s55 offen offset:384 lds\n"
    "s_sleep 1\n"
    "s_add_u32 m0, m0, 1408\n"
    "s_nop 0\n"
    "buffer_load_dwordx4 v92, s[40:43], s56 offen offset:0 lds\n"
    "s_sleep 1\n"
    "s_add_u32 m0, m0, s97\n"
    "s_nop 0\n"
    "buffer_load_dwordx4 v92, s[40:43], s56 offen offset:128 lds\n"
    "s_sleep 1\n"
    "s_add_u32 m0, m0, s97\n"
    "s_nop 0\n"
    "buffer_load_dwordx4 v92, s[40:43], s56 offen offset:256 lds\n"
    "s_sleep 1\n"
    "s_add_u32 m0, m0, s97\n"
    "v_add_u32 v88, s62, v88\n"
    "v_add_u32 v89, s62, v89\n"
    "buffer_load_dwordx4 v92, s[40:43], s56 offen offset:384 lds\n"
    "s_sleep 1\n"
    "s_add_u32 s55, s55, 0x8000\n"
    "ds_read_b128 v[16:19], v93 offset:0\n"
    "ds_read_b128 v[20:23], v93 offset:1024\n"
    "s_add_u32 s56, s56, s96\n"
    "s_waitcnt lgkmcnt(0)\n"
    "buffer_store_dwordx4 v[16:19], v92, s[48:51], s98 offen offset:0 nt\n"
    "buffer_store_dwordx4 v[20:23], v92, s[48:51], s98 offen offset:128 nt\n"
    "s_add_u32 s98, s98, s99\n"
    "s_xor_b32 s99, s99, 0xe00\n"
    "s_waitcnt vmcnt(12) lgkmcnt(0)\n"
    "s_barrier\n"
    "v_add_u32 v93, 2048, v93\n"
    "s_sub_u32 s65, s65, 1\n"
    "s_cbranch_scc0 GLQ3_%=\n"
    "s_mov_b32 m0, s61\n"
    "s_nop 0\n"
    "buffer_load_dwordx4 v92, s[40:43], s55 offen offset:0 lds\n"
    "s_sleep 1\n"
    "s_add_u32 m0, m0, s97\n"
    "s_sub_u32 s64, s60, s61\n"
    "s_mov_b32 s71, s61\n"
    "v_add_u32 v90, s64, v90\n"
    "v_add_u32 v91, s64, v91\n"
    "buffer_load_dwordx4 v92, s[40:43], s55 offen offset:128 lds\n"
    "s_sleep 1\n"
    "s_add_u32 m0, m0, s97\n"
    "s_mov_b32 s61, s60\n"
    "buffer_load_dwordx4 v92, s[40:43], s55 offen offset:256 lds\n"
    "s_sleep 1\n"
    "s_add_u32 m0, m0, s97\n"
    "s_nop 0\n"
    "buffer_load_dwordx4 v92, s[40:43], s55 offen offset:384 lds\n"
    "s_sleep 1\n"
    "s_add_u32 m0, m0, 1408\n"
    "v_add_u32 v93, 0xffffc000, v93\n"
    "s_mov_b32 s60, s63\n"
    "buffer_load_dwordx4 v92, s[40:43], s56 offen offset:0 lds\n"
    "s_sleep 1\n"
    "s_add_u32 m0, m0, s97\n"
    "s_nop 0\n"
    "buffer_load_dwordx4 v92, s[40:43], s56 offen offset:128 lds\n"
    "s_sleep 1\n"
    "s_add_u32 m0, m0, 896\n"
    "s_mov_b32 s63, s71\n"
    "buffer_load_dwordx4 v92, s[40:43], s56 offen offset:256 lds\n"
    "s_sleep 1\n"
    "s_add_u32 m0, m0, s97\n"
    "s_nop 0\n"
    "buffer_load_dwordx4 v92, s[40:43], s56 offen offset:384 lds\n"
    "s_sleep 1\n"
    "s_add_u32 s55, s55, s96\n"
    "s_add_u32 s56, s56, s96\n"
    "s_sub_u32 s62, s63, s60\n"
    "s_mov_b32 m0, s59\n"
    "s_nop 0\n"
    "buffer_load_dwordx4 v92, s[44:47], s57 offen offset:0 nt lds\n"
    "s_sleep 1\n"
    "s_add_u32 m0, m0, 896\n"
    "s_mov_b32 s50, 434176\n"
    "buffer_load_dwordx4 v92, s[44:47], s57 offen offset:128 nt lds\n"
    "s_sleep 1\n"
    "s_add_u32 s57, s57, s58\n"
    "s_xor_b32 s58, s58, 0xe00\n"
    "s_add_u32 s59, s59, 0x800\n"
    "s_add_u32 s48, s48, 434176\n"
    "s_addc_u32 s49, s49, 0\n"
    "s_add_u32 s66, s66, 1\n"
    "s_waitcnt vmcnt(12) lgkmcnt(0)\n"
    "s_barrier\n"
    "s_mov_b32 m0, s60\n"
    "s_sub_u32 s64, s63, s61\n"
    "buffer_load_dwordx4 v92, s[36:39], s53 offen offset:0 lds\n"
    "s_sleep 1\n"
    "s_add_u32 m0, m0, s97\n"
    "s_lshl_b32 s72, s69, 12\n"
    "buffer_load_dwordx4 v92, s[36:39], s53 offen offset:128 lds\n"
    "s_sleep 1\n"
    "s_add_u32 m0, m0, s97\n"
    "s_sub_u32 s72, s63, s72\n"
    "s_mov_b32 s71, s61\n"
    "s_mov_b32 s61, s63\n"
    "buffer_load_dwordx4 v92, s[36:39], s53 offen offset:256 lds\n"
    "s_sleep 1\n"
    "s_add_u32 m0, m0, 896\n"
    "s_mov_b32 s63, s71\n"
    "s_sub_u32 s62, s63, s60\n"
    "s_mov_b32 s57, s79\n"
    "buffer_load_dwordx4 v92, s[36:39], s53 offen offset:384 lds\n"
    "s_sleep 1\n"
    "s_add_u32 m0, m0, 1408\n"
    "s_mov_b32 s58, 0x100\n"
    "s_mov_b32 s59, s70\n"
    "s_mov_b32 s98, s79\n"
    "buffer_load_dwordx4 v92, s[36:39], s54 offen offset:0 lds\n"
    "s_sleep 1\n"
    "s_add_u32 m0, m0, s97\n"
    "s_mov_b32 s99, 0x100\n"
    "s_mov_b32 s65, 5\n"
    "buffer_load_dwordx4 v92, s[36:39], s54 offen offset:128 lds\n"
    "s_sleep 1\n"
    "s_add_u32 m0, m0, s97\n"
    "s_nop 0\n"
    "buffer_load_dwordx4 v92, s[36:39], s54 offen offset:256 lds\n"
    "s_sleep 1\n"
    "s_add_u32 m0, m0, 896\n"
    "s_nop 0\n"
    "buffer_load_dwordx4 v92, s[36:39], s54 offen offset:384 lds\n"
    "s_sleep 1\n"
    "s_add_u32 s53, s53, 0x8000\n"
    "s_add_u32 s54, s54, s96\n"
    "s_waitcnt vmcnt(8)\n"
    "ds_read_b128 a[0:3], v94 offset:0\n"
    "ds_read_b128 a[4:7], v95 offset:0\n"
    "ds_read_b128 a[8:11], v94 offset:1024\n"
    "ds_read_b128 a[12:15], v95 offset:1024\n"
    "ds_read_b128 a[16:19], v94 offset:2048\n"
    "ds_read_b128 a[20:23], v95 offset:2048\n"
    "ds_read_b128 a[24:27], v94 offset:3072\n"
    "ds_read_b128 a[28:31], v95 offset:3072\n"
    "ds_read_b128 a[32:35], v94 offset:8192\n"
    "ds_read_b128 a[36:39], v95 offset:8192\n"
    "ds_read_b128 a[40:43], v94 offset:9216\n"
    "ds_read_b128 a[44:47], v95 offset:9216\n"
    "ds_read_b128 a[48:51], v94 offset:10240\n"
    "ds_read_b128 a[52:55], v95 offset:10240\n"
    "ds_read_b128 a[56:59], v94 offset:11264\n"
    "ds_read_b128 a[60:63], v95 offset:11264\n"
    "s_waitcnt lgkmcnt(0)\n"
    "s_add_u32 s73, s78, 16384\n"
    "s_add_u32 s74, s78, 20480\n"
    "s_add_u32 m0, s70, 0\n"
    "s_nop 0\n"
    "buffer_load_dwordx4 v92, s[44:47], s73 offen offset:0 nt lds\n"
    "s_sleep 1\n"
    "s_add_u32 m0, m0, s97\n"
    "s_nop 0\n"
    "buffer_load_dwordx4 v92, s[44:47], s73 offen offset:128 nt lds\n"
    "s_sleep 1\n"
    "s_add_u32 m0, m0, s97\n"
    "s_nop 0\n"
    "buffer_load_dwordx4 v92, s[44:47], s73 offen offset:256 nt lds\n"
    "s_sleep 1\n"
    "s_add_u32 m0, m0, s97\n"
    "s_nop 0\n"
    "buffer_load_dwordx4 v92, s[44:47], s73 offen offset:384 nt lds\n"
    "s_sleep 1\n"
    "s_add_u32 m0, m0, 1408\n"
    "s_nop 0\n"
    "buffer_load_dwordx4 v92, s[44:47], s74 offen offset:0 nt lds\n"
    "s_sleep 1\n"
    "s_add_u32 m0, m0, s97\n"
    "v_add_u32 v90, s64, v90\n"
    "v_add_u32_e64 v91, s64, v91\n"
    "s_waitcnt vmcnt(15) lgkmcnt(0)\n"
    "buffer_load_dwordx4 v92, s[44:47], s74 offen offset:128 nt lds\n"
    "s_sleep 1\n"
    "s_barrier\n"
    "s_add_u32 m0, m0, 896\n"
    "s_nop 0\n"
    "buffer_load_dwordx4 v92, s[44:47], s74 offen offset:256 nt lds\n"
    "s_sleep 1\n"
    "s_add_u32 m0, m0, 896\n"
    "s_nop 0\n"
    "buffer_load_dwordx4 v92, s[44:47], s74 offen offset:384 nt lds\n"
    "s_sleep 1\n"
    "s_add_u32 s73, s78, 24576\n"
    "s_add_u32 s74, s78, 28672\n"
    "s_add_u32 m0, s70, 8192\n"
    "s_nop 0\n"
    "buffer_load_dwordx4 v92, s[44:47], s73 offen offset:0 nt lds\n"
    "s_sleep 1\n"
    "s_add_u32 m0, m0, s97\n"
    "s_nop 0\n"
    "buffer_load_dwordx4 v92, s[44:47], s73 offen offset:128 nt lds\n"
    "s_sleep 1\n"
    "s_add_u32 m0, m0, 896\n"
    "s_nop 0\n"
    "buffer_load_dwordx4 v92, s[44:47], s73 offen offset:256 nt lds\n"
    "s_sleep 1\n"
    "s_add_u32 m0, m0, s97\n"
    "s_nop 0\n"
    "buffer_load_dwordx4 v92, s[44:47], s73 offen offset:384 nt lds\n"
    "s_sleep 1\n"
    "s_add_u32 m0, m0, 1408\n"
    "s_nop 0\n"
    "buffer_load_dwordx4 v92, s[44:47], s74 offen offset:0 nt lds\n"
    "s_sleep 1\n"
    "s_add_u32 m0, m0, s97\n"
    "s_nop 0\n"
    "buffer_load_dwordx4 v92, s[44:47], s74 offen offset:128 nt lds\n"
    "s_sleep 1\n"
    "s_add_u32 m0, m0, 896\n"
    "s_waitcnt vmcnt(14) lgkmcnt(0)\n"
    "buffer_load_dwordx4 v92, s[44:47], s74 offen offset:256 nt lds\n"
    "s_sleep 1\n"
    "s_barrier\n"
    "s_add_u32 m0, m0, 896\n"
    "s_nop 0\n"
    "buffer_load_dwordx4 v92, s[44:47], s74 offen offset:384 nt lds\n"
    "s_sleep 1\n"
    "s_add_u32 s44, s44, 434176\n"
    "s_addc_u32 s45, s45, 0\n"
    "s_cmp_ge_u32 s66, 3\n"
    "s_cselect_b32 s46, 0, s46\n"
    "s_lshl_b32 s68, s68, 1\n"
    "s_cmp_eq_u32 s66, 4\n"
    "s_cbranch_scc1 LEND_%=\n"
    "ds_read_b128 a[128:131], v88 offset:0\n"
    "ds_read_b128 a[132:135], v89 offset:0\n"
    "ds_read_b128 a[136:139], v88 offset:1024\n"
    "ds_read_b128 a[140:143], v89 offset:1024\n"
    "ds_read_b128 a[144:147], v88 offset:2048\n"
    "ds_read_b128 a[148:151], v89 offset:2048\n"
    "ds_read_b128 a[152:155], v88 offset:3072\n"
    "ds_read_b128 a[156:159], v89 offset:3072\n"
    "s_cmp_lt_u32 s69, 1\n"
    "s_cbranch_scc1 GLXS3_%=\n"
    "s_nop 3\n"
    "s_cmp_lt_u32 s69, 2\n"
    "s_cbranch_scc1 GLXS3_%=\n"
    "s_nop 3\n"
    "s_cmp_lt_u32 s69, 3\n"
    "s_cbranch_scc1 GLXS3_%=\n"
    "s_nop 3\n"
    "GLXS3_%=:\n"
    "s_add_u32 s76, s79, 16384\n"
    "s_add_u32 s77, s79, 20480\n"
    "s_waitcnt vmcnt(0)\n"
    "ds_read_b128 a[64:67], v94 offset:0\n"
    "ds_read_b128 a[68:71], v95 offset:0\n"
    "ds_read_b128 a[96:99], v94 offset:8192\n"
    "ds_read_b128 a[100:103], v95 offset:8192\n"
    "ds_read_b128 a[72:75], v94 offset:1024\n"
    "ds_read_b128 a[76:79], v95 offset:1024\n"
    "ds_read_b128 a[104:107], v94 offset:9216\n"
    "ds_read_b128 a[108:111], v95 offset:9216\n"
    "ds_read_b128 a[80:83], v94 offset:2048\n"
    "ds_read_b128 a[84:87], v95 offset:2048\n"
    "ds_read_b128 a[112:115], v94 offset:10240\n"
    "ds_read_b128 a[116:119], v95 offset:10240\n"
    "ds_read_b128 a[88:91], v94 offset:3072\n"
    "ds_read_b128 a[92:95], v95 offset:3072\n"
    "ds_read_b128 a[120:123], v94 offset:11264\n"
    "ds_read_b128 a[124:127], v95 offset:11264\n"
    "s_branch LSP4_%=\n"
    ".p2align 5\n"
    "LEND4_%=:\n"
    "s_cmp_lt_u32 s69, 1\n"
    "s_cbranch_scc1 LFS4_%=\n"
    "s_nop 3\n"
    "s_cmp_lt_u32 s69, 2\n"
    "s_cbranch_scc1 LFS4_%=\n"
    "s_nop 3\n"
    "s_cmp_lt_u32 s69, 3\n"
    "s_cbranch_scc1 LFS4_%=\n"
    "s_nop 3\n"
    "LFS4_%=:\n"
    "v_pk_mul_f32 v[224:225], v[224:225], v[82:83] op_sel_hi:[1,0]\n"
    "v_pk_mul_f32 v[226:227], v[226:227], v[82:83] op_sel_hi:[1,0]\n"
    "v_pk_mul_f32 v[228:229], v[228:229], v[82:83] op_sel_hi:[1,0]\n"
    "s_nop 0\n"
    "v_pk_mul_f32 v[230:231], v[230:231], v[82:83] op_sel_hi:[1,0]\n"
    "v_pk_mul_f32 v[232:233], v[232:233], v[82:83] op_sel_hi:[1,0]\n"
    "v_pk_mul_f32 v[234:235], v[234:235], v[82:83] op_sel_hi:[1,0]\n"
    "v_pk_mul_f32 v[236:237], v[236:237], v[82:83] op_sel_hi:[1,0]\n"
    "v_pk_mul_f32 v[238:239], v[238:239], v[82:83] op_sel_hi:[1,0]\n"
    "v_cvt_pk_bf16_f32 v224, v224, v225\n"
    "v_cvt_pk_bf16_f32 v225, v226, v227\n"
    "v_cvt_pk_bf16_f32 v226, v228, v229\n"
    "v_cvt_pk_bf16_f32 v227, v230, v231\n"
    "v_cvt_pk_bf16_f32 v232, v232, v233\n"
    "v_cvt_pk_bf16_f32 v233, v234, v235\n"
    "v_cvt_pk_bf16_f32 v234, v236, v237\n"
    "v_cvt_pk_bf16_f32 v235, v238, v239\n"
    "ds_write_b128 v16, v[224:227] offset:0\n"
    "ds_write_b128 v17, v[232:235] offset:0\n"
    "v_pk_mul_f32 v[240:241], v[240:241], v[82:83] op_sel_hi:[1,0]\n"
    "v_pk_mul_f32 v[242:243], v[242:243], v[82:83] op_sel_hi:[1,0]\n"
    "v_pk_mul_f32 v[244:245], v[244:245], v[82:83] op_sel_hi:[1,0]\n"
    "v_pk_mul_f32 v[246:247], v[246:247], v[82:83] op_sel_hi:[1,0]\n"
    "v_pk_mul_f32 v[248:249], v[248:249], v[82:83] op_sel_hi:[1,0]\n"
    "v_pk_mul_f32 v[250:251], v[250:251], v[82:83] op_sel_hi:[1,0]\n"
    "v_pk_mul_f32 v[252:253], v[252:253], v[82:83] op_sel_hi:[1,0]\n"
    "v_pk_mul_f32 v[254:255], v[254:255], v[82:83] op_sel_hi:[1,0]\n"
    "ds_read_b128 v[64:67], v18 offset:0\n"
    "ds_read_b128 v[68:71], v18 offset:1024\n"
    "v_cvt_pk_bf16_f32 v240, v240, v241\n"
    "v_cvt_pk_bf16_f32 v241, v242, v243\n"
    "v_cvt_pk_bf16_f32 v242, v244, v245\n"
    "v_cvt_pk_bf16_f32 v243, v246, v247\n"
    "v_cvt_pk_bf16_f32 v248, v248, v249\n"
    "v_cvt_pk_bf16_f32 v249, v250, v251\n"
    "v_cvt_pk_bf16_f32 v250, v252, v253\n"
    "v_cvt_pk_bf16_f32 v251, v254, v255\n"
    "ds_write_b128 v16, v[240:243] offset:2048\n"
    "ds_write_b128 v17, v[248:251] offset:2048\n"
    "v_accvgpr_read_b32 v32, a224\n"
    "v_accvgpr_read_b32 v33, a225\n"
    "v_accvgpr_read_b32 v34, a226\n"
    "v_accvgpr_read_b32 v35, a227\n"
    "v_accvgpr_read_b32 v36, a228\n"
    "v_accvgpr_read_b32 v37, a229\n"
    "v_accvgpr_read_b32 v38, a230\n"
    "v_accvgpr_read_b32 v39, a231\n"
    "v_accvgpr_read_b32 v40, a232\n"
    "v_accvgpr_read_b32 v41, a233\n"
    "v_accvgpr_read_b32 v42, a234\n"
    "v_accvgpr_read_b32 v43, a235\n"
    "v_accvgpr_read_b32 v44, a236\n"
    "v_accvgpr_read_b32 v45, a237\n"
    "v_accvgpr_read_b32 v46, a238\n"
    "v_accvgpr_read_b32 v47, a239\n"
    "ds_read_b128 v[72:75], v18 offset:2048\n"
    "ds_read_b128 v[76:79], v18 offset:3072\n"
    "v_pk_mul_f32 v[32:33], v[32:33], v[82:83] op_sel_hi:[1,0]\n"
    "v_pk_mul_f32 v[34:35], v[34:35], v[82:83] op_sel_hi:[1,0]\n"
    "v_pk_mul_f32 v[36:37], v[36:37], v[82:83] op_sel_hi:[1,0]\n"
    "v_pk_mul_f32 v[38:39], v[38:39], v[82:83] op_sel_hi:[1,0]\n"
    "v_pk_mul_f32 v[40:41], v[40:41], v[82:83] op_sel_hi:[1,0]\n"
    "v_pk_mul_f32 v[42:43], v[42:43], v[82:83] op_sel_hi:[1,0]\n"
    "v_pk_mul_f32 v[44:45], v[44:45], v[82:83] op_sel_hi:[1,0]\n"
    "v_pk_mul_f32 v[46:47], v[46:47], v[82:83] op_sel_hi:[1,0]\n"
    "v_cvt_pk_bf16_f32 v32, v32, v33\n"
    "v_cvt_pk_bf16_f32 v33, v34, v35\n"
    "v_cvt_pk_bf16_f32 v34, v36, v37\n"
    "v_cvt_pk_bf16_f32 v35, v38, v39\n"
    "v_cvt_pk_bf16_f32 v40, v40, v41\n"
    "v_cvt_pk_bf16_f32 v41, v42, v43\n"
    "v_cvt_pk_bf16_f32 v42, v44, v45\n"
    "v_cvt_pk_bf16_f32 v43, v46, v47\n"
    "s_waitcnt lgkmcnt(4)\n"
    "s_add_u32 s76, s78, 16384\n"
    "s_add_u32 s77, s78, 20480\n"
    "s_nop 0\n"
    "buffer_store_dwordx4 v[64:67], v19, s[48:51], s76 offen offset:0 nt\n"
    "buffer_store_dwordx4 v[68:71], v19, s[48:51], s77 offen offset:0 nt\n"
    "ds_write_b128 v16, v[32:35] offset:0\n"
    "ds_write_b128 v17, v[40:43] offset:0\n"
    "v_accvgpr_read_b32 v48, a240\n"
    "v_accvgpr_read_b32 v49, a241\n"
    "v_accvgpr_read_b32 v50, a242\n"
    "v_accvgpr_read_b32 v51, a243\n"
    "v_accvgpr_read_b32 v52, a244\n"
    "v_accvgpr_read_b32 v53, a245\n"
    "v_accvgpr_read_b32 v54, a246\n"
    "v_accvgpr_read_b32 v55, a247\n"
    "v_accvgpr_read_b32 v56, a248\n"
    "v_accvgpr_read_b32 v57, a249\n"
    "v_accvgpr_read_b32 v58, a250\n"
    "v_accvgpr_read_b32 v59, a251\n"
    "v_accvgpr_read_b32 v60, a252\n"
    "v_accvgpr_read_b32 v61, a253\n"
    "v_accvgpr_read_b32 v62, a254\n"
    "v_accvgpr_read_b32 v63, a255\n"
    "ds_read_b128 v[64:67], v18 offset:0\n"
    "ds_read_b128 v[68:71], v18 offset:1024\n"
    "v_pk_mul_f32 v[48:49], v[48:49], v[82:83] op_sel_hi:[1,0]\n"
    "v_pk_mul_f32 v[50:51], v[50:51], v[82:83] op_sel_hi:[1,0]\n"
    "v_pk_mul_f32 v[52:53], v[52:53], v[82:83] op_sel_hi:[1,0]\n"
    "v_pk_mul_f32 v[54:55], v[54:55], v[82:83] op_sel_hi:[1,0]\n"
    "v_pk_mul_f32 v[56:57], v[56:57], v[82:83] op_sel_hi:[1,0]\n"
    "v_pk_mul_f32 v[58:59], v[58:59], v[82:83] op_sel_hi:[1,0]\n"
    "v_pk_mul_f32 v[60:61], v[60:61], v[82:83] op_sel_hi:[1,0]\n"
    "v_pk_mul_f32 v[62:63], v[62:63], v[82:83] op_sel_hi:[1,0]\n"
    "v_cvt_pk_bf16_f32 v48, v48, v49\n"
    "v_cvt_pk_bf16_f32 v49, v50, v51\n"
    "v_cvt_pk_bf16_f32 v50, v52, v53\n"
    "v_cvt_pk_bf16_f32 v51, v54, v55\n"
    "v_cvt_pk_bf16_f32 v56, v56, v57\n"
    "v_cvt_pk_bf16_f32 v57, v58, v59\n"
    "v_cvt_pk_bf16_f32 v58, v60, v61\n"
    "v_cvt_pk_bf16_f32 v59, v62, v63\n"
    "s_waitcnt lgkmcnt(4)\n"
    "buffer_store_dwordx4 v[72:75], v19, s[48:51], s76 offen offset:128 nt\n"
    "s_nop 0\n"
    "buffer_store_dwordx4 v[76:79], v19, s[48:51], s77 offen offset:128 nt\n"
    "ds_write_b128 v16, v[48:51] offset:2048\n"
    "ds_write_b128 v17, v[56:59] offset:2048\n"
    "v_accvgpr_read_b32 v32, a160\n"
    "v_accvgpr_read_b32 v33, a161\n"
    "v_accvgpr_read_b32 v34, a162\n"
    "v_accvgpr_read_b32 v35, a163\n"
    "v_accvgpr_read_b32 v36, a164\n"
    "v_accvgpr_read_b32 v37, a165\n"
    "v_accvgpr_read_b32 v38, a166\n"
    "v_accvgpr_read_b32 v39, a167\n"
    "v_accvgpr_read_b32 v40, a168\n"
    "v_accvgpr_read_b32 v41, a169\n"
    "v_accvgpr_read_b32 v42, a170\n"
    "v_accvgpr_read_b32 v43, a171\n"
    "v_accvgpr_read_b32 v44, a172\n"
    "v_accvgpr_read_b32 v45, a173\n"
    "v_accvgpr_read_b32 v46, a174\n"
    "v_accvgpr_read_b32 v47, a175\n"
    "ds_read_b128 v[72:75], v18 offset:2048\n"
    "ds_read_b128 v[76:79], v18 offset:3072\n"
    "v_pk_mul_f32 v[32:33], v[32:33], v[82:83] op_sel:[0,1] op_sel_hi:[1,1]\n"
    "v_pk_mul_f32 v[34:35], v[34:35], v[82:83] op_sel:[0,1] op_sel_hi:[1,1]\n"
    "v_pk_mul_f32 v[36:37], v[36:37], v[82:83] op_sel:[0,1] op_sel_hi:[1,1]\n"
    "v_pk_mul_f32 v[38:39], v[38:39], v[82:83] op_sel:[0,1] op_sel_hi:[1,1]\n"
    "v_pk_mul_f32 v[40:41], v[40:41], v[82:83] op_sel:[0,1] op_sel_hi:[1,1]\n"
    "v_pk_mul_f32 v[42:43], v[42:43], v[82:83] op_sel:[0,1] op_sel_hi:[1,1]\n"
    "v_pk_mul_f32 v[44:45], v[44:45], v[82:83] op_sel:[0,1] op_sel_hi:[1,1]\n"
    "v_pk_mul_f32 v[46:47], v[46:47], v[82:83] op_sel:[0,1] op_sel_hi:[1,1]\n"
    "v_cvt_pk_bf16_f32 v32, v32, v33\n"
    "v_cvt_pk_bf16_f32 v33, v34, v35\n"
    "v_cvt_pk_bf16_f32 v34, v36, v37\n"
    "v_cvt_pk_bf16_f32 v35, v38, v39\n"
    "v_cvt_pk_bf16_f32 v40, v40, v41\n"
    "v_cvt_pk_bf16_f32 v41, v42, v43\n"
    "v_cvt_pk_bf16_f32 v42, v44, v45\n"
    "v_cvt_pk_bf16_f32 v43, v46, v47\n"
    "s_waitcnt lgkmcnt(4)\n"
    "buffer_store_dwordx4 v[64:67], v19, s[48:51], s76 offen offset:256 nt\n"
    "buffer_store_dwordx4 v[68:71], v19, s[48:51], s77 offen offset:256 nt\n"
    "s_nop 0\n"
    "ds_write_b128 v16, v[32:35] offset:0\n"
    "ds_write_b128 v17, v[40:43] offset:0\n"
    "v_accvgpr_read_b32 v48, a176\n"
    "v_accvgpr_read_b32 v49, a177\n"
    "v_accvgpr_read_b32 v50, a178\n"
    "v_accvgpr_read_b32 v51, a179\n"
    "v_accvgpr_read_b32 v52, a180\n"
    "v_accvgpr_read_b32 v53, a181\n"
    "v_accvgpr_read_b32 v54, a182\n"
    "v_accvgpr_read_b32 v55, a183\n"
    "v_accvgpr_read_b32 v56, a184\n"
    "v_accvgpr_read_b32 v57, a185\n"
    "v_accvgpr_read_b32 v58, a186\n"
    "v_accvgpr_read_b32 v59, a187\n"
    "v_accvgpr_read_b32 v60, a188\n"
    "v_accvgpr_read_b32 v61, a189\n"
    "v_accvgpr_read_b32 v62, a190\n"
    "v_accvgpr_read_b32 v63, a191\n"
    "ds_read_b128 v[64:67], v18 offset:0\n"
    "ds_read_b128 v[68:71], v18 offset:1024\n"
    "v_pk_mul_f32 v[48:49], v[48:49], v[82:83] op_sel:[0,1] op_sel_hi:[1,1]\n"
    "v_pk_mul_f32 v[50:51], v[50:51], v[82:83] op_sel:[0,1] op_sel_hi:[1,1]\n"
    "v_pk_mul_f32 v[52:53], v[52:53], v[82:83] op_sel:[0,1] op_sel_hi:[1,1]\n"
    "v_pk_mul_f32 v[54:55], v[54:55], v[82:83] op_sel:[0,1] op_sel_hi:[1,1]\n"
    "v_pk_mul_f32 v[56:57], v[56:57], v[82:83] op_sel:[0,1] op_sel_hi:[1,1]\n"
    "v_pk_mul_f32 v[58:59], v[58:59], v[82:83] op_sel:[0,1] op_sel_hi:[1,1]\n"
    "v_pk_mul_f32 v[60:61], v[60:61], v[82:83] op_sel:[0,1] op_sel_hi:[1,1]\n"
    "v_pk_mul_f32 v[62:63], v[62:63], v[82:83] op_sel:[0,1] op_sel_hi:[1,1]\n"
    "v_cvt_pk_bf16_f32 v48, v48, v49\n"
    "v_cvt_pk_bf16_f32 v49, v50, v51\n"
    "v_cvt_pk_bf16_f32 v50, v52, v53\n"
    "v_cvt_pk_bf16_f32 v51, v54, v55\n"
    "v_cvt_pk_bf16_f32 v56, v56, v57\n"
    "v_cvt_pk_bf16_f32 v57, v58, v59\n"
    "v_cvt_pk_bf16_f32 v58, v60, v61\n"
    "v_cvt_pk_bf16_f32 v59, v62, v63\n"
    "s_waitcnt lgkmcnt(4)\n"
    "buffer_store_dwordx4 v[72:75], v19, s[48:51], s76 offen offset:384 nt\n"
    "buffer_store_dwordx4 v[76:79], v19, s[48:51], s77 offen offset:384 nt\n"
    "ds_write_b128 v16, v[48:51] offset:2048\n"
    "s_nop 0\n"
    "ds_write_b128 v17, v[56:59] offset:2048\n"
    "v_accvgpr_read_b32 v32, a192\n"
    "v_accvgpr_read_b32 v33, a193\n"
    "v_accvgpr_read_b32 v34, a194\n"
    "v_accvgpr_read_b32 v35, a195\n"
    "v_accvgpr_read_b32 v36, a196\n"
    "v_accvgpr_read_b32 v37, a197\n"
    "v_accvgpr_read_b32 v38, a198\n"
    "v_accvgpr_read_b32 v39, a199\n"
    "v_accvgpr_read_b32 v40, a200\n"
    "v_accvgpr_read_b32 v41, a201\n"
    "v_accvgpr_read_b32 v42, a202\n"
    "v_accvgpr_read_b32 v43, a203\n"
    "v_accvgpr_read_b32 v44, a204\n"
    "v_accvgpr_read_b32 v45, a205\n"
    "v_accvgpr_read_b32 v46, a206\n"
    "v_accvgpr_read_b32 v47, a207\n"
    "ds_read_b128 v[72:75], v18 offset:2048\n"
    "ds_read_b128 v[76:79], v18 offset:3072\n"
    "v_pk_mul_f32 v[32:33], v[32:33], v[82:83] op_sel:[0,1] op_sel_hi:[1,1]\n"
    "v_pk_mul_f32 v[34:35], v[34:35], v[82:83] op_sel:[0,1] op_sel_hi:[1,1]\n"
    "v_pk_mul_f32 v[36:37], v[36:37], v[82:83] op_sel:[0,1] op_sel_hi:[1,1]\n"
    "v_pk_mul_f32 v[38:39], v[38:39], v[82:83] op_sel:[0,1] op_sel_hi:[1,1]\n"
    "v_pk_mul_f32 v[40:41], v[40:41], v[82:83] op_sel:[0,1] op_sel_hi:[1,1]\n"
    "v_pk_mul_f32 v[42:43], v[42:43], v[82:83] op_sel:[0,1] op_sel_hi:[1,1]\n"
    "v_pk_mul_f32 v[44:45], v[44:45], v[82:83] op_sel:[0,1] op_sel_hi:[1,1]\n"
    "v_pk_mul_f32 v[46:47], v[46:47], v[82:83] op_sel:[0,1] op_sel_hi:[1,1]\n"
    "v_cvt_pk_bf16_f32 v32, v32, v33\n"
    "v_cvt_pk_bf16_f32 v33, v34, v35\n"
    "v_cvt_pk_bf16_f32 v34, v36, v37\n"
    "v_cvt_pk_bf16_f32 v35, v38, v39\n"
    "v_cvt_pk_bf16_f32 v40, v40, v41\n"
    "v_cvt_pk_bf16_f32 v41, v42, v43\n"
    "v_cvt_pk_bf16_f32 v42, v44, v45\n"
    "v_cvt_pk_bf16_f32 v43, v46, v47\n"
    "s_waitcnt lgkmcnt(4)\n"
    "s_nop 0\n"
    "s_add_u32 s76, s78, 24576\n"
    "s_add_u32 s77, s78, 28672\n"
    "buffer_store_dwordx4 v[64:67], v19, s[48:51], s76 offen offset:0 nt\n"
    "buffer_store_dwordx4 v[68:71], v19, s[48:51], s77 offen offset:0 nt\n"
    "ds_write_b128 v16, v[32:35] offset:0\n"
    "ds_write_b128 v17, v[40:43] offset:0\n"
    "v_accvgpr_read_b32 v48, a208\n"
    "v_accvgpr_read_b32 v49, a209\n"
    "v_accvgpr_read_b32 v50, a210\n"
    "v_accvgpr_read_b32 v51, a211\n"
    "v_accvgpr_read_b32 v52, a212\n"
    "v_accvgpr_read_b32 v53, a213\n"
    "v_accvgpr_read_b32 v54, a214\n"
    "v_accvgpr_read_b32 v55, a215\n"
    "v_accvgpr_read_b32 v56, a216\n"
    "v_accvgpr_read_b32 v57, a217\n"
    "v_accvgpr_read_b32 v58, a218\n"
    "v_accvgpr_read_b32 v59, a219\n"
    "v_accvgpr_read_b32 v60, a220\n"
    "v_accvgpr_read_b32 v61, a221\n"
    "v_accvgpr_read_b32 v62, a222\n"
    "v_accvgpr_read_b32 v63, a223\n"
    "ds_read_b128 v[64:67], v18 offset:0\n"
    "ds_read_b128 v[68:71], v18 offset:1024\n"
    "v_pk_mul_f32 v[48:49], v[48:49], v[82:83] op_sel:[0,1] op_sel_hi:[1,1]\n"
    "v_pk_mul_f32 v[50:51], v[50:51], v[82:83] op_sel:[0,1] op_sel_hi:[1,1]\n"
    "v_pk_mul_f32 v[52:53], v[52:53], v[82:83] op_sel:[0,1] op_sel_hi:[1,1]\n"
    "v_pk_mul_f32 v[54:55], v[54:55], v[82:83] op_sel:[0,1] op_sel_hi:[1,1]\n"
    "v_pk_mul_f32 v[56:57], v[56:57], v[82:83] op_sel:[0,1] op_sel_hi:[1,1]\n"
    "v_pk_mul_f32 v[58:59], v[58:59], v[82:83] op_sel:[0,1] op_sel_hi:[1,1]\n"
    "v_pk_mul_f32 v[60:61], v[60:61], v[82:83] op_sel:[0,1] op_sel_hi:[1,1]\n"
    "v_pk_mul_f32 v[62:63], v[62:63], v[82:83] op_sel:[0,1] op_sel_hi:[1,1]\n"
    "v_cvt_pk_bf16_f32 v48, v48, v49\n"
    "v_cvt_pk_bf16_f32 v49, v50, v51\n"
    "v_cvt_pk_bf16_f32 v50, v52, v53\n"
    "v_cvt_pk_bf16_f32 v51, v54, v55\n"
    "v_cvt_pk_bf16_f32 v56, v56, v57\n"
    "v_cvt_pk_bf16_f32 v57, v58, v59\n"
    "v_cvt_pk_bf16_f32 v58, v60, v61\n"
    "v_cvt_pk_bf16_f32 v59, v62, v63\n"
    "s_waitcnt lgkmcnt(4)\n"
    "buffer_store_dwordx4 v[72:75], v19, s[48:51], s76 offen offset:128 nt\n"
    "buffer_store_dwordx4 v[76:79], v19, s[48:51], s77 offen offset:128 nt\n"
    "ds_write_b128 v16, v[48:51] offset:2048\n"
    "ds_write_b128 v17, v[56:59] offset:2048\n"
    "ds_read_b128 v[72:75], v18 offset:2048\n"
    "ds_read_b128 v[76:79], v18 offset:3072\n"
    "s_waitcnt lgkmcnt(4)\n"
    "buffer_store_dwordx4 v[64:67], v19, s[48:51], s76 offen offset:256 nt\n"
    "buffer_store_dwordx4 v[68:71], v19, s[48:51], s77 offen offset:256 nt\n"
    "s_waitcnt vmcnt(14)\n"
    "ds_write_b128 v94, v[96:99] offset:0\n"
    "ds_write_b128 v95, v[104:107] offset:0\n"
    "s_nop 0\n"
    "v_pk_mul_f32 v[120:121], v[120:121], v[80:81] op_sel_hi:[1,0]\n"
    "v_pk_mul_f32 v[122:123], v[122:123], v[80:81] op_sel_hi:[1,0]\n"
    "v_pk_mul_f32 v[124:125], v[124:125], v[80:81] op_sel_hi:[1,0]\n"
    "v_pk_mul_f32 v[126:127], v[126:127], v[80:81] op_sel_hi:[1,0]\n"
    "v_cvt_pk_bf16_f32 v113, v114, v115\n"
    "ds_read_b128 v[64:67], v93 offset:0\n"
    "ds_read_b128 v[68:71], v93 offset:4096\n"
    "v_cvt_pk_bf16_f32 v114, v116, v117\n"
    "v_cvt_pk_bf16_f32 v115, v118, v119\n"
    "v_cvt_pk_bf16_f32 v120, v120, v121\n"
    "v_cvt_pk_bf16_f32 v121, v122, v123\n"
    "v_cvt_pk_bf16_f32 v122, v124, v125\n"
    "v_cvt_pk_bf16_f32 v123, v126, v127\n"
    "s_waitcnt lgkmcnt(4)\n"
    "buffer_store_dwordx4 v[72:75], v19, s[48:51], s76 offen offset:384 nt\n"
    "buffer_store_dwordx4 v[76:79], v19, s[48:51], s77 offen offset:384 nt\n"
    "s_nop 0\n"
    "ds_write_b128 v94, v[112:115] offset:1024\n"
    "ds_write_b128 v95, v[120:123] offset:1024\n"
    "v_pk_mul_f32 v[128:129], v[128:129], v[80:81] op_sel_hi:[1,0]\n"
    "v_pk_mul_f32 v[130:131], v[130:131], v[80:81] op_sel_hi:[1,0]\n"
    "v_pk_mul_f32 v[132:133], v[132:133], v[80:81] op_sel_hi:[1,0]\n"
    "v_pk_mul_f32 v[134:135], v[134:135], v[80:81] op_sel_hi:[1,0]\n"
    "v_pk_mul_f32 v[136:137], v[136:137], v[80:81] op_sel_hi:[1,0]\n"
    "v_pk_mul_f32 v[138:139], v[138:139], v[80:81] op_sel_hi:[1,0]\n"
    "v_pk_mul_f32 v[140:141], v[140:141], v[80:81] op_sel_hi:[1,0]\n"
    "v_pk_mul_f32 v[142:143], v[142:143], v[80:81] op_sel_hi:[1,0]\n"
    "ds_read_b128 v[72:75], v93 offset:1024\n"
    "ds_read_b128 v[76:79], v93 offset:5120\n"
    "v_cvt_pk_bf16_f32 v128, v128, v129\n"
    "v_cvt_pk_bf16_f32 v129, v130, v131\n"
    "v_cvt_pk_bf16_f32 v130, v132, v133\n"
    "v_cvt_pk_bf16_f32 v131, v134, v135\n"
    "v_cvt_pk_bf16_f32 v136, v136, v137\n"
    "v_cvt_pk_bf16_f32 v137, v138, v139\n"
    "v_cvt_pk_bf16_f32 v138, v140, v141\n"
    "v_cvt_pk_bf16_f32 v139, v142, v143\n"
    "s_waitcnt lgkmcnt(4)\n"
    "s_add_u32 s76, s78, 0\n"
    "s_add_u32 s77, s78, 4096\n"
    "buffer_store_dwordx4 v[64:67], v92, s[48:51], s76 offen offset:0 nt\n"
    "buffer_store_dwordx4 v[68:71], v92, s[48:51], s77 offen offset:0 nt\n"
    "ds_write_b128 v94, v[128:131] offset:2048\n"
    "ds_write_b128 v95, v[136:139] offset:2048\n"
    "v_pk_mul_f32 v[144:145], v[144:145], v[80:81] op_sel_hi:[1,0]\n"
    "v_pk_mul_f32 v[146:147], v[146:147], v[80:81] op_sel_hi:[1,0]\n"
    "v_pk_mul_f32 v[148:149], v[148:149], v[80:81] op_sel_hi:[1,0]\n"
    "v_pk_mul_f32 v[150:151], v[150:151], v[80:81] op_sel_hi:[1,0]\n"
    "v_pk_mul_f32 v[152:153], v[152:153], v[80:81] op_sel_hi:[1,0]\n"
    "v_pk_mul_f32 v[154:155], v[154:155], v[80:81] op_sel_hi:[1,0]\n"
    "v_pk_mul_f32 v[156:157], v[156:157], v[80:81] op_sel_hi:[1,0]\n"
    "v_pk_mul_f32 v[158:159], v[158:159], v[80:81] op_sel_hi:[1,0]\n"
    "ds_read_b128 v[64:67], v93 offset:2048\n"
    "ds_read_b128 v[68:71], v93 offset:6144\n"
    "v_cvt_pk_bf16_f32 v144, v144, v145\n"
    "v_cvt_pk_bf16_f32 v145, v146, v147\n"
    "v_cvt_pk_bf16_f32 v146, v148, v149\n"
    "v_cvt_pk_bf16_f32 v147, v150, v151\n"
    "v_cvt_pk_bf16_f32 v152, v152, v153\n"
    "v_cvt_pk_bf16_f32 v153, v154, v155\n"
    "v_cvt_pk_bf16_f32 v154, v156, v157\n"
    "v_cvt_pk_bf16_f32 v155, v158, v159\n"
    "s_waitcnt lgkmcnt(4)\n"
    "buffer_store_dwordx4 v[72:75], v92, s[48:51], s76 offen offset:128 nt\n"
    "buffer_store_dwordx4 v[76:79], v92, s[48:51], s77 offen offset:128 nt\n"
    "ds_write_b128 v94, v[144:147] offset:3072\n"
    "ds_write_b128 v95, v[152:155] offset:3072\n"
    "ds_read_b128 v[72:75], v93 offset:3072\n"
    "ds_read_b128 v[76:79], v93 offset:7168\n"
    "s_waitcnt lgkmcnt(4)\n"
    "buffer_store_dwordx4 v[64:67], v92, s[48:51], s76 offen offset:256 nt\n"
    "buffer_store_dwordx4 v[68:71], v92, s[48:51], s77 offen offset:256 nt\n"
    "ds_write_b128 v94, v[160:163] offset:8192\n"
    "ds_write_b128 v95, v[168:171] offset:8192\n"
    "v_pk_mul_f32 v[176:177], v[176:177], v[80:81] op_sel:[0,1] op_sel_hi:[1,1]\n"
    "v_pk_mul_f32 v[178:179], v[178:179], v[80:81] op_sel:[0,1] op_sel_hi:[1,1]\n"
    "v_pk_mul_f32 v[180:181], v[180:181], v[80:81] op_sel:[0,1] op_sel_hi:[1,1]\n"
    "v_pk_mul_f32 v[182:183], v[182:183], v[80:81] op_sel:[0,1] op_sel_hi:[1,1]\n"
    "v_pk_mul_f32 v[184:185], v[184:185], v[80:81] op_sel:[0,1] op_sel_hi:[1,1]\n"
    "v_pk_mul_f32 v[186:187], v[186:187], v[80:81] op_sel:[0,1] op_sel_hi:[1,1]\n"
    "v_pk_mul_f32 v[188:189], v[188:189], v[80:81] op_sel:[0,1] op_sel_hi:[1,1]\n"
    "v_pk_mul_f32 v[190:191], v[190:191], v[80:81] op_sel:[0,1] op_sel_hi:[1,1]\n"
    "ds_read_b128 v[64:67], v93 offset:8192\n"
    "ds_read_b128 v[68:71], v93 offset:12288\n"
    "v_cvt_pk_bf16_f32 v176, v176, v177\n"
    "v_cvt_pk_bf16_f32 v177, v178, v179\n"
    "v_cvt_pk_bf16_f32 v178, v180, v181\n"
    "v_cvt_pk_bf16_f32 v179, v182, v183\n"
    "v_cvt_pk_bf16_f32 v184, v184, v185\n"
    "v_cvt_pk_bf16_f32 v185, v186, v187\n"
    "v_cvt_pk_bf16_f32 v186, v188, v189\n"
    "v_cvt_pk_bf16_f32 v187, v190, v191\n"
    "s_waitcnt lgkmcnt(4)\n"
    "buffer_store_dwordx4 v[72:75], v92, s[48:51], s76 offen offset:384 nt\n"
    "buffer_store_dwordx4 v[76:79], v92, s[48:51], s77 offen offset:384 nt\n"
    "s_nop 0\n"
    "ds_write_b128 v94, v[176:179] offset:9216\n"
    "ds_write_b128 v95, v[184:187] offset:9216\n"
    "v_pk_mul_f32 v[192:193], v[192:193], v[80:81] op_sel:[0,1] op_sel_hi:[1,1]\n"
    "v_pk_mul_f32 v[194:195], v[194:195], v[80:81] op_sel:[0,1] op_sel_hi:[1,1]\n"
    "v_pk_mul_f32 v[196:197], v[196:197], v[80:81] op_sel:[0,1] op_sel_hi:[1,1]\n"
    "v_pk_mul_f32 v[198:199], v[198:199], v[80:81] op_sel:[0,1] op_sel_hi:[1,1]\n"
    "v_pk_mul_f32 v[200:201], v[200:201], v[80:81] op_sel:[0,1] op_sel_hi:[1,1]\n"
    "v_pk_mul_f32 v[202:203], v[202:203], v[80:81] op_sel:[0,1] op_sel_hi:[1,1]\n"
    "v_pk_mul_f32 v[204:205], v[204:205], v[80:81] op_sel:[0,1] op_sel_hi:[1,1]\n"
    "v_pk_mul_f32 v[206:207], v[206:207], v[80:81] op_sel:[0,1] op_sel_hi:[1,1]\n"
    "ds_read_b128 v[72:75], v93 offset:9216\n"
    "ds_read_b128 v[76:79], v93 offset:13312\n"
    "v_cvt_pk_bf16_f32 v192, v192, v193\n"
    "v_cvt_pk_bf16_f32 v193, v194, v195\n"
    "v_cvt_pk_bf16_f32 v194, v196, v197\n"
    "v_cvt_pk_bf16_f32 v195, v198, v199\n"
    "v_cvt_pk_bf16_f32 v200, v200, v201\n"
    "v_cvt_pk_bf16_f32 v201, v202, v203\n"
    "v_cvt_pk_bf16_f32 v202, v204, v205\n"
    "v_cvt_pk_bf16_f32 v203, v206, v207\n"
    "s_waitcnt lgkmcnt(4)\n"
    "s_add_u32 s76, s78, 8192\n"
    "s_add_u32 s77, s78, 12288\n"
    "buffer_store_dwordx4 v[64:67], v92, s[48:51], s76 offen offset:0 nt\n"
    "s_nop 0\n"
    "buffer_store_dwordx4 v[68:71], v92, s[48:51], s77 offen offset:0 nt\n"
    "ds_write_b128 v94, v[192:195] offset:10240\n"
    "ds_write_b128 v95, v[200:203] offset:10240\n"
    "v_pk_mul_f32 v[208:209], v[208:209], v[80:81] op_sel:[0,1] op_sel_hi:[1,1]\n"
    "v_pk_mul_f32 v[210:211], v[210:211], v[80:81] op_sel:[0,1] op_sel_hi:[1,1]\n"
    "v_pk_mul_f32 v[212:213], v[212:213], v[80:81] op_sel:[0,1] op_sel_hi:[1,1]\n"
    "v_pk_mul_f32 v[214:215], v[214:215], v[80:81] op_sel:[0,1] op_sel_hi:[1,1]\n"
    "v_pk_mul_f32 v[216:217], v[216:217], v[80:81] op_sel:[0,1] op_sel_hi:[1,1]\n"
    "v_pk_mul_f32 v[218:219], v[218:219], v[80:81] op_sel:[0,1] op_sel_hi:[1,1]\n"
    "v_pk_mul_f32 v[220:221], v[220:221], v[80:81] op_sel:[0,1] op_sel_hi:[1,1]\n"
    "v_pk_mul_f32 v[222:223], v[222:223], v[80:81] op_sel:[0,1] op_sel_hi:[1,1]\n"
    "ds_read_b128 v[64:67], v93 offset:10240\n"
    "ds_read_b128 v[68:71], v93 offset:14336\n"
    "v_cvt_pk_bf16_f32 v208, v208, v209\n"
    "v_cvt_pk_bf16_f32 v209, v210, v211\n"
    "v_cvt_pk_bf16_f32 v210, v212, v213\n"
    "v_cvt_pk_bf16_f32 v211, v214, v215\n"
    "v_cvt_pk_bf16_f32 v216, v216, v217\n"
    "v_cvt_pk_bf16_f32 v217, v218, v219\n"
    "v_cvt_pk_bf16_f32 v218, v220, v221\n"
    "v_cvt_pk_bf16_f32 v219, v222, v223\n"
    "s_waitcnt lgkmcnt(4)\n"
    "buffer_store_dwordx4 v[72:75], v92, s[48:51], s76 offen offset:128 nt\n"
    "buffer_store_dwordx4 v[76:79], v92, s[48:51], s77 offen offset:128 nt\n"
    "s_nop 0\n"
    "ds_write_b128 v94, v[208:211] offset:11264\n"
    "ds_write_b128 v95, v[216:219] offset:11264\n"
    "ds_read_b128 v[72:75], v93 offset:11264\n"
    "ds_read_b128 v[76:79], v93 offset:15360\n"
    "s_waitcnt lgkmcnt(4)\n"
    "buffer_store_dwordx4 v[64:67], v92, s[48:51], s76 offen offset:256 nt\n"
    "buffer_store_dwordx4 v[68:71], v92, s[48:51], s77 offen offset:256 nt\n"
    "s_waitcnt lgkmcnt(0)\n"
    "buffer_store_dwordx4 v[72:75], v92, s[48:51], s76 offen offset:384 nt\n"
    "buffer_store_dwordx4 v[76:79], v92, s[48:51], s77 offen offset:384 nt\n"
    "s_branch LEND_%=\n"
    ".p2align 5\n"
    "LEND3_%=:\n"
    "s_cmp_lt_u32 s69, 1\n"
    "s_cbranch_scc1 LFS3_%=\n"
    "s_nop 3\n"
    "s_cmp_lt_u32 s69, 2\n"
    "s_cbranch_scc1 LFS3_%=\n"
    "s_nop 3\n"
    "s_cmp_lt_u32 s69, 3\n"
    "s_cbranch_scc1 LFS3_%=\n"
    "s_nop 3\n"
    "LFS3_%=:\n"
    "v_pk_mul_f32 v[224:225], v[224:225], v[82:83] op_sel_hi:[1,0]\n"
    "v_pk_mul_f32 v[226:227], v[226:227], v[82:83] op_sel_hi:[1,0]\n"
    "v_pk_mul_f32 v[228:229], v[228:229], v[82:83] op_sel_hi:[1,0]\n"
    "s_nop 0\n"
    "v_pk_mul_f32 v[230:231], v[230:231], v[82:83] op_sel_hi:[1,0]\n"
    "v_pk_mul_f32 v[232:233], v[232:233], v[82:83] op_sel_hi:[1,0]\n"
    "v_pk_mul_f32 v[234:235], v[234:235], v[82:83] op_sel_hi:[1,0]\n"
    "v_pk_mul_f32 v[236:237], v[236:237], v[82:83] op_sel_hi:[1,0]\n"
    "v_pk_mul_f32 v[238:239], v[238:239], v[82:83] op_sel_hi:[1,0]\n"
    "v_cvt_pk_bf16_f32 v224, v224, v225\n"
    "v_cvt_pk_bf16_f32 v225, v226, v227\n"
    "v_cvt_pk_bf16_f32 v226, v228, v229\n"
    "v_cvt_pk_bf16_f32 v227, v230, v231\n"
    "v_cvt_pk_bf16_f32 v232, v232, v233\n"
    "v_cvt_pk_bf16_f32 v233, v234, v235\n"
    "v_cvt_pk_bf16_f32 v234, v236, v237\n"
    "v_cvt_pk_bf16_f32 v235, v238, v239\n"
    "ds_write_b128 v16, v[224:227] offset:0\n"
    "ds_write_b128 v17, v[232:235] offset:0\n"
    "v_pk_mul_f32 v[240:241], v[240:241], v[82:83] op_sel_hi:[1,0]\n"
    "v_pk_mul_f32 v[242:243], v[242:243], v[82:83] op_sel_hi:[1,0]\n"
    "v_pk_mul_f32 v[244:245], v[244:245], v[82:83] op_sel_hi:[1,0]\n"
    "v_pk_mul_f32 v[246:247], v[246:247], v[82:83] op_sel_hi:[1,0]\n"
    "v_pk_mul_f32 v[248:249], v[248:249], v[82:83] op_sel_hi:[1,0]\n"
    "v_pk_mul_f32 v[250:251], v[250:251], v[82:83] op_sel_hi:[1,0]\n"
    "v_pk_mul_f32 v[252:253], v[252:253], v[82:83] op_sel_hi:[1,0]\n"
    "v_pk_mul_f32 v[254:255], v[254:255], v[82:83] op_sel_hi:[1,0]\n"
    "ds_read_b128 v[64:67], v18 offset:0\n"
    "ds_read_b128 v[68:71], v18 offset:1024\n"
    "v_cvt_pk_bf16_f32 v240, v240, v241\n"
    "v_cvt_pk_bf16_f32 v241, v242, v243\n"
    "v_cvt_pk_bf16_f32 v242, v244, v245\n"
    "v_cvt_pk_bf16_f32 v243, v246, v247\n"
    "v_cvt_pk_bf16_f32 v248, v248, v249\n"
    "v_cvt_pk_bf16_f32 v249, v250, v251\n"
    "v_cvt_pk_bf16_f32 v250, v252, v253\n"
    "v_cvt_pk_bf16_f32 v251, v254, v255\n"
    "ds_write_b128 v16, v[240:243] offset:2048\n"
    "ds_write_b128 v17, v[248:251] offset:2048\n"
    "v_accvgpr_read_b32 v32, a224\n"
    "v_accvgpr_read_b32 v33, a225\n"
    "v_accvgpr_read_b32 v34, a226\n"
    "v_accvgpr_read_b32 v35, a227\n"
    "v_accvgpr_read_b32 v36, a228\n"
    "v_accvgpr_read_b32 v37, a229\n"
    "v_accvgpr_read_b32 v38, a230\n"
    "v_accvgpr_read_b32 v39, a231\n"
    "v_accvgpr_read_b32 v40, a232\n"
    "v_accvgpr_read_b32 v41, a233\n"
    "v_accvgpr_read_b32 v42, a234\n"
    "v_accvgpr_read_b32 v43, a235\n"
    "v_accvgpr_read_b32 v44, a236\n"
    "v_accvgpr_read_b32 v45, a237\n"
    "v_accvgpr_read_b32 v46, a238\n"
    "v_accvgpr_read_b32 v47, a239\n"
    "ds_read_b128 v[72:75], v18 offset:2048\n"
    "ds_read_b128 v[76:79], v18 offset:3072\n"
    "v_pk_mul_f32 v[32:33], v[32:33], v[82:83] op_sel_hi:[1,0]\n"
    "v_pk_mul_f32 v[34:35], v[34:35], v[82:83] op_sel_hi:[1,0]\n"
    "v_pk_mul_f32 v[36:37], v[36:37], v[82:83] op_sel_hi:[1,0]\n"
    "v_pk_mul_f32 v[38:39], v[38:39], v[82:83] op_sel_hi:[1,0]\n"
    "v_pk_mul_f32 v[40:41], v[40:41], v[82:83] op_sel_hi:[1,0]\n"
    "v_pk_mul_f32 v[42:43], v[42:43], v[82:83] op_sel_hi:[1,0]\n"
    "v_pk_mul_f32 v[44:45], v[44:45], v[82:83] op_sel_hi:[1,0]\n"
    "v_pk_mul_f32 v[46:47], v[46:47], v[82:83] op_sel_hi:[1,0]\n"
    "v_cvt_pk_bf16_f32 v32, v32, v33\n"
    "v_cvt_pk_bf16_f32 v33, v34, v35\n"
    "v_cvt_pk_bf16_f32 v34, v36, v37\n"
    "v_cvt_pk_bf16_f32 v35, v38, v39\n"
    "v_cvt_pk_bf16_f32 v40, v40, v41\n"
    "v_cvt_pk_bf16_f32 v41, v42, v43\n"
    "v_cvt_pk_bf16_f32 v42, v44, v45\n"
    "v_cvt_pk_bf16_f32 v43, v46, v47\n"
    "s_waitcnt lgkmcnt(4)\n"
    "s_add_u32 s76, s79, 16384\n"
    "s_add_u32 s77, s79, 20480\n"
    "s_nop 0\n"
    "buffer_store_dwordx4 v[64:67], v19, s[48:51], s76 offen offset:0 nt\n"
    "buffer_store_dwordx4 v[68:71], v19, s[48:51], s77 offen offset:0 nt\n"
    "ds_write_b128 v16, v[32:35] offset:0\n"
    "ds_write_b128 v17, v[40:43] offset:0\n"
    "v_accvgpr_read_b32 v48, a240\n"
    "v_accvgpr_read_b32 v49, a241\n"
    "v_accvgpr_read_b32 v50, a242\n"
    "v_accvgpr_read_b32 v51, a243\n"
    "v_accvgpr_read_b32 v52, a244\n"
    "v_accvgpr_read_b32 v53, a245\n"
    "v_accvgpr_read_b32 v54, a246\n"
    "v_accvgpr_read_b32 v55, a247\n"
    "v_accvgpr_read_b32 v56, a248\n"
    "v_accvgpr_read_b32 v57, a249\n"
    "v_accvgpr_read_b32 v58, a250\n"
    "v_accvgpr_read_b32 v59, a251\n"
    "v_accvgpr_read_b32 v60, a252\n"
    "v_accvgpr_read_b32 v61, a253\n"
    "v_accvgpr_read_b32 v62, a254\n"
    "v_accvgpr_read_b32 v63, a255\n"
    "ds_read_b128 v[64:67], v18 offset:0\n"
    "ds_read_b128 v[68:71], v18 offset:1024\n"
    "v_pk_mul_f32 v[48:49], v[48:49], v[82:83] op_sel_hi:[1,0]\n"
    "v_pk_mul_f32 v[50:51], v[50:51], v[82:83] op_sel_hi:[1,0]\n"
    "v_pk_mul_f32 v[52:53], v[52:53], v[82:83] op_sel_hi:[1,0]\n"
    "v_pk_mul_f32 v[54:55], v[54:55], v[82:83] op_sel_hi:[1,0]\n"
    "v_pk_mul_f32 v[56:57], v[56:57], v[82:83] op_sel_hi:[1,0]\n"
    "v_pk_mul_f32 v[58:59], v[58:59], v[82:83] op_sel_hi:[1,0]\n"
    "v_pk_mul_f32 v[60:61], v[60:61], v[82:83] op_sel_hi:[1,0]\n"
    "v_pk_mul_f32 v[62:63], v[62:63], v[82:83] op_sel_hi:[1,0]\n"
    "v_cvt_pk_bf16_f32 v48, v48, v49\n"
    "v_cvt_pk_bf16_f32 v49, v50, v51\n"
    "v_cvt_pk_bf16_f32 v50, v52, v53\n"
    "v_cvt_pk_bf16_f32 v51, v54, v55\n"
    "v_cvt_pk_bf16_f32 v56, v56, v57\n"
    "v_cvt_pk_bf16_f32 v57, v58, v59\n"
    "v_cvt_pk_bf16_f32 v58, v60, v61\n"
    "v_cvt_pk_bf16_f32 v59, v62, v63\n"
    "s_waitcnt lgkmcnt(4)\n"
    "buffer_store_dwordx4 v[72:75], v19, s[48:51], s76 offen offset:128 nt\n"
    "s_nop 0\n"
    "buffer_store_dwordx4 v[76:79], v19, s[48:51], s77 offen offset:128 nt\n"
    "ds_write_b128 v16, v[48:51] offset:2048\n"
    "ds_write_b128 v17, v[56:59] offset:2048\n"
    "v_pk_mul_f32 v[104:105], v[104:105], v[80:81] op_sel_hi:[1,0]\n"
    "v_pk_mul_f32 v[106:107], v[106:107], v[80:81] op_sel_hi:[1,0]\n"
    "v_pk_mul_f32 v[108:109], v[108:109], v[80:81] op_sel_hi:[1,0]\n"
    "v_pk_mul_f32 v[110:111], v[110:111], v[80:81] op_sel_hi:[1,0]\n"
    "ds_read_b128 v[72:75], v18 offset:2048\n"
    "ds_read_b128 v[76:79], v18 offset:3072\n"
    "v_cvt_pk_bf16_f32 v104, v104, v105\n"
    "v_cvt_pk_bf16_f32 v105, v106, v107\n"
    "v_cvt_pk_bf16_f32 v106, v108, v109\n"
    "v_cvt_pk_bf16_f32 v107, v110, v111\n"
    "s_waitcnt lgkmcnt(4)\n"
    "buffer_store_dwordx4 v[64:67], v19, s[48:51], s76 offen offset:256 nt\n"
    "buffer_store_dwordx4 v[68:71], v19, s[48:51], s77 offen offset:256 nt\n"
    "s_waitcnt vmcnt(6)\n"
    "ds_write_b128 v94, v[96:99] offset:0\n"
    "ds_write_b128 v95, v[104:107] offset:0\n"
    "v_pk_mul_f32 v[112:113], v[112:113], v[80:81] op_sel_hi:[1,0]\n"
    "v_pk_mul_f32 v[114:115], v[114:115], v[80:81] op_sel_hi:[1,0]\n"
    "v_pk_mul_f32 v[116:117], v[116:117], v[80:81] op_sel_hi:[1,0]\n"
    "v_pk_mul_f32 v[118:119], v[118:119], v[80:81] op_sel_hi:[1,0]\n"
    "v_pk_mul_f32 v[120:121], v[120:121], v[80:81] op_sel_hi:[1,0]\n"
    "v_pk_mul_f32 v[122:123], v[122:123], v[80:81] op_sel_hi:[1,0]\n"
    "v_pk_mul_f32 v[124:125], v[124:125], v[80:81] op_sel_hi:[1,0]\n"
    "v_pk_mul_f32 v[126:127], v[126:127], v[80:81] op_sel_hi:[1,0]\n"
    "ds_read_b128 v[64:67], v93 offset:0\n"
    "ds_read_b128 v[68:71], v93 offset:4096\n"
    "v_cvt_pk_bf16_f32 v112, v112, v113\n"
    "v_cvt_pk_bf16_f32 v113, v114, v115\n"
    "v_cvt_pk_bf16_f32 v114, v116, v117\n"
    "v_cvt_pk_bf16_f32 v115, v118, v119\n"
    "v_cvt_pk_bf16_f32 v120, v120, v121\n"
    "v_cvt_pk_bf16_f32 v121, v122, v123\n"
    "v_cvt_pk_bf16_f32 v122, v124, v125\n"
    "v_cvt_pk_bf16_f32 v123, v126, v127\n"
    "s_waitcnt lgkmcnt(4)\n"
    "buffer_store_dwordx4 v[72:75], v19, s[48:51], s76 offen offset:384 nt\n"
    "buffer_store_dwordx4 v[76:79], v19, s[48:51], s77 offen offset:384 nt\n"
    "ds_write_b128 v94, v[112:115] offset:1024\n"
    "s_nop 0\n"
    "ds_write_b128 v95, v[120:123] offset:1024\n"
    "v_pk_mul_f32 v[128:129], v[128:129], v[80:81] op_sel_hi:[1,0]\n"
    "v_pk_mul_f32 v[130:131], v[130:131], v[80:81] op_sel_hi:[1,0]\n"
    "v_pk_mul_f32 v[132:133], v[132:133], v[80:81] op_sel_hi:[1,0]\n"
    "v_pk_mul_f32 v[134:135], v[134:135], v[80:81] op_sel_hi:[1,0]\n"
    "v_pk_mul_f32 v[136:137], v[136:137], v[80:81] op_sel_hi:[1,0]\n"
    "v_pk_mul_f32 v[138:139], v[138:139], v[80:81] op_sel_hi:[1,0]\n"
    "v_pk_mul_f32 v[140:141], v[140:141], v[80:81] op_sel_hi:[1,0]\n"
    "v_pk_mul_f32 v[142:143], v[142:143], v[80:81] op_sel_hi:[1,0]\n"
    "ds_read_b128 v[72:75], v93 offset:1024\n"
    "ds_read_b128 v[76:79], v93 offset:5120\n"
    "v_cvt_pk_bf16_f32 v128, v128, v129\n"
    "v_cvt_pk_bf16_f32 v129, v130, v131\n"
    "v_cvt_pk_bf16_f32 v130, v132, v133\n"
    "v_cvt_pk_bf16_f32 v131, v134, v135\n"
    "v_cvt_pk_bf16_f32 v136, v136, v137\n"
    "v_cvt_pk_bf16_f32 v137, v138, v139\n"
    "v_cvt_pk_bf16_f32 v138, v140, v141\n"
    "v_cvt_pk_bf16_f32 v139, v142, v143\n"
    "s_waitcnt lgkmcnt(4)\n"
    "s_add_u32 s76, s79, 0\n"
    "s_add_u32 s77, s79, 4096\n"
    "buffer_store_dwordx4 v[64:67], v92, s[48:51], s76 offen offset:0 nt\n"
    "buffer_store_dwordx4 v[68:71], v92, s[48:51], s77 offen offset:0 nt\n"
    "ds_write_b128 v94, v[128:131] offset:2048\n"
    "ds_write_b128 v95, v[136:139] offset:2048\n"
    "v_pk_mul_f32 v[144:145], v[144:145], v[80:81] op_sel_hi:[1,0]\n"
    "v_pk_mul_f32 v[146:147], v[146:147], v[80:81] op_sel_hi:[1,0]\n"
    "v_pk_mul_f32 v[148:149], v[148:149], v[80:81] op_sel_hi:[1,0]\n"
    "v_pk_mul_f32 v[150:151], v[150:151], v[80:81] op_sel_hi:[1,0]\n"
    "v_pk_mul_f32 v[152:153], v[152:153], v[80:81] op_sel_hi:[1,0]\n"
    "v_pk_mul_f32 v[154:155], v[154:155], v[80:81] op_sel_hi:[1,0]\n"
    "v_pk_mul_f32 v[156:157], v[156:157], v[80:81] op_sel_hi:[1,0]\n"
    "v_pk_mul_f32 v[158:159], v[158:159], v[80:81] op_sel_hi:[1,0]\n"
    "ds_read_b128 v[64:67], v93 offset:2048\n"
    "ds_read_b128 v[68:71], v93 offset:6144\n"
    "v_cvt_pk_bf16_f32 v144, v144, v145\n"
    "v_cvt_pk_bf16_f32 v145, v146, v147\n"
    "v_cvt_pk_bf16_f32 v146, v148, v149\n"
    "v_cvt_pk_bf16_f32 v147, v150, v151\n"
    "v_cvt_pk_bf16_f32 v152, v152, v153\n"
    "v_cvt_pk_bf16_f32 v153, v154, v155\n"
    "v_cvt_pk_bf16_f32 v154, v156, v157\n"
    "v_cvt_pk_bf16_f32 v155, v158, v159\n"
    "s_waitcnt lgkmcnt(4)\n"
    "s_nop 0\n"
    "buffer_store_dwordx4 v[72:75], v92, s[48:51], s76 offen offset:128 nt\n"
    "buffer_store_dwordx4 v[76:79], v92, s[48:51], s77 offen offset:128 nt\n"
    "ds_write_b128 v94, v[144:147] offset:3072\n"
    "ds_write_b128 v95, v[152:155] offset:3072\n"
    "v_pk_mul_f32 v[160:161], v[160:161], v[80:81] op_sel:[0,1] op_sel_hi:[1,1]\n"
    "v_pk_mul_f32 v[162:163], v[162:163], v[80:81] op_sel:[0,1] op_sel_hi:[1,1]\n"
    "v_pk_mul_f32 v[164:165], v[164:165], v[80:81] op_sel:[0,1] op_sel_hi:[1,1]\n"
    "v_pk_mul_f32 v[166:167], v[166:167], v[80:81] op_sel:[0,1] op_sel_hi:[1,1]\n"
    "v_pk_mul_f32 v[168:169], v[168:169], v[80:81] op_sel:[0,1] op_sel_hi:[1,1]\n"
    "v_pk_mul_f32 v[170:171], v[170:171], v[80:81] op_sel:[0,1] op_sel_hi:[1,1]\n"
    "v_pk_mul_f32 v[172:173], v[172:173], v[80:81] op_sel:[0,1] op_sel_hi:[1,1]\n"
    "v_pk_mul_f32 v[174:175], v[174:175], v[80:81] op_sel:[0,1] op_sel_hi:[1,1]\n"
    "ds_read_b128 v[72:75], v93 offset:3072\n"
    "ds_read_b128 v[76:79], v93 offset:7168\n"
    "v_cvt_pk_bf16_f32 v160, v160, v161\n"
    "v_cvt_pk_bf16_f32 v161, v162, v163\n"
    "v_cvt_pk_bf16_f32 v162, v164, v165\n"
    "v_cvt_pk_bf16_f32 v163, v166, v167\n"
    "v_cvt_pk_bf16_f32 v168, v168, v169\n"
    "v_cvt_pk_bf16_f32 v169, v170, v171\n"
    "v_cvt_pk_bf16_f32 v170, v172, v173\n"
    "v_cvt_pk_bf16_f32 v171, v174, v175\n"
    "s_waitcnt lgkmcnt(4)\n"
    "buffer_store_dwordx4 v[64:67], v92, s[48:51], s76 offen offset:256 nt\n"
    "s_nop 0\n"
    "buffer_store_dwordx4 v[68:71], v92, s[48:51], s77 offen offset:256 nt\n"
    "ds_write_b128 v94, v[160:163] offset:8192\n"
    "ds_write_b128 v95, v[168:171] offset:8192\n"
    "v_pk_mul_f32 v[176:177], v[176:177], v[80:81] op_sel:[0,1] op_sel_hi:[1,1]\n"
    "v_pk_mul_f32 v[178:179], v[178:179], v[80:81] op_sel:[0,1] op_sel_hi:[1,1]\n"
    "v_pk_mul_f32 v[180:181], v[180:181], v[80:81] op_sel:[0,1] op_sel_hi:[1,1]\n"
    "v_pk_mul_f32 v[182:183], v[182:183], v[80:81] op_sel:[0,1] op_sel_hi:[1,1]\n"
    "v_pk_mul_f32 v[184:185], v[184:185], v[80:81] op_sel:[0,1] op_sel_hi:[1,1]\n"
    "v_pk_mul_f32 v[186:187], v[186:187], v[80:81] op_sel:[0,1] op_sel_hi:[1,1]\n"
    "v_pk_mul_f32 v[188:189], v[188:189], v[80:81] op_sel:[0,1] op_sel_hi:[1,1]\n"
    "v_pk_mul_f32 v[190:191], v[190:191], v[80:81] op_sel:[0,1] op_sel_hi:[1,1]\n"
    "ds_read_b128 v[64:67], v93 offset:8192\n"
    "ds_read_b128 v[68:71], v93 offset:12288\n"
    "v_cvt_pk_bf16_f32 v176, v176, v177\n"
    "v_cvt_pk_bf16_f32 v177, v178, v179\n"
    "v_cvt_pk_bf16_f32 v178, v180, v181\n"
    "v_cvt_pk_bf16_f32 v179, v182, v183\n"
    "v_cvt_pk_bf16_f32 v184, v184, v185\n"
    "v_cvt_pk_bf16_f32 v185, v186, v187\n"
    "v_cvt_pk_bf16_f32 v186, v188, v189\n"
    "v_cvt_pk_bf16_f32 v187, v190, v191\n"
    "s_waitcnt lgkmcnt(4)\n"
    "buffer_store_dwordx4 v[72:75], v92, s[48:51], s76 offen offset:384 nt\n"
    "buffer_store_dwordx4 v[76:79], v92, s[48:51], s77 offen offset:384 nt\n"
    "s_nop 0\n"
    "ds_write_b128 v94, v[176:179] offset:9216\n"
    "ds_write_b128 v95, v[184:187] offset:9216\n"
    "v_pk_mul_f32 v[192:193], v[192:193], v[80:81] op_sel:[0,1] op_sel_hi:[1,1]\n"
    "v_pk_mul_f32 v[194:195], v[194:195], v[80:81] op_sel:[0,1] op_sel_hi:[1,1]\n"
    "v_pk_mul_f32 v[196:197], v[196:197], v[80:81] op_sel:[0,1] op_sel_hi:[1,1]\n"
    "v_pk_mul_f32 v[198:199], v[198:199], v[80:81] op_sel:[0,1] op_sel_hi:[1,1]\n"
    "v_pk_mul_f32 v[200:201], v[200:201], v[80:81] op_sel:[0,1] op_sel_hi:[1,1]\n"
    "v_pk_mul_f32 v[202:203], v[202:203], v[80:81] op_sel:[0,1] op_sel_hi:[1,1]\n"
    "v_pk_mul_f32 v[204:205], v[204:205], v[80:81] op_sel:[0,1] op_sel_hi:[1,1]\n"
    "v_pk_mul_f32 v[206:207], v[206:207], v[80:81] op_sel:[0,1] op_sel_hi:[1,1]\n"
    "ds_read_b128 v[72:75], v93 offset:9216\n"
    "ds_read_b128 v[76:79], v93 offset:13312\n"
    "v_cvt_pk_bf16_f32 v192, v192, v193\n"
    "v_cvt_pk_bf16_f32 v193, v194, v195\n"
    "v_cvt_pk_bf16_f32 v194, v196, v197\n"
    "v_cvt_pk_bf16_f32 v195, v198, v199\n"
    "v_cvt_pk_bf16_f32 v200, v200, v201\n"
    "v_cvt_pk_bf16_f32 v201, v202, v203\n"
    "v_cvt_pk_bf16_f32 v202, v204, v205\n"
    "v_cvt_pk_bf16_f32 v203, v206, v207\n"
    "s_waitcnt lgkmcnt(4)\n"
    "s_add_u32 s76, s79, 8192\n"
    "s_add_u32 s77, s79, 12288\n"
    "buffer_store_dwordx4 v[64:67], v92, s[48:51], s76 offen offset:0 nt\n"
    "s_nop 0\n"
    "buffer_store_dwordx4 v[68:71], v92, s[48:51], s77 offen offset:0 nt\n"
    "ds_write_b128 v94, v[192:195] offset:10240\n"
    "ds_write_b128 v95, v[200:203] offset:10240\n"
    "v_pk_mul_f32 v[208:209], v[208:209], v[80:81] op_sel:[0,1] op_sel_hi:[1,1]\n"
    "v_pk_mul_f32 v[210:211], v[210:211], v[80:81] op_sel:[0,1] op_sel_hi:[1,1]\n"
    "v_pk_mul_f32 v[212:213], v[212:213], v[80:81] op_sel:[0,1] op_sel_hi:[1,1]\n"
    "v_pk_mul_f32 v[214:215], v[214:215], v[80:81] op_sel:[0,1] op_sel_hi:[1,1]\n"
    "v_pk_mul_f32 v[216:217], v[216:217], v[80:81] op_sel:[0,1] op_sel_hi:[1,1]\n"
    "v_pk_mul_f32 v[218:219], v[218:219], v[80:81] op_sel:[0,1] op_sel_hi:[1,1]\n"
    "v_pk_mul_f32 v[220:221], v[220:221], v[80:81] op_sel:[0,1] op_sel_hi:[1,1]\n"
    "v_pk_mul_f32 v[222:223], v[222:223], v[80:81] op_sel:[0,1] op_sel_hi:[1,1]\n"
    "ds_read_b128 v[64:67], v93 offset:10240\n"
    "ds_read_b128 v[68:71], v93 offset:14336\n"
    "v_cvt_pk_bf16_f32 v208, v208, v209\n"
    "v_cvt_pk_bf16_f32 v209, v210, v211\n"
    "v_cvt_pk_bf16_f32 v210, v212, v213\n"
    "v_cvt_pk_bf16_f32 v211, v214, v215\n"
    "v_cvt_pk_bf16_f32 v216, v216, v217\n"
    "v_cvt_pk_bf16_f32 v217, v218, v219\n"
    "v_cvt_pk_bf16_f32 v218, v220, v221\n"
    "v_cvt_pk_bf16_f32 v219, v222, v223\n"
    "s_waitcnt lgkmcnt(4)\n"
    "buffer_store_dwordx4 v[72:75], v92, s[48:51], s76 offen offset:128 nt\n"
    "buffer_store_dwordx4 v[76:79], v92, s[48:51], s77 offen offset:128 nt\n"
    "s_nop 0\n"
    "ds_write_b128 v94, v[208:211] offset:11264\n"
    "ds_write_b128 v95, v[216:219] offset:11264\n"
    "ds_read_b128 v[72:75], v93 offset:11264\n"
    "ds_read_b128 v[76:79], v93 offset:15360\n"
    "s_waitcnt lgkmcnt(4)\n"
    "buffer_store_dwordx4 v[64:67], v92, s[48:51], s76 offen offset:256 nt\n"
    "buffer_store_dwordx4 v[68:71], v92, s[48:51], s77 offen offset:256 nt\n"
    "s_waitcnt lgkmcnt(0)\n"
    "buffer_store_dwordx4 v[72:75], v92, s[48:51], s76 offen offset:384 nt\n"
    "buffer_store_dwordx4 v[76:79], v92, s[48:51], s77 offen offset:384 nt\n"
    "LEND_%=:\n"
    "s_waitcnt vmcnt(0) lgkmcnt(0)\n"
    "s_mov_b32 m0, s82\n"
    "s_mov_b32 %0, s67\n"
    : "=s"(flags)
    : "s"(Q), "s"(K), "s"(V), "s"(O), "s"(bid), "s"(wid), "s"((unsigned)(size_t)lds)
    : "v0", "v1", "v2", "v3", "v4", "v5", "v6", "v7", "v8", "v9", "v10", "v11", "v12", "v13", "v14", "v15", "v16", "v17", "v18", "v19", "v20", "v21", "v22", "v23", "v24", "v25", "v26", "v27", "v28", "v29", "v30", "v31", "v32", "v33", "v34", "v35", "v36", "v37", "v38", "v39", "v40", "v41", "v42", "v43", "v44", "v45", "v46", "v47", "v48", "v49", "v50", "v51", "v52", "v53", "v54", "v55", "v56", "v57", "v58", "v59", "v60", "v61", "v62", "v63", "v64", "v65", "v66", "v67", "v68", "v69", "v70", "v71", "v72", "v73", "v74", "v75", "v76", "v77", "v78", "v79", "v80", "v81", "v82", "v83", "v84", "v85", "v86", "v87", "v88", "v89", "v90", "v91", "v92", "v93", "v94", "v95", "v96", "v97", "v98", "v99", "v100", "v101", "v102", "v103", "v104", "v105", "v106", "v107", "v108", "v109", "v110", "v111", "v112", "v113", "v114", "v115", "v116", "v117", "v118", "v119", "v120", "v121", "v122", "v123", "v124", "v125", "v126", "v127", "v128", "v129", "v130", "v131", "v132", "v133", "v134", "v135", "v136", "v137", "v138", "v139", "v140", "v141", "v142", "v143", "v144", "v145", "v146", "v147", "v148", "v149", "v150", "v151", "v152", "v153", "v154", "v155", "v156", "v157", "v158", "v159", "v160", "v161", "v162", "v163", "v164", "v165", "v166", "v167", "v168", "v169", "v170", "v171", "v172", "v173", "v174", "v175", "v176", "v177", "v178", "v179", "v180", "v181", "v182", "v183", "v184", "v185", "v186", "v187", "v188", "v189", "v190", "v191", "v192", "v193", "v194", "v195", "v196", "v197", "v198", "v199", "v200", "v201", "v202", "v203", "v204", "v205", "v206", "v207", "v208", "v209", "v210", "v211", "v212", "v213", "v214", "v215", "v216", "v217", "v218", "v219", "v220", "v221", "v222", "v223", "v224", "v225", "v226", "v227", "v228", "v229", "v230", "v231", "v232", "v233", "v234", "v235", "v236", "v237", "v238", "v239", "v240", "v241", "v242", "v243", "v244", "v245", "v246", "v247", "v248", "v249", "v250", "v251", "v252", "v253", "v254", "v255", "a0", "a1", "a2", "a3", "a4", "a5", "a6", "a7", "a8", "a9", "a10", "a11", "a12", "a13", "a14", "a15", "a16", "a17", "a18", "a19", "a20", "a21", "a22", "a23", "a24", "a25", "a26", "a27", "a28", "a29", "a30", "a31", "a32", "a33", "a34", "a35", "a36", "a37", "a38", "a39", "a40", "a41", "a42", "a43", "a44", "a45", "a46", "a47", "a48", "a49", "a50", "a51", "a52", "a53", "a54", "a55", "a56", "a57", "a58", "a59", "a60", "a61", "a62", "a63", "a64", "a65", "a66", "a67", "a68", "a69", "a70", "a71", "a72", "a73", "a74", "a75", "a76", "a77", "a78", "a79", "a80", "a81", "a82", "a83", "a84", "a85", "a86", "a87", "a88", "a89", "a90", "a91", "a92", "a93", "a94", "a95", "a96", "a97", "a98", "a99", "a100", "a101", "a102", "a103", "a104", "a105", "a106", "a107", "a108", "a109", "a110", "a111", "a112", "a113", "a114", "a115", "a116", "a117", "a118", "a119", "a120", "a121", "a122", "a123", "a124", "a125", "a126", "a127", "a128", "a129", "a130", "a131", "a132", "a133", "a134", "a135", "a136", "a137", "a138", "a139", "a140", "a141", "a142", "a143", "a144", "a145", "a146", "a147", "a148", "a149", "a150", "a151", "a152", "a153", "a154", "a155", "a156", "a157", "a158", "a159", "a160", "a161", "a162", "a163", "a164", "a165", "a166", "a167", "a168", "a169", "a170", "a171", "a172", "a173", "a174", "a175", "a176", "a177", "a178", "a179", "a180", "a181", "a182", "a183", "a184", "a185", "a186", "a187", "a188", "a189", "a190", "a191", "a192", "a193", "a194", "a195", "a196", "a197", "a198", "a199", "a200", "a201", "a202", "a203", "a204", "a205", "a206", "a207", "a208", "a209", "a210", "a211", "a212", "a213", "a214", "a215", "a216", "a217", "a218", "a219", "a220", "a221", "a222", "a223", "a224", "a225", "a226", "a227", "a228", "a229", "a230", "a231", "a232", "a233", "a234", "a235", "a236", "a237", "a238", "a239", "a240", "a241", "a242", "a243", "a244", "a245", "a246", "a247", "a248", "a249", "a250", "a251", "a252", "a253", "a254", "a255", "s36", "s37", "s38", "s39", "s40", "s41", "s42", "s43", "s44", "s45", "s46", "s47", "s48", "s49", "s50", "s51", "s52", "s53", "s54", "s55", "s56", "s57", "s58", "s59", "s60", "s61", "s62", "s63", "s64", "s65", "s66", "s67", "s68", "s69", "s70", "s71", "s72", "s73", "s74", "s75", "s76", "s77", "s78", "s79", "s80", "s81", "s82", "s83", "s84", "s85", "s86", "s87", "s88", "s89", "s90", "s91", "s92", "s93", "s94", "s95", "s96", "s97", "s98", "s99", "vcc", "scc", "memory");
  if (flags) {
    int lane;
    asm volatile("v_mbcnt_lo_u32_b32 %0, -1, 0\n\tv_mbcnt_hi_u32_b32 %0, -1, %0" : "=v"(lane));
    const int h0 = (bid & 7) * 32 + (bid >> 5) * 4, mu = (bid >> 3) & 3;
    for (int r = 0; r < 4; ++r) {
      if (!((flags >> r) & 1)) continue;
      const int nq = (((mu >> 1) + r) & 1) ? 3 : 4;
      const int row0 = (nq == 4 ? 256 * (mu & 1) : 512 + 192 * (mu & 1)) + 16 * nq * (int)wid;
      for (int t = 0; t < nq; ++t)
        if (row0 + 16 * t < SQ) slow_rows16(Q, K, V, O, h0 + r, row0 + 16 * t, lane);
    }
  }
}

extern "C" void kernel_launch(void* const* d_in, const int* in_sizes, int n_in, void* d_out, int out_size, void* d_ws, size_t ws_size,
                              hipStream_t stream) {
  attn_fwd<<<256, 256, 0, stream>>>((const unsigned short*)d_in[0], (const unsigned short*)d_in[1], (const unsigned short*)d_in[2],
                                    (unsigned short*)d_out);
}
```
